```python
import torch

def _softmax(x, axis=-1):
    return torch.softmax(x, dim=axis)
def _gelu(x):
    return torch.nn.functional.gelu(x, approximate="tanh")
def _silu(x):
    return torch.nn.functional.silu(x)
def _sigmoid(x):
    return torch.sigmoid(x)
def _layernorm(x, axis=-1, eps=1e-5):
    m = x.mean(dim=axis, keepdim=True)
    v = x.var(dim=axis, keepdim=True, unbiased=False)
    return (x - m) / torch.sqrt(v + eps)
def _scatter_add(tgt, idx, src, axis=0):
    return tgt.clone().index_add_(axis, idx, src)
def _segment_sum(src, seg, n):
    out = torch.zeros((n,) + src.shape[1:], dtype=src.dtype)
    return out.index_add_(0, seg, src)
def _pad(x, pw):
    flat = tuple(v for pair in reversed(pw) for v in pair)
    return torch.nn.functional.pad(x, flat)

kernel_name = 'parallel_sum__gqa_block_cached__glu_gated_bilinear__decode'

def setup_inputs():

    _w = (lambda a: (lambda t: t.to(torch.bfloat16) if t.is_floating_point() else t)(torch.as_tensor(a)))
    def _A_setup():
        B, Sq, D = 8, 1, 512
        Hq, Hkv, Sk, Dh = 32, 8, 8192, 128
        assert Hq % Hkv == 0, (Hq, Hkv)
        return {
            "x": _w((lambda *s: torch.randn(*s, dtype=torch.bfloat16) * 0.05)(B, Sq, D)),
            "k_cache": _w((lambda *s: torch.randn(*s, dtype=torch.bfloat16) * 1.6)(B, Hkv, Sk, Dh)),
            "v_cache": _w((lambda *s: torch.randn(*s, dtype=torch.bfloat16) * 0.8)(B, Hkv, Sk, Dh)),
            "wq": _w((lambda *s: torch.randn(*s, dtype=torch.bfloat16) * 1.6)(D, Hq * Dh) * (D ** -0.5)),
            "wo": _w((lambda *s: torch.randn(*s, dtype=torch.bfloat16) * 0.8)(Hq * Dh, D) * ((Hq * Dh) ** -0.5)),
        }
    _A_in = _A_setup()

    def _B_setup():
        B, S, D, F = 8, 1, 512, 1792
        return {
            "x": _w((lambda *s: torch.randn(*s, dtype=torch.bfloat16) * 0.05)(B, S, D)),
            "w_gate": _w((lambda *s: torch.randn(*s, dtype=torch.bfloat16) * 0.4)(D, F) * (D ** -0.5)),
            "w_up": _w((lambda *s: torch.randn(*s, dtype=torch.bfloat16) * 0.4)(D, F) * (D ** -0.5)),
            "w_down": _w((lambda *s: torch.randn(*s, dtype=torch.bfloat16) * 0.4)(F, D) * (F ** -0.5)),
        }
    _B_in = _B_setup()

    return {
        "A_x": _A_in["x"],
        "A_k_cache": _A_in["k_cache"],
        "A_v_cache": _A_in["v_cache"],
        "A_wq": _A_in["wq"],
        "A_wo": _A_in["wo"],
        "B_x": _B_in["x"],
        "B_w_gate": _B_in["w_gate"],
        "B_w_up": _B_in["w_up"],
        "B_w_down": _B_in["w_down"],
    }

def reference(A_x, A_k_cache, A_v_cache, A_wq, A_wo, B_x, B_w_gate, B_w_up, B_w_down):
    def _A_ref(x, k_cache, v_cache, wq, wo):
        B, Sq, D = x.shape
        Hq, Dh = 32, 128
        Hkv = k_cache.shape[1]
        rep = Hq // Hkv
        q = (lambda x, *d: x.permute(*d))((x @ wq).reshape(B, Sq, Hq, Dh), 0, 2, 1, 3)
        k = (lambda x, n, axis: torch.repeat_interleave(x, n, dim=axis))(k_cache, rep, axis=1)
        v = (lambda x, n, axis: torch.repeat_interleave(x, n, dim=axis))(v_cache, rep, axis=1)
        scores = torch.einsum("bhqd,bhkd->bhqk", q, k) * (Dh ** -0.5)
        p = _softmax(scores, axis=-1)
        o = torch.einsum("bhqk,bhkd->bhqd", p, v)
        return (lambda x, *d: x.permute(*d))(o, 0, 2, 1, 3).reshape(B, Sq, Hq * Dh) @ wo

    def _B_ref(x, w_gate, w_up, w_down):
        g = torch.einsum("bsd,df->bsf", x, w_gate)
        u = torch.einsum("bsd,df->bsf", x, w_up)
        h = g * u
        return torch.einsum("bsf,fd->bsd", h, w_down)

    x = A_x
    xn = _layernorm(x, axis=-1)
    return x + _A_ref(xn, A_k_cache, A_v_cache, A_wq, A_wo) + _B_ref(xn, B_w_gate, B_w_up, B_w_down)
```

```cpp
#include <hip/hip_runtime.h>
#include <hip/hip_bf16.h>
#include <stdint.h>

typedef unsigned short u16;
typedef unsigned int u32;
using u32x4 = __attribute__((ext_vector_type(4))) unsigned;
using f32x4 = __attribute__((ext_vector_type(4))) float;

constexpr int B = 8, D = 512, HQ = 32, HKV = 8, SK = 8192, DH = 128, F = 1792;
constexpr int NSPLIT = 8;

__device__ __forceinline__ float bf2f(u16 v) { return __uint_as_float(((u32)v) << 16); }
__device__ __forceinline__ u16 f2bf(float f) { u32 u = __float_as_uint(f); u += 0x7fffu + ((u >> 16) & 1u); return (u16)(u >> 16); }
__device__ __forceinline__ float rbf(float f) { return bf2f(f2bf(f)); }

__device__ void ln_rows(const u16* x, float* xn  , float* red) {
  int tid = threadIdx.x; int wave = tid >> 6, lane = tid & 63;
  for (int b = wave; b < B; b += 4) {
    float v[8]; float s = 0.f;
    for (int i = 0; i < 8; ++i) { v[i] = bf2f(x[b * D + lane * 8 + i]); s += v[i]; }
    for (int m = 32; m >= 1; m >>= 1) s += __shfl_xor(s, m);
    float mean = s * (1.f / D);
    float q = 0.f;
    for (int i = 0; i < 8; ++i) { float d = v[i] - mean; q += d * d; }
    for (int m = 32; m >= 1; m >>= 1) q += __shfl_xor(q, m);
    float var = rbf(q * (1.f / D));
    float mb = rbf(mean);
    float den = rbf(sqrtf(rbf(var + 1e-5f)));
    for (int i = 0; i < 8; ++i) xn[b * D + lane * 8 + i] = rbf(rbf(v[i] - mb) / den);
  }
  __syncthreads();
}

__global__ void __launch_bounds__(256) k1(const u16* x, const u16* wq, const u16* wg, const u16* wu, float* q, float* h) {
  __shared__ float xn[B * D];
  __shared__ float red[4 * 8 * 64 * 2];
  ln_rows(x, xn, red);
  int tid = threadIdx.x; int wave = tid >> 6, lane = tid & 63;
  int blk = blockIdx.x;
  if (blk < 64) {
    int col = blk * 64 + lane;
    float acc[8] = {0};
    for (int k = wave * 128; k < wave * 128 + 128; ++k) {
      float w = bf2f(wq[(size_t)k * 4096 + col]);
      for (int b = 0; b < 8; ++b) acc[b] += xn[b * D + k] * w;
    }
    for (int b = 0; b < 8; ++b) red[(wave * 8 + b) * 64 + lane] = acc[b];
    __syncthreads();
    for (int i = tid; i < 8 * 64; i += 256) {
      int b = i >> 6, c = i & 63;
      float s = red[(0 * 8 + b) * 64 + c] + red[(1 * 8 + b) * 64 + c] + red[(2 * 8 + b) * 64 + c] + red[(3 * 8 + b) * 64 + c];
      q[b * 4096 + blk * 64 + c] = rbf(s);
    }
  } else {
    int fb = blk - 64;
    int col = fb * 64 + lane;
    float ag[8] = {0}, au[8] = {0};
    for (int k = wave * 128; k < wave * 128 + 128; ++k) {
      float g = bf2f(wg[(size_t)k * F + col]);
      float u = bf2f(wu[(size_t)k * F + col]);
      for (int b = 0; b < 8; ++b) { ag[b] += xn[b * D + k] * g; au[b] += xn[b * D + k] * u; }
    }
    for (int b = 0; b < 8; ++b) { red[(wave * 8 + b) * 64 + lane] = ag[b]; red[2048 + (wave * 8 + b) * 64 + lane] = au[b]; }
    __syncthreads();
    for (int i = tid; i < 8 * 64; i += 256) {
      int b = i >> 6, c = i & 63;
      float g = 0, u = 0;
      for (int w = 0; w < 4; ++w) { g += red[(w * 8 + b) * 64 + c]; u += red[2048 + (w * 8 + b) * 64 + c]; }
      h[b * F + fb * 64 + c] = rbf(rbf(g) * rbf(u));
    }
  }
}

__global__ void __launch_bounds__(256) k2(const u16* kc, const u16* vc, const float* q, float* pm, float* pl, float* po) {
  __shared__ float sq[4 * 128];
  __shared__ float sc[4][1024];
  __shared__ float red[4][4];
  __shared__ float ro[4][4][128];
  int s = blockIdx.x, kvh = blockIdx.y, b = blockIdx.z;
  int tid = threadIdx.x, wave = tid >> 6, lane = tid & 63;
  for (int i = tid; i < 512; i += 256) sq[i] = q[b * 4096 + kvh * 512 + i];
  __syncthreads();
  const int KPS = SK / NSPLIT;
  const u16* kb = kc + ((size_t)(b * HKV + kvh) * SK + s * KPS) * DH;
  const u16* vb = vc + ((size_t)(b * HKV + kvh) * SK + s * KPS) * DH;
  int r = lane >> 4, c = lane & 15;
  float qr[4][8];
  for (int h = 0; h < 4; ++h) for (int i = 0; i < 8; ++i) qr[h][i] = sq[h * 128 + c * 8 + i];
  for (int k0 = wave * 4; k0 < KPS; k0 += 16) {
    u32x4 kv = *(const u32x4*)(kb + (size_t)(k0 + r) * DH + c * 8);
    float kf[8];
    for (int i = 0; i < 4; ++i) { kf[2 * i] = __uint_as_float(kv[i] << 16); kf[2 * i + 1] = __uint_as_float(kv[i] & 0xffff0000u); }
    for (int h = 0; h < 4; ++h) {
      float d = 0; for (int i = 0; i < 8; ++i) d += kf[i] * qr[h][i];
      for (int m = 8; m >= 1; m >>= 1) d += __shfl_xor(d, m);
      if (c == 0) sc[h][k0 + r] = d * 0.08838834764831845f;
    }
  }
  __syncthreads();
  {
    int h = wave; float m = -INFINITY;
    for (int i = lane; i < KPS; i += 64) m = fmaxf(m, sc[h][i]);
    for (int mm = 32; mm >= 1; mm >>= 1) m = fmaxf(m, __shfl_xor(m, mm));
    float l = 0;
    for (int i = lane; i < KPS; i += 64) { float p = __expf(sc[h][i] - m); sc[h][i] = p; l += p; }
    for (int mm = 32; mm >= 1; mm >>= 1) l += __shfl_xor(l, mm);
    if (lane == 0) { int slot = ((b * HQ + kvh * 4 + h) * NSPLIT + s); pm[slot] = m; pl[slot] = l; }
  }
  __syncthreads();
  float acc[4][8];
  for (int h = 0; h < 4; ++h) for (int i = 0; i < 8; ++i) acc[h][i] = 0;
  for (int k0 = wave * 4; k0 < KPS; k0 += 16) {
    u32x4 vv = *(const u32x4*)(vb + (size_t)(k0 + r) * DH + c * 8);
    float vf[8];
    for (int i = 0; i < 4; ++i) { vf[2 * i] = __uint_as_float(vv[i] << 16); vf[2 * i + 1] = __uint_as_float(vv[i] & 0xffff0000u); }
    for (int h = 0; h < 4; ++h) { float p = sc[h][k0 + r]; for (int i = 0; i < 8; ++i) acc[h][i] += p * vf[i]; }
  }
  for (int h = 0; h < 4; ++h) for (int i = 0; i < 8; ++i) { float t = acc[h][i]; t += __shfl_xor(t, 16); t += __shfl_xor(t, 32); acc[h][i] = t; }
  if (r == 0) for (int h = 0; h < 4; ++h) for (int i = 0; i < 8; ++i) ro[wave][h][c * 8 + i] = acc[h][i];
  __syncthreads();
  for (int i = tid; i < 512; i += 256) {
    int h = i >> 7, d = i & 127;
    float t = ro[0][h][d] + ro[1][h][d] + ro[2][h][d] + ro[3][h][d];
    po[((size_t)(b * HQ + kvh * 4 + h) * NSPLIT + s) * 128 + d] = t;
  }
}

__global__ void __launch_bounds__(256) k3(const u16* x, const u16* wo, const u16* wd, const float* pm, const float* pl, const float* po, const float* h, u16* out) {
  __shared__ float so[8 * 4096 / 2];
  __shared__ float red[256 * 8];
  int tid = threadIdx.x;
  int col0 = blockIdx.x * 8;
  int cc = tid & 7, kk = tid >> 3;
  float acc[8] = {0};
  float accf[8] = {0};
  for (int half = 0; half < 2; ++half) {
    __syncthreads();
    for (int i = tid; i < 8 * 2048; i += 256) {
      int b = i >> 11, r = i & 2047; int k = half * 2048 + r; int hq = k >> 7, d = k & 127;
      int slot = (b * HQ + hq) * NSPLIT;
      float M = -INFINITY; for (int s = 0; s < NSPLIT; ++s) M = fmaxf(M, pm[slot + s]);
      float L = 0, o = 0;
      for (int s = 0; s < NSPLIT; ++s) { float w = __expf(pm[slot + s] - M); L += w * pl[slot + s]; o += w * po[(size_t)(slot + s) * 128 + d]; }
      so[i] = rbf(o / L);
    }
    __syncthreads();
    for (int r = kk; r < 2048; r += 32) {
      float w = bf2f(wo[(size_t)(half * 2048 + r) * D + col0 + cc]);
      for (int b = 0; b < 8; ++b) acc[b] += so[b * 2048 + r] * w;
    }
  }
  for (int r = kk; r < F; r += 32) {
    float w = bf2f(wd[(size_t)r * D + col0 + cc]);
    for (int b = 0; b < 8; ++b) accf[b] += h[b * F + r] * w;
  }
  for (int pass = 0; pass < 2; ++pass) {
    __syncthreads();
    for (int b = 0; b < 8; ++b) red[tid * 8 + b] = pass ? accf[b] : acc[b];
    __syncthreads();
    if (tid < 64) {
      int b = tid >> 3, c = tid & 7; float t = 0;
      for (int g = 0; g < 32; ++g) t += red[(g * 8 + c) * 8 + b];
      if (pass == 0) acc[0] = t; else {
        float xa = bf2f(x[b * D + col0 + c]);
        float a = rbf(acc[0]); float f = rbf(t);
        out[b * D + col0 + c] = f2bf(rbf(xa + a) + f);
      }
    }
  }
}

extern "C" void kernel_launch(void* const* d_in, const int* in_sizes, int n_in, void* d_out, int out_size, void* d_ws, size_t ws_size, hipStream_t stream) {
  const u16* x = (const u16*)d_in[0];
  const u16* kc = (const u16*)d_in[1];
  const u16* vc = (const u16*)d_in[2];
  const u16* wq = (const u16*)d_in[3];
  const u16* wo = (const u16*)d_in[4];
  const u16* wg = (const u16*)d_in[6];
  const u16* wu = (const u16*)d_in[7];
  const u16* wd = (const u16*)d_in[8];
  float* ws = (float*)d_ws;
  float* q = ws;
  float* h = q + 8 * 4096;
  float* pm = h + 8 * 2048;
  float* pl = pm + 8 * 32 * NSPLIT;
  float* po = pl + 8 * 32 * NSPLIT;
  k1<<<64 + 28, 256, 0, stream>>>(x, wq, wg, wu, q, h);
  k2<<<dim3(NSPLIT, HKV, B), 256, 0, stream>>>(kc, vc, q, pm, pl, po);
  k3<<<64, 256, 0, stream>>>(x, wo, wd, pm, pl, po, h, (u16*)d_out);
}
```

```cpp
#define ADAPT 0
#define DAHEAD 2
#define H1DLY 24
#define H2DLY 30
#define HOLD 6000
#define K0CFG 1
#define NCODE 20
#include <hip/hip_runtime.h>
#include <stdint.h>

#define GAS __attribute__((address_space(1)))
#define LAS __attribute__((address_space(3)))
typedef unsigned u32;
typedef unsigned long long u64;
typedef unsigned short u16;
typedef u32 u32x4 __attribute__((ext_vector_type(4)));
typedef u32 u32x2 __attribute__((ext_vector_type(2)));
typedef float f32x4 __attribute__((ext_vector_type(4)));
typedef float f32x2 __attribute__((ext_vector_type(2)));
typedef short s16x4 __attribute__((ext_vector_type(4)));
typedef __bf16 bf16x8 __attribute__((ext_vector_type(8)));
typedef __bf16 bf16x2 __attribute__((ext_vector_type(2)));

#define DIAG 0
#define PACE10 80
#define SENT 8
#define HPRIO 3
#define FAKEC 0
#define NOPOLL 0
#define STREAMONLY 0
#define TQ0 380
#define TQR 48
#define H1MODE 0
#define TG15 950
#define TG2 1350
#define TGS 800
#define DRY 0
#define H2MODE 0
#define ASTOP 57
#define ASLP 40
#define ABIAS 1000
#define ALEAD 15
#define FINE 0
#define KT 0
#define NROUND 2
#define TA0 1100
#define TAP 700
#define TAW 220
#define APDEF 56
#define ALAT 120
#define REPORT 0
#define OUTSC1 0
#define DPRO 1
#define LEADT 30
#define POLLSLP 4
#define SKIP_FFN 0
#define WNT 1

constexpr int NB = 8, DM = 512, SK = 8192, DH = 128, FF = 1792;
constexpr int NWG = 256, NTHR = 512;
#define RINGN 8
constexpr int RING = RINGN;
constexpr int NUNIT = 64;
constexpr float SC = 0.08838834764831845f * 1.4426950408889634f;
constexpr float THR_RAW = 8.0f / SC;

constexpr u32 WS_EPOCH = 0;
constexpr u32 WS_DUMMY = 2048;
constexpr u32 WS_START = 4096;
constexpr u32 WS_PROG = 8192;
constexpr u32 WS_Q = 65536;
constexpr u32 WS_GU = WS_Q + 8 * 8 * 4 * 512 * 8;
constexpr u32 WS_H = WS_GU + 28 * 2 * 4 * 8 * 64 * 8;
constexpr u32 WS_OP = WS_H + 8 * 1792 * 8;
constexpr u32 WS_ML = WS_OP + 32 * 8 * 4 * 64 * 8;
constexpr u32 WS_OUT = WS_ML + 32 * 8 * 4 * 2 * 8;
constexpr u32 WS_END = WS_OUT + 8 * 32 * 48 * 16 * 8;
constexpr u32 WS_DIAG = 8u << 20;
static_assert(WS_END <= WS_DIAG, "ws map");

constexpr u32 L_RING = 0;
constexpr u32 L_STAGE = 4 * RING * 4096;
constexpr u32 L_XN = L_STAGE + 16384;
constexpr u32 XN_STR = 1056;
constexpr u32 L_Q = L_XN + 8 * XN_STR;
constexpr u32 L_O = L_Q + 1024;
constexpr u32 L_FLAG = L_O + 2048;
constexpr u32 L_SCR = L_FLAG + 256;
constexpr u32 L_END = L_SCR + 4096;
static_assert(L_END <= 163840, "lds");
constexpr u32 F_SYNC = 0, F_Q = 4, F_SYNC2 = 8, F_WO = 12, F_EPOCH = 16, F_SYNC3 = 24, F_PROG = 28, F_PACE = 32;

constexpr u32 PER256 = (u32)(4096.0 * 1024.0 / ((PACE10 ? PACE10 : 1) * 0.1e12) * 1e8 * 256.0);
constexpr u32 SPIN_LDS = 1u << 22, SPIN_GLB = 1u << 17;

struct Params {
  const u16* x; const u16* k; const u16* v; const u16* wq; const u16* wo; const u16* wg; const u16* wu; const u16* wd;
  u16* out; unsigned char* ws;
};

__device__ __forceinline__ u32 rt32() { return (u32)__builtin_amdgcn_s_memrealtime(); }
__device__ __forceinline__ float bflo(u32 w) { return __uint_as_float(w << 16); }
__device__ __forceinline__ float bfhi(u32 w) { return __uint_as_float(w & 0xffff0000u); }
__device__ __forceinline__ u32 pk2(float lo, float hi) { f32x2 t = {lo, hi}; return __builtin_bit_cast(u32, __builtin_convertvector(t, bf16x2)); }
__device__ __forceinline__ float rbf(float f) { return bflo(pk2(f, f)); }
template <int CTRL> __device__ __forceinline__ float dpp(float x) {
  return __builtin_bit_cast(float, __builtin_amdgcn_mov_dpp(__builtin_bit_cast(int, x), CTRL, 0xf, 0xf, true));
}
__device__ __forceinline__ float xrow_max(float x) {
  auto s = __builtin_amdgcn_permlane16_swap(__float_as_uint(x), __float_as_uint(x), false, false);
  x = fmaxf(__uint_as_float(s[0]), __uint_as_float(s[1]));
  auto t = __builtin_amdgcn_permlane32_swap(__float_as_uint(x), __float_as_uint(x), false, false);
  return fmaxf(__uint_as_float(t[0]), __uint_as_float(t[1]));
}
__device__ __forceinline__ float xrow_sum(float x) {
  auto s = __builtin_amdgcn_permlane16_swap(__float_as_uint(x), __float_as_uint(x), false, false);
  x = __uint_as_float(s[0]) + __uint_as_float(s[1]);
  auto t = __builtin_amdgcn_permlane32_swap(__float_as_uint(x), __float_as_uint(x), false, false);
  return __uint_as_float(t[0]) + __uint_as_float(t[1]);
}
__device__ __forceinline__ float wave_sum(float x) {
  x += dpp<0xB1>(x); x += dpp<0x4E>(x); x += dpp<0x141>(x); x += dpp<0x128>(x);
  return xrow_sum(x);
}
__device__ __forceinline__ f32x4 mfma32(u32x4 a, u32x4 b, f32x4 c) {
  return __builtin_amdgcn_mfma_f32_16x16x32_bf16(__builtin_bit_cast(bf16x8, a), __builtin_bit_cast(bf16x8, b), c, 0, 0, 0);
}
__device__ __forceinline__ f32x4 mfma16(u32x2 a, u32x2 b, f32x4 c) {
  return __builtin_amdgcn_mfma_f32_16x16x16bf16_1k(__builtin_bit_cast(s16x4, a), __builtin_bit_cast(s16x4, b), c, 0, 0, 0);
}
__device__ __forceinline__ u32x2 trread(LAS unsigned char* p) {
  return __builtin_bit_cast(u32x2, __builtin_amdgcn_ds_read_tr16_b64_v4i16((LAS s16x4*)p));
}
__device__ __forceinline__ u32x4 trread2(LAS unsigned char* p0, LAS unsigned char* p1) {
  const u32x2 a = trread(p0), b = trread(p1); return u32x4{a.x, a.y, b.x, b.y};
}
#define DMA4(NT_)                                                                                                          \
  asm volatile("s_waitcnt lgkmcnt(0)\n\ts_mov_b32 %0, m0\n\ts_mov_b32 m0, %1\n\ts_nop 0\n\t"                              \
               "buffer_load_dwordx4 %2, %6, %7 offen " NT_ " lds\n\t"                                                      \
               "buffer_load_dwordx4 %3, %6, %7 offen offset:1024 " NT_ " lds\n\t"                                          \
               "buffer_load_dwordx4 %4, %6, %7 offen offset:2048 " NT_ " lds\n\t"                                          \
               "buffer_load_dwordx4 %5, %6, %7 offen offset:3072 " NT_ " lds\n\t"                                          \
               "s_mov_b32 m0, %0"                                                                                          \
               : "=&s"(keep) : "s"(ldst), "v"(v0), "v"(v1), "v"(v2), "v"(v3), "s"(srd), "s"(soff) : "memory")
#define KPOL "nt"
#define VPOL "nt"
__device__ __forceinline__ void dma4k(u32 ldst, u32 v0, u32 v1, u32 v2, u32 v3, __amdgpu_buffer_rsrc_t srd, u32 soff) {
  u32 keep; DMA4(KPOL);
}
__device__ __forceinline__ void dma4v(u32 ldst, u32 v0, u32 v1, u32 v2, u32 v3, __amdgpu_buffer_rsrc_t srd, u32 soff) {
  u32 keep; DMA4(VPOL);
}
__device__ __forceinline__ void dma1(u32 ldst, const void* gp) {
  u32 keep;
  asm volatile("s_mov_b32 %0, m0\n\ts_mov_b32 m0, %1\n\ts_nop 0\n\tglobal_load_lds_dwordx4 %2, off nt\n\ts_mov_b32 m0, %0"
               : "=&s"(keep) : "s"(ldst), "v"(gp) : "memory");
}
__device__ __forceinline__ void dma1p(u32 ldst, const void* gp) {
  u32 keep;
  asm volatile("s_mov_b32 %0, m0\n\ts_mov_b32 m0, %1\n\ts_nop 0\n\tglobal_load_lds_dwordx4 %2, off\n\ts_mov_b32 m0, %0"
               : "=&s"(keep) : "s"(ldst), "v"(gp) : "memory");
}
#define WNT_S " nt"
#define VMCNT(N) asm volatile("s_waitcnt vmcnt(" #N ")" ::: "memory")
#define LGKM0() asm volatile("s_waitcnt lgkmcnt(0)" ::: "memory")
__device__ __forceinline__ void wait_units(int n) {
  if (n > 11) { VMCNT(48); return; }
  switch (n) {
    case 0: VMCNT(0); break;   case 1: VMCNT(4); break;   case 2: VMCNT(8); break;   case 3: VMCNT(12); break;
    case 4: VMCNT(16); break;  case 5: VMCNT(20); break;  case 6: VMCNT(24); break;  case 7: VMCNT(28); break;
    case 8: VMCNT(32); break;  case 9: VMCNT(36); break;  case 10: VMCNT(40); break; default: VMCNT(44); break;
  }
}
__device__ __forceinline__ void bar() { LGKM0(); __builtin_amdgcn_s_barrier(); asm volatile("" ::: "memory"); }
__device__ __forceinline__ u32 lds_ld(LAS unsigned char* p) { return *(volatile LAS u32*)p; }
__device__ __forceinline__ void lds_wait_ge(LAS unsigned char* p, u32 want) {
_Pragma("nounroll")
  for (u32 n = 0; n < SPIN_LDS; ++n) { if (lds_ld(p) >= want) break; __builtin_amdgcn_s_sleep(1); }
  asm volatile("" ::: "memory");
}
__device__ __forceinline__ void st_gran(unsigned char* ws, u32 off, u32 lo, u32 hi) {
  __hip_atomic_store((GAS u64*)(ws + off), ((u64)hi << 32) | lo, __ATOMIC_RELAXED, __HIP_MEMORY_SCOPE_AGENT);
}
__device__ __forceinline__ u64 ld_gran(unsigned char* ws, u32 off) {
  return __hip_atomic_load((GAS u64*)(ws + off), __ATOMIC_RELAXED, __HIP_MEMORY_SCOPE_AGENT);
}
#define STAMP(i) do { } while (0)
#define STAMP_FLUSH() do { } while (0)

__global__ void __launch_bounds__(NTHR, 2) fused_decode_kernel(Params p) {
  __shared__ __attribute__((aligned(16))) unsigned char lds_raw[L_END];
  LAS unsigned char* L = (LAS unsigned char*)lds_raw;
  const u32 lbase = (u32)(size_t)L;
  const int tid = threadIdx.x, lane = tid & 63, wave = __builtin_amdgcn_readfirstlane(tid >> 6);
  const int g = lane >> 4, c = lane & 15;
  const int blk = blockIdx.x, xg = blk & 7, r = blk >> 3;
  const u32 t0 = rt32();
  u64 pc0; asm volatile("s_getpc_b64 %0" : "=s"(pc0));
  STAMP(0);
  const __amdgpu_buffer_rsrc_t wsr = __builtin_amdgcn_make_buffer_rsrc(p.ws, 0, WS_END, 0x00020000);
  const int ab = r >> 2, as = r & 3;
  const int whead = 4 * xg + (r & 3), wcc = r >> 2;

  if (wave < 4) {
    const size_t pbase = (size_t)(ab * 8 + xg) * SK * DH;
    const __amdgpu_buffer_rsrc_t ksr = __builtin_amdgcn_make_buffer_rsrc((void*)(p.k + pbase), 0, SK * DH * 2, 0x00020000);
    const __amdgpu_buffer_rsrc_t vsr = __builtin_amdgcn_make_buffer_rsrc((void*)(p.v + pbase), 0, SK * DH * 2, 0x00020000);
    const u32 vk0 = g * 256 + 16 * (c ^ g), vk1 = g * 256 + 16 * (c ^ (4 + g)), vk2 = g * 256 + 16 * (c ^ (8 + g)), vk3 = g * 256 + 16 * (c ^ (12 + g));
    const u32 vv0 = g * 256 + 16 * (c ^ (g << 1)), vv1 = g * 256 + 16 * (c ^ ((4 + g) << 1));
    const u32 ring = lbase + L_RING + wave * (RING * 4096);
    const u32 sbase = as * 16384 + wave * 4096;
    int nis = 0;
    int landed = 0;
    u32 pA = t0 - LEADT, pP = PER256, tnow = t0, tgt = 0;
    auto pace = [&]() {
      if (PACE10 && (int)(tnow - (pA + (((u32)nis * pP) >> 8))) < 0) {
        _Pragma("nounroll")
        for (u32 n = 0; n < SPIN_LDS; ++n) {
          tnow = rt32();
          if ((int)(tnow - (pA + (((u32)nis * pP) >> 8))) >= 0) break;
          __builtin_amdgcn_s_sleep(1);
        }
      }
    };
    auto issue = [&]() {
      pace();
      const int mi = nis < 2 * KT ? nis >> 1 : nis - KT;
      const u32 soff = (u32)(nis >> 1) * 65536u + sbase, ldst = ring + (u32)(mi & (RING - 1)) * 4096u;
      if (nis & 1) dma4v(ldst, vv0, vv1, vv0, vv1, vsr, soff); else dma4k(ldst, vk0, vk1, vk2, vk3, ksr, soff);
      ++nis;
    };
    const u32 kfo = c * 256 + g * 16;
    u32x4 kr[KT ? 4 * KT : 1];
#define KREG(T)                                                                                                          \
    if constexpr (T < KT) {                                                                                              \
      if (T > 0) { if (2 * T >= DPRO) wait_units(DPRO - 1); pace(); }                                                    \
      asm volatile("buffer_load_dwordx4 %0, %4, %5, %6 offen nt\n\tbuffer_load_dwordx4 %1, %4, %5, %6 offen offset:64 nt\n\t"   \
                   "buffer_load_dwordx4 %2, %4, %5, %6 offen offset:128 nt\n\tbuffer_load_dwordx4 %3, %4, %5, %6 offen offset:192 nt" \
                   : "=&v"(kr[(4 * T) % (4 * KT + !KT)]), "=&v"(kr[(4 * T + 1) % (4 * KT + !KT)]), "=&v"(kr[(4 * T + 2) % (4 * KT + !KT)]), "=&v"(kr[(4 * T + 3) % (4 * KT + !KT)]) \
                   : "v"(kfo), "s"(ksr), "s"((u32)(T * 65536u + sbase)) : "memory");                                     \
      ++nis;                                                                                                             \
      if (T == 0) bar();                                                           \
      if (2 * T + 1 >= DPRO) wait_units(DPRO - 1);                                                                       \
      issue();                                                                                                           \
    }
    if constexpr (KT == 0) { issue(); bar(); }
    KREG(0) KREG(1) KREG(2) KREG(3) KREG(4) KREG(5)
#undef KREG
    _Pragma("nounroll")
    while (nis < KT + RING) {
      if (nis >= DPRO) wait_units(DPRO - 1);
      if (lds_ld(L + L_FLAG + F_Q) >= 4) break;
      issue();
    }
    landed = nis > DPRO ? nis - DPRO : 0;
    auto refill = [&](int rcons) {
      while (nis - landed < DAHEAD && nis < NUNIT && (nis < 2 * KT ? nis >> 1 : nis - KT) < rcons + RING) issue();
    };
    STAMP(1);
    if (!STREAMONLY) lds_wait_ge(L + L_FLAG + F_Q, 4);
    STAMP(2);
    if (REPORT == 2 && wave == 0 && lane == 0) *(volatile LAS u32*)(L + L_FLAG + 48) = rt32() - t0;
    u32x4 qf[4];
#pragma unroll
    for (int s = 0; s < 4; ++s) qf[s] = *(LAS u32x4*)(L + L_Q + (c & 3) * 256 + 64 * s + 16 * g);
    refill(0);
    LAS unsigned char* R = L + L_RING + wave * (RING * 4096);
    const u32 ka = c * 256 + 16 * (g ^ c);
    const u32 va = (4 * g + (c >> 2)) * 256 + 32 * ((4 * g + (c >> 2)) & 7) + 8 * (c & 3);
    f32x4 pre[KT ? KT : 1];
    if constexpr (KT > 0) {
      wait_units(nis - (2 * KT - 1));
      if constexpr (KT == 4)
        asm volatile("" : "+v"(kr[0]), "+v"(kr[1]), "+v"(kr[2]), "+v"(kr[3]), "+v"(kr[4]), "+v"(kr[5]), "+v"(kr[6]), "+v"(kr[7]),
                          "+v"(kr[8]), "+v"(kr[9]), "+v"(kr[10]), "+v"(kr[11]), "+v"(kr[12]), "+v"(kr[13]), "+v"(kr[14]), "+v"(kr[15]) :: "memory");
      if constexpr (KT == 6) {
        asm volatile("" : "+v"(kr[0]), "+v"(kr[1]), "+v"(kr[2]), "+v"(kr[3]), "+v"(kr[4]), "+v"(kr[5]), "+v"(kr[6]), "+v"(kr[7]),
                          "+v"(kr[8]), "+v"(kr[9]), "+v"(kr[10]), "+v"(kr[11]) :: "memory");
        asm volatile("" : "+v"(kr[12 % (4 * KT)]), "+v"(kr[13 % (4 * KT)]), "+v"(kr[14 % (4 * KT)]), "+v"(kr[15 % (4 * KT)]), "+v"(kr[16 % (4 * KT)]), "+v"(kr[17 % (4 * KT)]), "+v"(kr[18 % (4 * KT)]), "+v"(kr[19 % (4 * KT)]),
                          "+v"(kr[20 % (4 * KT)]), "+v"(kr[21 % (4 * KT)]), "+v"(kr[22 % (4 * KT)]), "+v"(kr[23 % (4 * KT)]) :: "memory");
      }
      landed = landed > 2 * KT - 1 ? landed : 2 * KT - 1;
#pragma unroll
      for (int t = 0; t < KT; ++t) {
        f32x4 sa = {0.f, 0.f, 0.f, 0.f}, sb = {0.f, 0.f, 0.f, 0.f};
        sa = mfma32(kr[4 * t], qf[0], sa); sb = mfma32(kr[4 * t + 1], qf[1], sb); sa = mfma32(kr[4 * t + 2], qf[2], sa); sb = mfma32(kr[4 * t + 3], qf[3], sb);
        pre[t] = sa + sb;
      }
    }
    float m = -INFINITY, lsum = 0.f;
    f32x4 acc[8];
#pragma unroll
    for (int i = 0; i < 8; ++i) acc[i] = f32x4{0.f, 0.f, 0.f, 0.f};
    _Pragma("nounroll")
    for (int j = 0; j < NUNIT / 2; ++j) {
      f32x4 sc;
      const int rk = j < KT ? j : 2 * j - KT;
      if (j < KT) {
        sc = pre[0];
#pragma unroll
        for (int t = 0; t + 1 < KT; ++t) pre[t] = pre[t + 1];
      } else {
        wait_units(nis - 2 * j - 1);
        landed = landed > 2 * j + 1 ? landed : 2 * j + 1;
        refill(rk);
        LAS unsigned char* Kt = R + (rk & (RING - 1)) * 4096;
        f32x4 sa = {0.f, 0.f, 0.f, 0.f}, sb = {0.f, 0.f, 0.f, 0.f};
        const u32x4 k0 = *(LAS u32x4*)(Kt + ka), k1 = *(LAS u32x4*)(Kt + (ka ^ 64)), k2 = *(LAS u32x4*)(Kt + (ka ^ 128)), k3 = *(LAS u32x4*)(Kt + (ka ^ 192));
        sa = mfma32(k0, qf[0], sa); sb = mfma32(k1, qf[1], sb); sa = mfma32(k2, qf[2], sa); sb = mfma32(k3, qf[3], sb);
        sc = sa + sb;
      }
      if (ADAPT) {
        const u32 te = __builtin_amdgcn_readfirstlane(lds_ld(L + L_FLAG + F_PACE + 8));
        tnow = rt32();
        if (te != tgt && nis < NUNIT - 1) {
          tgt = te;
          const int span = (int)(te - tnow);
          u32 np = span > 0 ? __builtin_amdgcn_readfirstlane((u32)((float)span * 256.f * __builtin_amdgcn_rcpf((float)(NUNIT - 1 - nis + 1)))) : PER256;
          np = np < PER256 ? PER256 : np;
          pP = np; pA = te - (((u32)(NUNIT - 1) * np) >> 8);
        }
        if (wave == 0) *(volatile LAS u32*)(L + L_FLAG + F_PROG) = (tnow << 8) | (u32)(2 * j + 1);
      }
      if (REPORT == 4 && j == 10 && wave == 0 && lane == 0) *(volatile LAS u32*)(L + L_FLAG + 48) = rt32() - t0;
      if (REPORT == 5 && j == 20 && wave == 0 && lane == 0) *(volatile LAS u32*)(L + L_FLAG + 48) = rt32() - t0;
      const int rv = j < KT ? j : rk + 1;
      LAS unsigned char* Vt = R + (rv & (RING - 1)) * 4096;
      const float tmax = fmaxf(fmaxf(sc[0], sc[1]), fmaxf(sc[2], sc[3]));
      if (__builtin_amdgcn_ballot_w64(tmax > m + THR_RAW) != 0ull) {
        const float mn = fmaxf(m, xrow_max(tmax));
        const float al = __builtin_amdgcn_exp2f((m - mn) * SC);
        m = mn; lsum *= al;
        const float a0 = dpp<0x00>(al), a1 = dpp<0x55>(al), a2 = dpp<0xAA>(al), a3 = dpp<0xFF>(al);
#pragma unroll
        for (int i = 0; i < 8; ++i) { acc[i][0] *= a0; acc[i][1] *= a1; acc[i][2] *= a2; acc[i][3] *= a3; }
      }
      const float mc = m * SC;
      const float p0 = __builtin_amdgcn_exp2f(fmaf(sc[0], SC, -mc)), p1 = __builtin_amdgcn_exp2f(fmaf(sc[1], SC, -mc));
      const float p2 = __builtin_amdgcn_exp2f(fmaf(sc[2], SC, -mc)), p3 = __builtin_amdgcn_exp2f(fmaf(sc[3], SC, -mc));
      lsum += (p0 + p1) + (p2 + p3);
      const u32x2 pa = {pk2(p0, p1), pk2(p2, p3)};
      wait_units(nis - 2 * j - 2);
      landed = landed > 2 * j + 2 ? landed : 2 * j + 2;
      refill(rv);
#pragma unroll
      for (int i = 0; i < 8; ++i) acc[i] = mfma16(pa, trread(Vt + (va ^ (32 * i))), acc[i]);
      refill(rv + 1);
    }
    STAMP(3);
    if (REPORT == 3 && wave == 0 && lane == 0) *(volatile LAS u32*)(L + L_FLAG + 48) = rt32() - t0;
    lsum = xrow_sum(lsum);
    if (g == 0) {
#pragma unroll
      for (int i = 0; i < 8; ++i) *(LAS f32x4*)(R + i * 256 + c * 16) = acc[i];
      if (c < 4) *(LAS f32x2*)(R + 2048 + c * 8) = f32x2{m * SC, lsum};
    }
  } else {
    const int hw = wave - 4;
    if (STREAMONLY) { bar(); bar(); STAMP_FLUSH(); return; }
    if (HPRIO) __builtin_amdgcn_s_setprio(HPRIO);
    u32x4 xa, xb; u32x2 ev;
    if (hw == 0 && lane < 16) {
      constexpr u32 per256 = (u32)(4096.0 * 1024.0 / ((PACE10 ? PACE10 : 1) * 0.1e12) * 1e8 * 256.0);
      *(LAS u32x4*)(L + L_FLAG + lane * 16) = lane == 2 ? u32x4{t0 - LEADT, per256, 0, 0} : u32x4{0, 0, 0, 0};
    }
    const int kq = r & 3;
    if (NCODE && r * 4 + hw < NCODE) {
      dma1p(lbase + L_SCR + hw * 1024, (const char*)(pc0 & ~255ull) + (r * 4 + hw) * 1024 + lane * 16);
    }
    {
      const u16* xp = p.x + (2 * hw) * DM + lane * 8;
      const u16* wp = p.wq + (size_t)(kq * 128 + hw * 32 + (lane >> 3)) * 4096 + xg * 512 + (r >> 2) * 64 + (lane & 7) * 8;
      const u32 ldst = lbase + L_STAGE + hw * 4096;
      u32 keep;
      asm volatile("global_load_dwordx4 %0, %4, off\n\tglobal_load_dwordx4 %1, %4, off offset:1024\n\tglobal_load_dwordx2 %2, %5, off\n\t"
                   "s_mov_b32 %3, m0\n\ts_mov_b32 m0, %10\n\ts_nop 0\n\t"
                   "global_load_lds_dwordx4 %6, off" WNT_S "\n\ts_add_u32 m0, m0, 0x400\n\ts_nop 0\n\t"
                   "global_load_lds_dwordx4 %7, off" WNT_S "\n\ts_add_u32 m0, m0, 0x400\n\ts_nop 0\n\t"
                   "global_load_lds_dwordx4 %8, off" WNT_S "\n\ts_add_u32 m0, m0, 0x400\n\ts_nop 0\n\t"
                   "global_load_lds_dwordx4 %9, off" WNT_S "\n\ts_mov_b32 m0, %3\n\t"
                   "s_waitcnt lgkmcnt(0)\n\ts_barrier\n\ts_waitcnt vmcnt(4)"
                   : "=&v"(xa), "=&v"(xb), "=&v"(ev), "=&s"(keep)
                   : "v"(xp), "v"(p.ws + WS_EPOCH), "v"(wp), "v"(wp + (size_t)8 * 4096), "v"(wp + (size_t)16 * 4096), "v"(wp + (size_t)24 * 4096), "s"(ldst)
                   : "memory", "scc");
    }
    const u64 ep = (((u64)ev.y << 32) | ev.x) + 1ull;
    const u32 tag32 = (u32)ep, t48lo = ((u32)ep) << 16, t48hi = (u32)(ep >> 16), t40lo = ((u32)ep) << 24, t40hi = (u32)(ep >> 8);
    STAMP(1);
#pragma unroll
    for (int rr = 0; rr < 2; ++rr) {
      const u32x4 xv = rr ? xb : xa;
      float v[8];
#pragma unroll
      for (int i = 0; i < 4; ++i) { v[2 * i] = bflo(xv[i]); v[2 * i + 1] = bfhi(xv[i]); }
      const float mean = wave_sum(((v[0] + v[1]) + (v[2] + v[3])) + ((v[4] + v[5]) + (v[6] + v[7]))) * (1.f / DM);
      float q2 = 0.f;
#pragma unroll
      for (int i = 0; i < 8; ++i) { const float d = v[i] - mean; q2 = fmaf(d, d, q2); }
      const float var = rbf(wave_sum(q2) * (1.f / DM));
      const float rden = __builtin_amdgcn_rcpf(rbf(__builtin_amdgcn_sqrtf(rbf(var + 1e-5f))));
      const float mb = rbf(mean);
      u32x4 o;
#pragma unroll
      for (int i = 0; i < 4; ++i) { const u32 d = pk2(v[2 * i] - mb, v[2 * i + 1] - mb); o[i] = pk2(bflo(d) * rden, bfhi(d) * rden); }
      *(LAS u32x4*)(L + L_XN + (2 * hw + rr) * XN_STR + lane * 16) = o;
    }
    u32 nsync = 0;
    auto hsync = [&]() {
      asm volatile("s_waitcnt vmcnt(0) lgkmcnt(0)" ::: "memory");
      if (lane == 0) __hip_atomic_fetch_add((LAS u32*)(L + L_FLAG + F_SYNC), 1u, __ATOMIC_RELAXED, __HIP_MEMORY_SCOPE_WORKGROUP);
      nsync += 4;
      lds_wait_ge(L + L_FLAG + F_SYNC, nsync);
    };
    const int tq = c >> 2, tp = c & 3;
    auto tile_mma = [&](const u32x4 (&act)[4], int cg) {
      f32x4 a0 = {0.f, 0.f, 0.f, 0.f}, a1 = {0.f, 0.f, 0.f, 0.f};
      LAS unsigned char* ap = L + L_STAGE + (8 * g + tq) * 128 + 32 * cg + 8 * tp;
      a0 = mfma32(act[0], trread2(ap, ap + 512), a0);
      a1 = mfma32(act[1], trread2(ap + 4096, ap + 4096 + 512), a1);
      a0 = mfma32(act[2], trread2(ap + 8192, ap + 8192 + 512), a0);
      a1 = mfma32(act[3], trread2(ap + 12288, ap + 12288 + 512), a1);
      return a0 + a1;
    };
    auto xn_frags = [&](u32x4 (&act)[4], int k0) {
#pragma unroll
      for (int s = 0; s < 4; ++s) act[s] = *(LAS u32x4*)(L + L_XN + (c & 7) * XN_STR + 2 * k0 + 64 * s + 16 * g);
    };
    auto f24 = [&](float f) { const u32 u = __float_as_uint(f); return t40lo | ((u + 0x7fu + ((u >> 8) & 1u)) >> 8); };
    auto ok40 = [&](const u32x4& t) { return ((t[0] ^ t40lo) >> 24) == 0 && t[1] == t40hi && ((t[2] ^ t40lo) >> 24) == 0 && t[3] == t40hi; };
    auto ok48 = [&](const u32x4& t) { return ((t[0] ^ t48lo) >> 16) == 0 && t[1] == t48hi && ((t[2] ^ t48lo) >> 16) == 0 && t[3] == t48hi; };
    u32x4 act[4];
    LGKM0();
    STAMP(11);
    hsync();
    STAMP(2);
    xn_frags(act, kq * 128);
    {
      const f32x4 d = tile_mma(act, hw);
      if (g < 2) {
#pragma unroll
        for (int i = 0; i < 4; ++i) st_gran(p.ws, WS_Q + ((((xg * 8 + 4 * g + i) * 4 + kq) * 512) + (r >> 2) * 64 + 16 * hw + c) * 8, f24(d[i]), t40hi);
      }
    }
    STAMP(3);
    if (hw < 2) {
      LGKM0();
      if (lane == 0) __hip_atomic_fetch_add((LAS u32*)(L + L_FLAG + F_SYNC), 1u, __ATOMIC_RELAXED, __HIP_MEMORY_SCOPE_WORKGROUP);
    }
    if (hw < 2) {
      const u32 qb = WS_Q + ((xg * 8 + ab) * 4) * 512 * 8;
      {
        const u32 due = t0 + TQ0 - (u32)(r * TQR) / 16;
        while ((int)(rt32() - due) < 0) __builtin_amdgcn_s_sleep(2);
      }
      float q0[2], q1[2];
_Pragma("nounroll")
      for (u32 n = 0; n < SPIN_GLB; ++n) {
        bool ok = true;
#pragma unroll
        for (int h = 0; h < 2; ++h) {
          q0[h] = 0.f; q1[h] = 0.f;
#pragma unroll
          for (int i = 0; i < 4; ++i) {
            const u32x4 t = __builtin_amdgcn_raw_buffer_load_b128(wsr, qb + (i * 512 + (2 * hw + h) * 128 + 2 * lane) * 8, 0, 16);
            ok &= ok40(t); q0[h] += __uint_as_float(t[0] << 8); q1[h] += __uint_as_float(t[2] << 8);
          }
        }
        if (__all(ok)) break;
        __builtin_amdgcn_s_sleep(POLLSLP);
        asm volatile("" ::: "memory");
      }
      *(LAS u32*)(L + L_Q + (2 * hw) * 256 + lane * 4) = pk2(q0[0], q1[0]);
      *(LAS u32*)(L + L_Q + (2 * hw + 1) * 256 + lane * 4) = pk2(q0[1], q1[1]);
      LGKM0();
      if (lane == 0) __hip_atomic_fetch_add((LAS u32*)(L + L_FLAG + F_Q), 2u, __ATOMIC_RELAXED, __HIP_MEMORY_SCOPE_WORKGROUP);
      STAMP(4);
      if (hw == 1) {
        { const u32 due = t0 + TGS; while ((int)(rt32() - due) < 0) __builtin_amdgcn_s_sleep(32); }
_Pragma("nounroll")
        for (u32 n = 0; n < SPIN_GLB; ++n) {
          const u32x4 a = __builtin_amdgcn_raw_buffer_load_b128(wsr, WS_START + lane * 16, 0, 16);
          const u32x4 b = __builtin_amdgcn_raw_buffer_load_b128(wsr, WS_START + 1024 + lane * 16, 0, 16);
          const u32 e0 = (u32)ep, e1 = (u32)(ep >> 32);
          const bool ok = a[0] == e0 && a[1] == e1 && a[2] == e0 && a[3] == e1 && b[0] == e0 && b[1] == e1 && b[2] == e0 && b[3] == e1;
          if (__all(ok)) break;
          __builtin_amdgcn_s_sleep(32);
          asm volatile("" ::: "memory");
        }
        STAMP(7);
        if (ADAPT) {
          float gnp[4] = {0.f, 0.f, 0.f, 0.f}, gtp[4] = {0.f, 0.f, 0.f, 0.f};
          _Pragma("nounroll")
          for (int round = 0; round < NROUND; ++round) {
            { const u32 due = t0 + TA0 + round * TAP - (u32)(r * TQR) / 16; while ((int)(rt32() - due) < 0) __builtin_amdgcn_s_sleep(8); }
            const u32 pv = lds_ld(L + L_FLAG + F_PROG);
            if (lane == 0) st_gran(p.ws, WS_PROG + (round * 256 + blk) * 8, t40lo | ((pv >> 8 & 0x1ffffu) << 7) | (pv & 127u), t40hi);
            { const u32 due = t0 + TA0 + TAW + round * TAP - (u32)(r * TQR) / 16; while ((int)(rt32() - due) < 0) __builtin_amdgcn_s_sleep(8); }
            u32x4 a, b;
            bool okk = false;
            _Pragma("nounroll")
            for (int n = 0; n < 3; ++n) {
              a = __builtin_amdgcn_raw_buffer_load_b128(wsr, WS_PROG + round * 2048 + lane * 32, 0, 16);
              b = __builtin_amdgcn_raw_buffer_load_b128(wsr, WS_PROG + round * 2048 + lane * 32 + 16, 0, 16);
              okk = __all(ok40(a) && ok40(b));
              if (okk) break;
              __builtin_amdgcn_s_sleep(32);
              asm volatile("" ::: "memory");
            }
            if (!okk) continue;
            const u32 now = rt32();
            const u32 w4[4] = {a[0], a[2], b[0], b[2]};
            float gn[4], gt[4];
#pragma unroll
            for (int i = 0; i < 4; ++i) {
              float n_ = (float)(w4[i] & 127u), t_ = (float)(((int)(((w4[i] >> 7) - now) << 15)) >> 15);
              n_ += dpp<0x122>(n_); n_ += dpp<0x124>(n_); n_ += dpp<0x128>(n_); gn[i] = xrow_sum(n_) * (1.f / 32.f);
              t_ += dpp<0x122>(t_); t_ += dpp<0x124>(t_); t_ += dpp<0x128>(t_); gt[i] = xrow_sum(t_) * (1.f / 32.f) + (float)(int)(now - t0);
            }
            float sn = (gn[0] + gn[1]) + (gn[2] + gn[3]), st = (gt[0] + gt[1]) + (gt[2] + gt[3]);
            sn = (sn + dpp<0xB1>(sn)) * 0.125f; st = (st + dpp<0xB1>(st)) * 0.125f;
            const float tn = (float)(int)(now - t0);
            float P = 0.f, pm = 0.f;
            if (round > 0 && gtp[0] != 0.f && sn - gnp[0] > 2.f) {
              P = (st - gtp[0]) * __builtin_amdgcn_rcpf(sn - gnp[0]);
              pm = st + (64.f - sn) * P * (ABIAS * 0.001f) - (float)ALAT;
              if (lane == 0 && ADAPT == 1) *(volatile LAS u32*)(L + L_FLAG + F_PACE + 8) = (t0 + (u32)(int)pm) | 1u;
            }
            gnp[0] = sn; gtp[0] = st;
          }
        }
      }
    } else {
      const int h2 = hw - 2;
      u32 nsync2 = 0;
      auto arrive2 = [&]() {
        LGKM0();
        if (lane == 0) __hip_atomic_fetch_add((LAS u32*)(L + L_FLAG + F_SYNC2), 1u, __ATOMIC_RELAXED, __HIP_MEMORY_SCOPE_WORKGROUP);
        nsync2 += 2;
        lds_wait_ge(L + L_FLAG + F_SYNC2, nsync2);
      };
      auto stage2 = [&](const u16* w, int ld, const u16* wbase = nullptr, int tidx = 0) {
        if (FAKEC && wbase) { w = wbase + (size_t)tidx * 8192; ld = 64; }
        const u16* wp = w + (size_t)(h2 * 64 + (lane >> 3)) * ld + (lane & 7) * 8;
#pragma unroll
        for (int i = 0; i < 8; ++i) dma1(lbase + L_STAGE + h2 * 8192 + i * 1024, wp + (size_t)i * 8 * ld);
      };
      LGKM0();
      if (lane == 0) __hip_atomic_fetch_add((LAS u32*)(L + L_FLAG + F_SYNC), 1u, __ATOMIC_RELAXED, __HIP_MEMORY_SCOPE_WORKGROUP);
      if (h2 == 1) {
        const u32x4 z = {0u, ~tag32, 0u, ~tag32};
#pragma unroll
        for (int i = 0; i < 2; ++i)
          __builtin_amdgcn_raw_buffer_store_b128(z, wsr, WS_OP + ((((xg * 4 + 2 * i + (lane >> 5)) * 8 + ab) * 4 + as) * 64) * 8 + (lane & 31) * 16, 0, 16);
        if (lane < 4) __builtin_amdgcn_raw_buffer_store_b128(z, wsr, WS_ML + (((xg * 4 + lane) * 8 + ab) * 4 + as) * 16, 0, 16);
#pragma unroll
        for (int i = 0; i < 4; ++i) {
          const int pc = 8 * i + (lane >> 3), b = pc >> 2, w = pc & 3;
          __builtin_amdgcn_raw_buffer_store_b128(z, wsr, WS_OUT + (((b * 32 + 4 * wcc + w) * 48) + whead) * 128 + (lane & 7) * 16, 0, 16);
          if (r >= 18) __builtin_amdgcn_raw_buffer_store_b128(z, wsr, WS_OUT + (((b * 32 + 4 * xg + w) * 48) + 32 + (r - 18)) * 128 + (lane & 7) * 16, 0, 16);
        }
      }
      lds_wait_ge(L + L_FLAG + F_SYNC, 8);
      const bool f1 = r < 14 && !SKIP_FFN, f15 = r >= 14 && r < 18 && !SKIP_FFN, f2 = r >= 18 && !SKIP_FFN;
      const int t1 = xg * 14 + r, sl1 = t1 >> 2, fq = t1 & 3, ks = r - 18;
      const u16* wo_tile = p.wo + (size_t)whead * 128 * DM + wcc * 64;
      if (f1) stage2(p.wg + (size_t)fq * 128 * FF + sl1 * 64, FF, p.wg, t1);
      else if (f2) stage2(p.wd + (size_t)ks * 128 * DM + xg * 64, DM, p.wd, ks * 8 + xg);
      else stage2(wo_tile, DM, p.wo, r * 8 + xg);
      VMCNT(0);
      if (h2 == 1 && lane == 0) {
        __hip_atomic_store((GAS u64*)(p.ws + WS_START + blk * 8), ep, __ATOMIC_RELAXED, __HIP_MEMORY_SCOPE_AGENT);
        *(volatile LAS u64*)(L + L_FLAG + F_EPOCH) = ep;
      }
      arrive2();
      if (f1) {
        xn_frags(act, fq * 128);
        f32x4 d[2];
        d[0] = tile_mma(act, 2 * h2); d[1] = tile_mma(act, 2 * h2 + 1);
        asm volatile("" : "+v"(d[0]), "+v"(d[1]));
        arrive2();
        stage2(p.wu + (size_t)fq * 128 * FF + sl1 * 64, FF, p.wu, t1);
#pragma unroll
        for (int w = 0; w < 2; ++w) if (g < 2) {
#pragma unroll
          for (int i = 0; i < 4; ++i) st_gran(p.ws, WS_GU + ((((sl1 * 2 + 0) * 4 + fq) * 8 + 4 * g + i) * 64 + 16 * (2 * h2 + w) + c) * 8, f24(d[w][i]), t40hi);
        }
        VMCNT(8);
        arrive2();
        d[0] = tile_mma(act, 2 * h2); d[1] = tile_mma(act, 2 * h2 + 1);
        asm volatile("" : "+v"(d[0]), "+v"(d[1]));
        arrive2();
        stage2(wo_tile, DM, p.wo, r * 8 + xg);
#pragma unroll
        for (int w = 0; w < 2; ++w) if (g < 2) {
#pragma unroll
          for (int i = 0; i < 4; ++i) st_gran(p.ws, WS_GU + ((((sl1 * 2 + 1) * 4 + fq) * 8 + 4 * g + i) * 64 + 16 * (2 * h2 + w) + c) * 8, f24(d[w][i]), t40hi);
        }
        VMCNT(8);
        arrive2();
      } else if (f15) {
        const int sl = xg * 4 + (r - 14);
        if (sl < 28 && !NOPOLL) {
          { const u32 due = t0 + TG15; while ((int)(rt32() - due) < 0) __builtin_amdgcn_s_sleep(16); }
          {
            const u32 so = WS_GU + ((((sl * 2 + 1) * 4 + (lane & 3)) * 8) * 64 + 63) * 8;
_Pragma("nounroll")
            for (u32 n = 0; n < SPIN_GLB; ++n) {
              const u64 t = ld_gran(p.ws, so);
              if (__all((((u32)t ^ t40lo) >> 24) == 0 && (u32)(t >> 32) == t40hi)) break;
              __builtin_amdgcn_s_sleep(16);
            }
          }
          float gs[2][2], us[2][2];
          const int cp = lane & 31;
_Pragma("nounroll")
          for (u32 n = 0; n < SPIN_GLB; ++n) {
            bool ok = true;
#pragma unroll
            for (int bb = 0; bb < 2; ++bb) {
              const int b = 4 * h2 + 2 * bb + (lane >> 5);
              gs[bb][0] = gs[bb][1] = us[bb][0] = us[bb][1] = 0.f;
#pragma unroll
              for (int i = 0; i < 4; ++i) {
                const u32x4 tg = __builtin_amdgcn_raw_buffer_load_b128(wsr, WS_GU + ((((sl * 2 + 0) * 4 + i) * 8 + b) * 64 + 2 * cp) * 8, 0, 16);
                const u32x4 tu = __builtin_amdgcn_raw_buffer_load_b128(wsr, WS_GU + ((((sl * 2 + 1) * 4 + i) * 8 + b) * 64 + 2 * cp) * 8, 0, 16);
                ok &= ok40(tg) && ok40(tu);
                gs[bb][0] += __uint_as_float(tg[0] << 8); gs[bb][1] += __uint_as_float(tg[2] << 8);
                us[bb][0] += __uint_as_float(tu[0] << 8); us[bb][1] += __uint_as_float(tu[2] << 8);
              }
            }
            if (__all(ok)) break;
            __builtin_amdgcn_s_sleep(16);
            asm volatile("" ::: "memory");
          }
#pragma unroll
          for (int bb = 0; bb < 2; ++bb) {
            const int b = 4 * h2 + 2 * bb + (lane >> 5);
            const u32 hp = pk2(rbf(gs[bb][0]) * rbf(us[bb][0]), rbf(gs[bb][1]) * rbf(us[bb][1]));
            st_gran(p.ws, WS_H + (b * FF + sl * 64 + 2 * cp) * 8, t48lo | (hp & 0xffffu), t48hi);
            st_gran(p.ws, WS_H + (b * FF + sl * 64 + 2 * cp + 1) * 8, t48lo | (hp >> 16), t48hi);
          }
        }
      } else if (f2) {
        { const u32 due = t0 + TG2; while ((int)(rt32() - due) < 0) __builtin_amdgcn_s_sleep(16); }
        {
          const u32 so = WS_H + (7 * FF + ks * 128 + 64 * (lane & 1) + 63) * 8;
_Pragma("nounroll")
          for (u32 n = 0; n < (NOPOLL ? 0 : SPIN_GLB); ++n) {
            const u64 t = ld_gran(p.ws, so);
            if (__all((((u32)t ^ t48lo) >> 16) == 0 && (u32)(t >> 32) == t48hi)) break;
            __builtin_amdgcn_s_sleep(32);
          }
        }
        const int b = lane >> 3, k0 = 64 * h2 + 8 * (lane & 7);
        u32x4 hv = {0, 0, 0, 0};
_Pragma("nounroll")
        for (u32 n = 0; n < (NOPOLL ? 0 : SPIN_GLB); ++n) {
          bool ok = true;
#pragma unroll
          for (int i = 0; i < 4; ++i) {
            const u32x4 t = __builtin_amdgcn_raw_buffer_load_b128(wsr, WS_H + (b * FF + ks * 128 + k0 + 2 * i) * 8, 0, 16);
            ok &= ok48(t);
            hv[i] = (t[0] & 0xffffu) | (t[2] << 16);
          }
          if (__all(ok)) break;
          __builtin_amdgcn_s_sleep(16);
          asm volatile("" ::: "memory");
        }
        *(LAS u32x4*)(L + L_O + b * 256 + k0 * 2) = hv;
        arrive2();
#pragma unroll
        for (int s = 0; s < 4; ++s) act[s] = *(LAS u32x4*)(L + L_O + (c & 7) * 256 + 64 * s + 16 * g);
        f32x4 d[2];
        d[0] = tile_mma(act, 2 * h2); d[1] = tile_mma(act, 2 * h2 + 1);
        asm volatile("" : "+v"(d[0]), "+v"(d[1]));
        arrive2();
        stage2(wo_tile, DM, p.wo, r * 8 + xg);
#pragma unroll
        for (int w = 0; w < 2; ++w) if (g < 2) {
#pragma unroll
          for (int i = 0; i < 4; ++i) st_gran(p.ws, WS_OUT + ((((4 * g + i) * 32 + 4 * xg + 2 * h2 + w) * 48) + 32 + ks) * 128 + c * 8, __float_as_uint(d[w][i]), tag32);
        }
        VMCNT(8);
        arrive2();
      }
      STAMP(5);
      if (h2 == 0 && lane == 0) *(volatile LAS u32*)(L + L_FLAG + F_WO) = 1;
      STAMP(6);
    }
_Pragma("nounroll")
    for (u32 n = 0; n < SPIN_LDS; ++n) { if (lds_ld(L + L_FLAG + F_WO) >= 1) break; __builtin_amdgcn_s_sleep(16); }
    asm volatile("" ::: "memory");
  }

  u32x4 wof[4];
  if (wave >= 4) {
    const int tq = c >> 2, tp = c & 3, w = wave - 4;
#pragma unroll
    for (int s = 0; s < 4; ++s) { LAS unsigned char* ap = L + L_STAGE + (32 * s + 8 * g + tq) * 128 + 32 * w + 8 * tp; wof[s] = trread2(ap, ap + 512); }
  }
  int pass0 = (DRY && wave >= 4) ? 0 : 1;
  asm volatile("" : "+v"(pass0));
#pragma nounroll
  for (int pass = __builtin_amdgcn_readfirstlane(pass0); pass < 2; ++pass) {
    const bool real = pass != 0;
    if (real) bar();
    STAMP(8);
    if (STREAMONLY) { STAMP(9); STAMP(10); if (wave < 4 && lane == 0) p.out[blk * 4 + wave] = *(LAS u16*)(L + L_RING + wave * (RING * 4096) + 8); STAMP_FLUSH(); return; }
    const u64 ep = *(volatile LAS u64*)(L + L_FLAG + F_EPOCH);
    const u32 tag32 = (u32)ep;
    const u32 nspin = real ? SPIN_GLB : 1u;
    if (wave < 4 || !real) {
      const int hh = wave & 3, ch = lane >> 3, c0 = (2 * lane) & 15;
      float mw[4], lw[4], v0[4], v1[4];
#pragma unroll
      for (int w = 0; w < 4; ++w) {
        LAS unsigned char* R = L + L_RING + w * (RING * 4096);
        const f32x2 ml = *(LAS f32x2*)(R + 2048 + hh * 8);
        mw[w] = ml[0]; lw[w] = ml[1];
        v0[w] = *(LAS float*)(R + ch * 256 + c0 * 16 + hh * 4);
        v1[w] = *(LAS float*)(R + ch * 256 + c0 * 16 + 16 + hh * 4);
      }
      const float M = fmaxf(fmaxf(mw[0], mw[1]), fmaxf(mw[2], mw[3]));
      float Ls = 0.f, o0 = 0.f, o1 = 0.f;
#pragma unroll
      for (int w = 0; w < 4; ++w) { const float f = __builtin_amdgcn_exp2f(mw[w] - M); Ls = fmaf(f, lw[w], Ls); o0 = fmaf(f, v0[w], o0); o1 = fmaf(f, v1[w], o1); }
      const float inv = __builtin_amdgcn_rcpf(Ls);
      const u32 slot = ((xg * 4 + hh) * 8 + ab) * 4 + as;
      const u32 ov = pk2(o0 * inv, o1 * inv);
      asm volatile("" :: "v"(ov), "v"(Ls), "v"(M));
      if (real) {
        st_gran(p.ws, WS_OP + (slot * 64 + lane) * 8, ov, tag32);
        if (lane < 2) st_gran(p.ws, WS_ML + (slot * 2 + lane) * 8, __float_as_uint(lane ? Ls : M), tag32);
      }
      STAMP(11);
    }
    if (wave >= 4) {
      const int w = wave - 4;
      const u32 slot = (whead * 8 + 2 * w) * 4;
      u64 o[8] = {0, 0, 0, 0, 0, 0, 0, 0}, ml = 0;
      if (H1DLY && real) __builtin_amdgcn_s_sleep(H1DLY);
      {
        u32 have = 0;
_Pragma("nounroll")
        for (u32 n = 0; n < nspin; ++n) {
          u64 t[9];
#pragma unroll
          for (int s = 0; s < 8; ++s) t[s] = ld_gran(p.ws, ((have >> s) & 1) ? WS_DUMMY : WS_OP + ((slot + s) * 64 + lane) * 8);
          t[8] = ld_gran(p.ws, ((have >> 8) & 1) ? WS_DUMMY : WS_ML + (slot * 2 + (lane & 15)) * 8);
#pragma unroll
          for (int s = 0; s < 9; ++s) {
            const bool fresh = !((have >> s) & 1);
            if (s < 8) o[s] = fresh ? t[s] : o[s]; else ml = fresh ? t[8] : ml;
            if (__all((u32)(t[s] >> 32) == tag32)) have |= 1u << s;
          }
          if (have == 0x1ffu) break;
          __builtin_amdgcn_s_sleep(POLLSLP);
        }
      }
      const u32 mlv = (u32)ml;
#pragma unroll
      for (int bb = 0; bb < 2; ++bb) {
        float ms[4], ls[4];
#pragma unroll
        for (int s = 0; s < 4; ++s) { ms[s] = __uint_as_float(__builtin_amdgcn_readlane(mlv, 8 * bb + 2 * s)); ls[s] = __uint_as_float(__builtin_amdgcn_readlane(mlv, 8 * bb + 2 * s + 1)); }
        const float M = fmaxf(fmaxf(ms[0], ms[1]), fmaxf(ms[2], ms[3]));
        float W = 0.f, o0 = 0.f, o1 = 0.f;
#pragma unroll
        for (int s = 0; s < 4; ++s) { const float wt = ls[s] * __builtin_amdgcn_exp2f(ms[s] - M); W += wt; o0 = fmaf(wt, bflo((u32)o[4 * bb + s]), o0); o1 = fmaf(wt, bfhi((u32)o[4 * bb + s]), o1); }
        const float inv = __builtin_amdgcn_rcpf(W);
        *(LAS u32*)(L + L_O + (2 * w + bb) * 256 + lane * 4) = pk2(o0 * inv, o1 * inv);
      }
      STAMP(9);
      LGKM0();
      if (real) {
        if (lane == 0) __hip_atomic_fetch_add((LAS u32*)(L + L_FLAG + F_SYNC3), 1u, __ATOMIC_RELAXED, __HIP_MEMORY_SCOPE_WORKGROUP);
_Pragma("nounroll")
        for (u32 n = 0; n < SPIN_LDS; ++n) { if (lds_ld(L + L_FLAG + F_SYNC3) >= 4) break; }
        asm volatile("" ::: "memory");
      }
      STAMP(12);
      f32x4 a = {0.f, 0.f, 0.f, 0.f}, a2 = {0.f, 0.f, 0.f, 0.f};
      a = mfma32(*(LAS u32x4*)(L + L_O + (c & 7) * 256 + 16 * g), wof[0], a);
      a2 = mfma32(*(LAS u32x4*)(L + L_O + (c & 7) * 256 + 64 + 16 * g), wof[1], a2);
      a = mfma32(*(LAS u32x4*)(L + L_O + (c & 7) * 256 + 128 + 16 * g), wof[2], a);
      a2 = mfma32(*(LAS u32x4*)(L + L_O + (c & 7) * 256 + 192 + 16 * g), wof[3], a2);
      a += a2;
      asm volatile("" :: "v"(a));
      if (real && g < 2) {
#pragma unroll
        for (int i = 0; i < 4; ++i) st_gran(p.ws, WS_OUT + ((((4 * g + i) * 32 + 4 * wcc + w) * 48) + whead) * 128 + c * 8, __float_as_uint(a[i]), tag32);
      }
      STAMP(13);
    }
    if (wave == 0 || !real) {
      const float xres = bflo(p.x[(r >> 2) * DM + 16 * (4 * xg + (r & 3)) + c]);
      const u32 base = WS_OUT + (((r >> 2) * 32 + 4 * xg + (r & 3)) * 48) * 128 + c * 8;
      u64 t[12] = {0, 0, 0, 0, 0, 0, 0, 0, 0, 0, 0, 0};
      if (H2DLY && real) __builtin_amdgcn_s_sleep(H2DLY);
      {
        u32 have = SKIP_FFN ? 0xf00u : 0u;
_Pragma("nounroll")
        for (u32 n = 0; n < nspin; ++n) {
          u64 tt[12];
#pragma unroll
          for (int i = 0; i < 12; ++i) { const int pr = (4 * i + g < 46) ? 4 * i + g : 45; tt[i] = ld_gran(p.ws, ((have >> i) & 1) ? WS_DUMMY : base + pr * 128); }
#pragma unroll
          for (int i = 0; i < 12; ++i) {
            t[i] = ((have >> i) & 1) ? t[i] : tt[i];
            if (__all((u32)(tt[i] >> 32) == tag32)) have |= 1u << i;
          }
          if (have == 0xfffu) break;
          __builtin_amdgcn_s_sleep(POLLSLP);
        }
      }
      float sa = 0.f, sf = 0.f;
#pragma unroll
      for (int i = 0; i < 8; ++i) sa += __uint_as_float((u32)t[i]);
#pragma unroll
      for (int i = 8; i < 12; ++i) sf += (4 * i + g < 46) ? __uint_as_float((u32)t[i]) : 0.f;
      sa = xrow_sum(sa); sf = xrow_sum(sf);
      const float res = rbf(xres + rbf(sa)) + rbf(sf);
      const u32 rv = pk2(res, 0.f);
      asm volatile("" :: "v"(rv));
      if (real) {
        if (HOLD && blk == 0) { while ((int)(rt32() - (t0 + HOLD)) < 0) __builtin_amdgcn_s_sleep(2); }
        u32 rv2 = rv;
        if (REPORT && blk == 0 && lane == 0) {
          const u32 tt = REPORT == 1 ? rt32() - t0 : lds_ld(L + L_FLAG + 48);
          rv2 = pk2((float)tt * 0.01f, 0.f);
        }
        if (OUTSC1) {
          const u32 nb = __builtin_amdgcn_mov_dpp((int)rv2, 0xB1, 0xf, 0xf, true);
          if (g == 0 && !(c & 1)) __hip_atomic_store((GAS u32*)(p.out + (r >> 2) * DM + 16 * (4 * xg + (r & 3)) + c), (rv2 & 0xffffu) | (nb << 16), __ATOMIC_RELAXED, __HIP_MEMORY_SCOPE_AGENT);
        } else if (g == 0) p.out[(r >> 2) * DM + 16 * (4 * xg + (r & 3)) + c] = (u16)(rv2 & 0xffffu);
        if (blk == 0 && lane == 0) __hip_atomic_store((GAS u64*)(p.ws + WS_EPOCH), ep, __ATOMIC_RELAXED, __HIP_MEMORY_SCOPE_AGENT);
      }
      STAMP(10);
    }
  }
  STAMP_FLUSH();
}

__global__ void __launch_bounds__(64) pre_kernel() {
  const u32 t0 = rt32();
  while ((int)(rt32() - (t0 + K0CFG)) < 0) __builtin_amdgcn_s_sleep(2);
}
extern "C" void kernel_launch(void* const* d_in, const int* in_sizes, int n_in, void* d_out, int out_size, void* d_ws, size_t ws_size, hipStream_t stream) {
  Params p;
  p.x = (const u16*)d_in[0]; p.k = (const u16*)d_in[1]; p.v = (const u16*)d_in[2]; p.wq = (const u16*)d_in[3]; p.wo = (const u16*)d_in[4];
  p.wg = (const u16*)d_in[6]; p.wu = (const u16*)d_in[7]; p.wd = (const u16*)d_in[8];
  p.out = (u16*)d_out; p.ws = (unsigned char*)d_ws;
  hipLaunchKernelGGL(pre_kernel, dim3(1), dim3(64), 0, stream);
  hipLaunchKernelGGL(fused_decode_kernel, dim3(NWG), dim3(NTHR), 0, stream, p);
}
```

```cpp
#define ADAPT 0
#define DAHEAD 2
#define H1DLY 24
#define H2DLY 30
#define HOLD 6000
#define K3CFG 1
#define NCODE 20
#include <hip/hip_runtime.h>
#include <stdint.h>

#define GAS __attribute__((address_space(1)))
#define LAS __attribute__((address_space(3)))
typedef unsigned u32;
typedef unsigned long long u64;
typedef unsigned short u16;
typedef u32 u32x4 __attribute__((ext_vector_type(4)));
typedef u32 u32x2 __attribute__((ext_vector_type(2)));
typedef float f32x4 __attribute__((ext_vector_type(4)));
typedef float f32x2 __attribute__((ext_vector_type(2)));
typedef short s16x4 __attribute__((ext_vector_type(4)));
typedef __bf16 bf16x8 __attribute__((ext_vector_type(8)));
typedef __bf16 bf16x2 __attribute__((ext_vector_type(2)));

#define DIAG 0
#define PACE10 80
#define SENT 8
#define HPRIO 3
#define FAKEC 0
#define NOPOLL 0
#define STREAMONLY 0
#define TQ0 380
#define TQR 48
#define H1MODE 0
#define TG15 950
#define TG2 1350
#define TGS 800
#define DRY 0
#define H2MODE 0
#define ASTOP 57
#define ASLP 40
#define ABIAS 1000
#define ALEAD 15
#define FINE 0
#define KT 0
#define NROUND 2
#define TA0 1100
#define TAP 700
#define TAW 220
#define APDEF 56
#define ALAT 120
#define REPORT 0
#define OUTSC1 0
#define DPRO 1
#define LEADT 30
#define POLLSLP 4
#define SKIP_FFN 0
#define WNT 1

constexpr int NB = 8, DM = 512, SK = 8192, DH = 128, FF = 1792;
constexpr int NWG = 256, NTHR = 512;
#define RINGN 8
constexpr int RING = RINGN;
constexpr int NUNIT = 64;
constexpr float SC = 0.08838834764831845f * 1.4426950408889634f;
constexpr float THR_RAW = 8.0f / SC;

constexpr u32 WS_EPOCH = 0;
constexpr u32 WS_DUMMY = 2048;
constexpr u32 WS_START = 4096;
constexpr u32 WS_PROG = 8192;
constexpr u32 WS_Q = 65536;
constexpr u32 WS_GU = WS_Q + 8 * 8 * 4 * 512 * 8;
constexpr u32 WS_H = WS_GU + 28 * 2 * 4 * 8 * 64 * 8;
constexpr u32 WS_OP = WS_H + 8 * 1792 * 8;
constexpr u32 WS_ML = WS_OP + 32 * 8 * 4 * 64 * 8;
constexpr u32 WS_OUT = WS_ML + 32 * 8 * 4 * 2 * 8;
constexpr u32 WS_END = WS_OUT + 8 * 32 * 48 * 16 * 8;
constexpr u32 WS_DIAG = 8u << 20;
static_assert(WS_END <= WS_DIAG, "ws map");

constexpr u32 L_RING = 0;
constexpr u32 L_STAGE = 4 * RING * 4096;
constexpr u32 L_XN = L_STAGE + 16384;
constexpr u32 XN_STR = 1056;
constexpr u32 L_Q = L_XN + 8 * XN_STR;
constexpr u32 L_O = L_Q + 1024;
constexpr u32 L_FLAG = L_O + 2048;
constexpr u32 L_SCR = L_FLAG + 256;
constexpr u32 L_END = L_SCR + 4096;
static_assert(L_END <= 163840, "lds");
constexpr u32 F_SYNC = 0, F_Q = 4, F_SYNC2 = 8, F_WO = 12, F_EPOCH = 16, F_SYNC3 = 24, F_PROG = 28, F_PACE = 32;

constexpr u32 PER256 = (u32)(4096.0 * 1024.0 / ((PACE10 ? PACE10 : 1) * 0.1e12) * 1e8 * 256.0);
constexpr u32 SPIN_LDS = 1u << 22, SPIN_GLB = 1u << 17;

struct Params {
  const u16* x; const u16* k; const u16* v; const u16* wq; const u16* wo; const u16* wg; const u16* wu; const u16* wd;
  u16* out; unsigned char* ws;
};

__device__ __forceinline__ u32 rt32() { return (u32)__builtin_amdgcn_s_memrealtime(); }
__device__ __forceinline__ float bflo(u32 w) { return __uint_as_float(w << 16); }
__device__ __forceinline__ float bfhi(u32 w) { return __uint_as_float(w & 0xffff0000u); }
__device__ __forceinline__ u32 pk2(float lo, float hi) { f32x2 t = {lo, hi}; return __builtin_bit_cast(u32, __builtin_convertvector(t, bf16x2)); }
__device__ __forceinline__ float rbf(float f) { return bflo(pk2(f, f)); }
template <int CTRL> __device__ __forceinline__ float dpp(float x) {
  return __builtin_bit_cast(float, __builtin_amdgcn_mov_dpp(__builtin_bit_cast(int, x), CTRL, 0xf, 0xf, true));
}
__device__ __forceinline__ float xrow_max(float x) {
  auto s = __builtin_amdgcn_permlane16_swap(__float_as_uint(x), __float_as_uint(x), false, false);
  x = fmaxf(__uint_as_float(s[0]), __uint_as_float(s[1]));
  auto t = __builtin_amdgcn_permlane32_swap(__float_as_uint(x), __float_as_uint(x), false, false);
  return fmaxf(__uint_as_float(t[0]), __uint_as_float(t[1]));
}
__device__ __forceinline__ float xrow_sum(float x) {
  auto s = __builtin_amdgcn_permlane16_swap(__float_as_uint(x), __float_as_uint(x), false, false);
  x = __uint_as_float(s[0]) + __uint_as_float(s[1]);
  auto t = __builtin_amdgcn_permlane32_swap(__float_as_uint(x), __float_as_uint(x), false, false);
  return __uint_as_float(t[0]) + __uint_as_float(t[1]);
}
__device__ __forceinline__ float wave_sum(float x) {
  x += dpp<0xB1>(x); x += dpp<0x4E>(x); x += dpp<0x141>(x); x += dpp<0x128>(x);
  return xrow_sum(x);
}
__device__ __forceinline__ f32x4 mfma32(u32x4 a, u32x4 b, f32x4 c) {
  return __builtin_amdgcn_mfma_f32_16x16x32_bf16(__builtin_bit_cast(bf16x8, a), __builtin_bit_cast(bf16x8, b), c, 0, 0, 0);
}
__device__ __forceinline__ f32x4 mfma16(u32x2 a, u32x2 b, f32x4 c) {
  return __builtin_amdgcn_mfma_f32_16x16x16bf16_1k(__builtin_bit_cast(s16x4, a), __builtin_bit_cast(s16x4, b), c, 0, 0, 0);
}
__device__ __forceinline__ u32x2 trread(LAS unsigned char* p) {
  return __builtin_bit_cast(u32x2, __builtin_amdgcn_ds_read_tr16_b64_v4i16((LAS s16x4*)p));
}
__device__ __forceinline__ u32x4 trread2(LAS unsigned char* p0, LAS unsigned char* p1) {
  const u32x2 a = trread(p0), b = trread(p1); return u32x4{a.x, a.y, b.x, b.y};
}
#define DMA4(NT_)                                                                                                          \
  asm volatile("s_waitcnt lgkmcnt(0)\n\ts_mov_b32 %0, m0\n\ts_mov_b32 m0, %1\n\ts_nop 0\n\t"                              \
               "buffer_load_dwordx4 %2, %6, %7 offen " NT_ " lds\n\t"                                                      \
               "buffer_load_dwordx4 %3, %6, %7 offen offset:1024 " NT_ " lds\n\t"                                          \
               "buffer_load_dwordx4 %4, %6, %7 offen offset:2048 " NT_ " lds\n\t"                                          \
               "buffer_load_dwordx4 %5, %6, %7 offen offset:3072 " NT_ " lds\n\t"                                          \
               "s_mov_b32 m0, %0"                                                                                          \
               : "=&s"(keep) : "s"(ldst), "v"(v0), "v"(v1), "v"(v2), "v"(v3), "s"(srd), "s"(soff) : "memory")
#define KPOL "nt"
#define VPOL "nt"
__device__ __forceinline__ void dma4k(u32 ldst, u32 v0, u32 v1, u32 v2, u32 v3, __amdgpu_buffer_rsrc_t srd, u32 soff) {
  u32 keep; DMA4(KPOL);
}
__device__ __forceinline__ void dma4v(u32 ldst, u32 v0, u32 v1, u32 v2, u32 v3, __amdgpu_buffer_rsrc_t srd, u32 soff) {
  u32 keep; DMA4(VPOL);
}
__device__ __forceinline__ void dma1(u32 ldst, const void* gp) {
  u32 keep;
  asm volatile("s_mov_b32 %0, m0\n\ts_mov_b32 m0, %1\n\ts_nop 0\n\tglobal_load_lds_dwordx4 %2, off nt\n\ts_mov_b32 m0, %0"
               : "=&s"(keep) : "s"(ldst), "v"(gp) : "memory");
}
__device__ __forceinline__ void dma1p(u32 ldst, const void* gp) {
  u32 keep;
  asm volatile("s_mov_b32 %0, m0\n\ts_mov_b32 m0, %1\n\ts_nop 0\n\tglobal_load_lds_dwordx4 %2, off\n\ts_mov_b32 m0, %0"
               : "=&s"(keep) : "s"(ldst), "v"(gp) : "memory");
}
#define WNT_S " nt"
#define VMCNT(N) asm volatile("s_waitcnt vmcnt(" #N ")" ::: "memory")
#define LGKM0() asm volatile("s_waitcnt lgkmcnt(0)" ::: "memory")
__device__ __forceinline__ void wait_units(int n) {
  if (n > 11) { VMCNT(48); return; }
  switch (n) {
    case 0: VMCNT(0); break;   case 1: VMCNT(4); break;   case 2: VMCNT(8); break;   case 3: VMCNT(12); break;
    case 4: VMCNT(16); break;  case 5: VMCNT(20); break;  case 6: VMCNT(24); break;  case 7: VMCNT(28); break;
    case 8: VMCNT(32); break;  case 9: VMCNT(36); break;  case 10: VMCNT(40); break; default: VMCNT(44); break;
  }
}
__device__ __forceinline__ void bar() { LGKM0(); __builtin_amdgcn_s_barrier(); asm volatile("" ::: "memory"); }
__device__ __forceinline__ u32 lds_ld(LAS unsigned char* p) { return *(volatile LAS u32*)p; }
__device__ __forceinline__ void lds_wait_ge(LAS unsigned char* p, u32 want) {
_Pragma("nounroll")
  for (u32 n = 0; n < SPIN_LDS; ++n) { if (lds_ld(p) >= want) break; __builtin_amdgcn_s_sleep(1); }
  asm volatile("" ::: "memory");
}
__device__ __forceinline__ void st_gran(unsigned char* ws, u32 off, u32 lo, u32 hi) {
  __hip_atomic_store((GAS u64*)(ws + off), ((u64)hi << 32) | lo, __ATOMIC_RELAXED, __HIP_MEMORY_SCOPE_AGENT);
}
__device__ __forceinline__ u64 ld_gran(unsigned char* ws, u32 off) {
  return __hip_atomic_load((GAS u64*)(ws + off), __ATOMIC_RELAXED, __HIP_MEMORY_SCOPE_AGENT);
}
#define STAMP(i) do { } while (0)
#define STAMP_FLUSH() do { } while (0)

__global__ void __launch_bounds__(NTHR, 2) fused_decode_kernel(Params p) {
  __shared__ __attribute__((aligned(16))) unsigned char lds_raw[L_END];
  LAS unsigned char* L = (LAS unsigned char*)lds_raw;
  const u32 lbase = (u32)(size_t)L;
  const int tid = threadIdx.x, lane = tid & 63, wave = __builtin_amdgcn_readfirstlane(tid >> 6);
  const int g = lane >> 4, c = lane & 15;
  const int blk = blockIdx.x, xg = blk & 7, r = blk >> 3;
  const u32 t0 = rt32();
  u64 pc0; asm volatile("s_getpc_b64 %0" : "=s"(pc0));
  STAMP(0);
  const __amdgpu_buffer_rsrc_t wsr = __builtin_amdgcn_make_buffer_rsrc(p.ws, 0, WS_END, 0x00020000);
  const int ab = r >> 2, as = r & 3;
  const int whead = 4 * xg + (r & 3), wcc = r >> 2;

  if (wave < 4) {
    const size_t pbase = (size_t)(ab * 8 + xg) * SK * DH;
    const __amdgpu_buffer_rsrc_t ksr = __builtin_amdgcn_make_buffer_rsrc((void*)(p.k + pbase), 0, SK * DH * 2, 0x00020000);
    const __amdgpu_buffer_rsrc_t vsr = __builtin_amdgcn_make_buffer_rsrc((void*)(p.v + pbase), 0, SK * DH * 2, 0x00020000);
    const u32 vk0 = g * 256 + 16 * (c ^ g), vk1 = g * 256 + 16 * (c ^ (4 + g)), vk2 = g * 256 + 16 * (c ^ (8 + g)), vk3 = g * 256 + 16 * (c ^ (12 + g));
    const u32 vv0 = g * 256 + 16 * (c ^ (g << 1)), vv1 = g * 256 + 16 * (c ^ ((4 + g) << 1));
    const u32 ring = lbase + L_RING + wave * (RING * 4096);
    const u32 sbase = as * 16384 + wave * 4096;
    int nis = 0;
    int landed = 0;
    u32 pA = t0 - LEADT, pP = PER256, tnow = t0, tgt = 0;
    auto pace = [&]() {
      if (PACE10 && (int)(tnow - (pA + (((u32)nis * pP) >> 8))) < 0) {
        _Pragma("nounroll")
        for (u32 n = 0; n < SPIN_LDS; ++n) {
          tnow = rt32();
          if ((int)(tnow - (pA + (((u32)nis * pP) >> 8))) >= 0) break;
          __builtin_amdgcn_s_sleep(1);
        }
      }
    };
    auto issue = [&]() {
      pace();
      const int mi = nis < 2 * KT ? nis >> 1 : nis - KT;
      const u32 soff = (u32)(nis >> 1) * 65536u + sbase, ldst = ring + (u32)(mi & (RING - 1)) * 4096u;
      if (nis & 1) dma4v(ldst, vv0, vv1, vv0, vv1, vsr, soff); else dma4k(ldst, vk0, vk1, vk2, vk3, ksr, soff);
      ++nis;
    };
    const u32 kfo = c * 256 + g * 16;
    u32x4 kr[KT ? 4 * KT : 1];
#define KREG(T)                                                                                                          \
    if constexpr (T < KT) {                                                                                              \
      if (T > 0) { if (2 * T >= DPRO) wait_units(DPRO - 1); pace(); }                                                    \
      asm volatile("buffer_load_dwordx4 %0, %4, %5, %6 offen nt\n\tbuffer_load_dwordx4 %1, %4, %5, %6 offen offset:64 nt\n\t"   \
                   "buffer_load_dwordx4 %2, %4, %5, %6 offen offset:128 nt\n\tbuffer_load_dwordx4 %3, %4, %5, %6 offen offset:192 nt" \
                   : "=&v"(kr[(4 * T) % (4 * KT + !KT)]), "=&v"(kr[(4 * T + 1) % (4 * KT + !KT)]), "=&v"(kr[(4 * T + 2) % (4 * KT + !KT)]), "=&v"(kr[(4 * T + 3) % (4 * KT + !KT)]) \
                   : "v"(kfo), "s"(ksr), "s"((u32)(T * 65536u + sbase)) : "memory");                                     \
      ++nis;                                                                                                             \
      if (T == 0) bar();                                                           \
      if (2 * T + 1 >= DPRO) wait_units(DPRO - 1);                                                                       \
      issue();                                                                                                           \
    }
    if constexpr (KT == 0) { issue(); bar(); }
    KREG(0) KREG(1) KREG(2) KREG(3) KREG(4) KREG(5)
#undef KREG
    _Pragma("nounroll")
    while (nis < KT + RING) {
      if (nis >= DPRO) wait_units(DPRO - 1);
      if (lds_ld(L + L_FLAG + F_Q) >= 4) break;
      issue();
    }
    landed = nis > DPRO ? nis - DPRO : 0;
    auto refill = [&](int rcons) {
      while (nis - landed < DAHEAD && nis < NUNIT && (nis < 2 * KT ? nis >> 1 : nis - KT) < rcons + RING) issue();
    };
    STAMP(1);
    if (!STREAMONLY) lds_wait_ge(L + L_FLAG + F_Q, 4);
    STAMP(2);
    if (REPORT == 2 && wave == 0 && lane == 0) *(volatile LAS u32*)(L + L_FLAG + 48) = rt32() - t0;
    u32x4 qf[4];
#pragma unroll
    for (int s = 0; s < 4; ++s) qf[s] = *(LAS u32x4*)(L + L_Q + (c & 3) * 256 + 64 * s + 16 * g);
    refill(0);
    LAS unsigned char* R = L + L_RING + wave * (RING * 4096);
    const u32 ka = c * 256 + 16 * (g ^ c);
    const u32 va = (4 * g + (c >> 2)) * 256 + 32 * ((4 * g + (c >> 2)) & 7) + 8 * (c & 3);
    f32x4 pre[KT ? KT : 1];
    if constexpr (KT > 0) {
      wait_units(nis - (2 * KT - 1));
      if constexpr (KT == 4)
        asm volatile("" : "+v"(kr[0]), "+v"(kr[1]), "+v"(kr[2]), "+v"(kr[3]), "+v"(kr[4]), "+v"(kr[5]), "+v"(kr[6]), "+v"(kr[7]),
                          "+v"(kr[8]), "+v"(kr[9]), "+v"(kr[10]), "+v"(kr[11]), "+v"(kr[12]), "+v"(kr[13]), "+v"(kr[14]), "+v"(kr[15]) :: "memory");
      if constexpr (KT == 6) {
        asm volatile("" : "+v"(kr[0]), "+v"(kr[1]), "+v"(kr[2]), "+v"(kr[3]), "+v"(kr[4]), "+v"(kr[5]), "+v"(kr[6]), "+v"(kr[7]),
                          "+v"(kr[8]), "+v"(kr[9]), "+v"(kr[10]), "+v"(kr[11]) :: "memory");
        asm volatile("" : "+v"(kr[12 % (4 * KT)]), "+v"(kr[13 % (4 * KT)]), "+v"(kr[14 % (4 * KT)]), "+v"(kr[15 % (4 * KT)]), "+v"(kr[16 % (4 * KT)]), "+v"(kr[17 % (4 * KT)]), "+v"(kr[18 % (4 * KT)]), "+v"(kr[19 % (4 * KT)]),
                          "+v"(kr[20 % (4 * KT)]), "+v"(kr[21 % (4 * KT)]), "+v"(kr[22 % (4 * KT)]), "+v"(kr[23 % (4 * KT)]) :: "memory");
      }
      landed = landed > 2 * KT - 1 ? landed : 2 * KT - 1;
#pragma unroll
      for (int t = 0; t < KT; ++t) {
        f32x4 sa = {0.f, 0.f, 0.f, 0.f}, sb = {0.f, 0.f, 0.f, 0.f};
        sa = mfma32(kr[4 * t], qf[0], sa); sb = mfma32(kr[4 * t + 1], qf[1], sb); sa = mfma32(kr[4 * t + 2], qf[2], sa); sb = mfma32(kr[4 * t + 3], qf[3], sb);
        pre[t] = sa + sb;
      }
    }
    float m = -INFINITY, lsum = 0.f;
    f32x4 acc[8];
#pragma unroll
    for (int i = 0; i < 8; ++i) acc[i] = f32x4{0.f, 0.f, 0.f, 0.f};
    _Pragma("nounroll")
    for (int j = 0; j < NUNIT / 2; ++j) {
      f32x4 sc;
      const int rk = j < KT ? j : 2 * j - KT;
      if (j < KT) {
        sc = pre[0];
#pragma unroll
        for (int t = 0; t + 1 < KT; ++t) pre[t] = pre[t + 1];
      } else {
        wait_units(nis - 2 * j - 1);
        landed = landed > 2 * j + 1 ? landed : 2 * j + 1;
        refill(rk);
        LAS unsigned char* Kt = R + (rk & (RING - 1)) * 4096;
        f32x4 sa = {0.f, 0.f, 0.f, 0.f}, sb = {0.f, 0.f, 0.f, 0.f};
        const u32x4 k0 = *(LAS u32x4*)(Kt + ka), k1 = *(LAS u32x4*)(Kt + (ka ^ 64)), k2 = *(LAS u32x4*)(Kt + (ka ^ 128)), k3 = *(LAS u32x4*)(Kt + (ka ^ 192));
        sa = mfma32(k0, qf[0], sa); sb = mfma32(k1, qf[1], sb); sa = mfma32(k2, qf[2], sa); sb = mfma32(k3, qf[3], sb);
        sc = sa + sb;
      }
      if (ADAPT) {
        const u32 te = __builtin_amdgcn_readfirstlane(lds_ld(L + L_FLAG + F_PACE + 8));
        tnow = rt32();
        if (te != tgt && nis < NUNIT - 1) {
          tgt = te;
          const int span = (int)(te - tnow);
          u32 np = span > 0 ? __builtin_amdgcn_readfirstlane((u32)((float)span * 256.f * __builtin_amdgcn_rcpf((float)(NUNIT - 1 - nis + 1)))) : PER256;
          np = np < PER256 ? PER256 : np;
          pP = np; pA = te - (((u32)(NUNIT - 1) * np) >> 8);
        }
        if (wave == 0) *(volatile LAS u32*)(L + L_FLAG + F_PROG) = (tnow << 8) | (u32)(2 * j + 1);
      }
      if (REPORT == 4 && j == 10 && wave == 0 && lane == 0) *(volatile LAS u32*)(L + L_FLAG + 48) = rt32() - t0;
      if (REPORT == 5 && j == 20 && wave == 0 && lane == 0) *(volatile LAS u32*)(L + L_FLAG + 48) = rt32() - t0;
      const int rv = j < KT ? j : rk + 1;
      LAS unsigned char* Vt = R + (rv & (RING - 1)) * 4096;
      const float tmax = fmaxf(fmaxf(sc[0], sc[1]), fmaxf(sc[2], sc[3]));
      if (__builtin_amdgcn_ballot_w64(tmax > m + THR_RAW) != 0ull) {
        const float mn = fmaxf(m, xrow_max(tmax));
        const float al = __builtin_amdgcn_exp2f((m - mn) * SC);
        m = mn; lsum *= al;
        const float a0 = dpp<0x00>(al), a1 = dpp<0x55>(al), a2 = dpp<0xAA>(al), a3 = dpp<0xFF>(al);
#pragma unroll
        for (int i = 0; i < 8; ++i) { acc[i][0] *= a0; acc[i][1] *= a1; acc[i][2] *= a2; acc[i][3] *= a3; }
      }
      const float mc = m * SC;
      const float p0 = __builtin_amdgcn_exp2f(fmaf(sc[0], SC, -mc)), p1 = __builtin_amdgcn_exp2f(fmaf(sc[1], SC, -mc));
      const float p2 = __builtin_amdgcn_exp2f(fmaf(sc[2], SC, -mc)), p3 = __builtin_amdgcn_exp2f(fmaf(sc[3], SC, -mc));
      lsum += (p0 + p1) + (p2 + p3);
      const u32x2 pa = {pk2(p0, p1), pk2(p2, p3)};
      wait_units(nis - 2 * j - 2);
      landed = landed > 2 * j + 2 ? landed : 2 * j + 2;
      refill(rv);
#pragma unroll
      for (int i = 0; i < 8; ++i) acc[i] = mfma16(pa, trread(Vt + (va ^ (32 * i))), acc[i]);
      refill(rv + 1);
    }
    STAMP(3);
    if (REPORT == 3 && wave == 0 && lane == 0) *(volatile LAS u32*)(L + L_FLAG + 48) = rt32() - t0;
    lsum = xrow_sum(lsum);
    if (g == 0) {
#pragma unroll
      for (int i = 0; i < 8; ++i) *(LAS f32x4*)(R + i * 256 + c * 16) = acc[i];
      if (c < 4) *(LAS f32x2*)(R + 2048 + c * 8) = f32x2{m * SC, lsum};
    }
  } else {
    const int hw = wave - 4;
    if (STREAMONLY) { bar(); bar(); STAMP_FLUSH(); return; }
    if (HPRIO) __builtin_amdgcn_s_setprio(HPRIO);
    u32x4 xa, xb; u32x2 ev;
    if (hw == 0 && lane < 16) {
      constexpr u32 per256 = (u32)(4096.0 * 1024.0 / ((PACE10 ? PACE10 : 1) * 0.1e12) * 1e8 * 256.0);
      *(LAS u32x4*)(L + L_FLAG + lane * 16) = lane == 2 ? u32x4{t0 - LEADT, per256, 0, 0} : u32x4{0, 0, 0, 0};
    }
    const int kq = r & 3;
    if (NCODE && r * 4 + hw < NCODE) {
      dma1p(lbase + L_SCR + hw * 1024, (const char*)(pc0 & ~255ull) + (r * 4 + hw) * 1024 + lane * 16);
    }
    {
      const u16* xp = p.x + (2 * hw) * DM + lane * 8;
      const u16* wp = p.wq + (size_t)(kq * 128 + hw * 32 + (lane >> 3)) * 4096 + xg * 512 + (r >> 2) * 64 + (lane & 7) * 8;
      const u32 ldst = lbase + L_STAGE + hw * 4096;
      u32 keep;
      asm volatile("global_load_dwordx4 %0, %4, off\n\tglobal_load_dwordx4 %1, %4, off offset:1024\n\tglobal_load_dwordx2 %2, %5, off\n\t"
                   "s_mov_b32 %3, m0\n\ts_mov_b32 m0, %10\n\ts_nop 0\n\t"
                   "global_load_lds_dwordx4 %6, off" WNT_S "\n\ts_add_u32 m0, m0, 0x400\n\ts_nop 0\n\t"
                   "global_load_lds_dwordx4 %7, off" WNT_S "\n\ts_add_u32 m0, m0, 0x400\n\ts_nop 0\n\t"
                   "global_load_lds_dwordx4 %8, off" WNT_S "\n\ts_add_u32 m0, m0, 0x400\n\ts_nop 0\n\t"
                   "global_load_lds_dwordx4 %9, off" WNT_S "\n\ts_mov_b32 m0, %3\n\t"
                   "s_waitcnt lgkmcnt(0)\n\ts_barrier\n\ts_waitcnt vmcnt(4)"
                   : "=&v"(xa), "=&v"(xb), "=&v"(ev), "=&s"(keep)
                   : "v"(xp), "v"(p.ws + WS_EPOCH), "v"(wp), "v"(wp + (size_t)8 * 4096), "v"(wp + (size_t)16 * 4096), "v"(wp + (size_t)24 * 4096), "s"(ldst)
                   : "memory", "scc");
    }
    const u64 ep = (((u64)ev.y << 32) | ev.x) + 1ull;
    const u32 tag32 = (u32)ep, t48lo = ((u32)ep) << 16, t48hi = (u32)(ep >> 16), t40lo = ((u32)ep) << 24, t40hi = (u32)(ep >> 8);
    STAMP(1);
#pragma unroll
    for (int rr = 0; rr < 2; ++rr) {
      const u32x4 xv = rr ? xb : xa;
      float v[8];
#pragma unroll
      for (int i = 0; i < 4; ++i) { v[2 * i] = bflo(xv[i]); v[2 * i + 1] = bfhi(xv[i]); }
      const float mean = wave_sum(((v[0] + v[1]) + (v[2] + v[3])) + ((v[4] + v[5]) + (v[6] + v[7]))) * (1.f / DM);
      float q2 = 0.f;
#pragma unroll
      for (int i = 0; i < 8; ++i) { const float d = v[i] - mean; q2 = fmaf(d, d, q2); }
      const float var = rbf(wave_sum(q2) * (1.f / DM));
      const float rden = __builtin_amdgcn_rcpf(rbf(__builtin_amdgcn_sqrtf(rbf(var + 1e-5f))));
      const float mb = rbf(mean);
      u32x4 o;
#pragma unroll
      for (int i = 0; i < 4; ++i) { const u32 d = pk2(v[2 * i] - mb, v[2 * i + 1] - mb); o[i] = pk2(bflo(d) * rden, bfhi(d) * rden); }
      *(LAS u32x4*)(L + L_XN + (2 * hw + rr) * XN_STR + lane * 16) = o;
    }
    u32 nsync = 0;
    auto hsync = [&]() {
      asm volatile("s_waitcnt vmcnt(0) lgkmcnt(0)" ::: "memory");
      if (lane == 0) __hip_atomic_fetch_add((LAS u32*)(L + L_FLAG + F_SYNC), 1u, __ATOMIC_RELAXED, __HIP_MEMORY_SCOPE_WORKGROUP);
      nsync += 4;
      lds_wait_ge(L + L_FLAG + F_SYNC, nsync);
    };
    const int tq = c >> 2, tp = c & 3;
    auto tile_mma = [&](const u32x4 (&act)[4], int cg) {
      f32x4 a0 = {0.f, 0.f, 0.f, 0.f}, a1 = {0.f, 0.f, 0.f, 0.f};
      LAS unsigned char* ap = L + L_STAGE + (8 * g + tq) * 128 + 32 * cg + 8 * tp;
      a0 = mfma32(act[0], trread2(ap, ap + 512), a0);
      a1 = mfma32(act[1], trread2(ap + 4096, ap + 4096 + 512), a1);
      a0 = mfma32(act[2], trread2(ap + 8192, ap + 8192 + 512), a0);
      a1 = mfma32(act[3], trread2(ap + 12288, ap + 12288 + 512), a1);
      return a0 + a1;
    };
    auto xn_frags = [&](u32x4 (&act)[4], int k0) {
#pragma unroll
      for (int s = 0; s < 4; ++s) act[s] = *(LAS u32x4*)(L + L_XN + (c & 7) * XN_STR + 2 * k0 + 64 * s + 16 * g);
    };
    auto f24 = [&](float f) { const u32 u = __float_as_uint(f); return t40lo | ((u + 0x7fu + ((u >> 8) & 1u)) >> 8); };
    auto ok40 = [&](const u32x4& t) { return ((t[0] ^ t40lo) >> 24) == 0 && t[1] == t40hi && ((t[2] ^ t40lo) >> 24) == 0 && t[3] == t40hi; };
    auto ok48 = [&](const u32x4& t) { return ((t[0] ^ t48lo) >> 16) == 0 && t[1] == t48hi && ((t[2] ^ t48lo) >> 16) == 0 && t[3] == t48hi; };
    u32x4 act[4];
    LGKM0();
    STAMP(11);
    hsync();
    STAMP(2);
    xn_frags(act, kq * 128);
    {
      const f32x4 d = tile_mma(act, hw);
      if (g < 2) {
#pragma unroll
        for (int i = 0; i < 4; ++i) st_gran(p.ws, WS_Q + ((((xg * 8 + 4 * g + i) * 4 + kq) * 512) + (r >> 2) * 64 + 16 * hw + c) * 8, f24(d[i]), t40hi);
      }
    }
    STAMP(3);
    if (hw < 2) {
      LGKM0();
      if (lane == 0) __hip_atomic_fetch_add((LAS u32*)(L + L_FLAG + F_SYNC), 1u, __ATOMIC_RELAXED, __HIP_MEMORY_SCOPE_WORKGROUP);
    }
    if (hw < 2) {
      const u32 qb = WS_Q + ((xg * 8 + ab) * 4) * 512 * 8;
      {
        const u32 due = t0 + TQ0 - (u32)(r * TQR) / 16;
        while ((int)(rt32() - due) < 0) __builtin_amdgcn_s_sleep(2);
      }
      float q0[2], q1[2];
_Pragma("nounroll")
      for (u32 n = 0; n < SPIN_GLB; ++n) {
        bool ok = true;
#pragma unroll
        for (int h = 0; h < 2; ++h) {
          q0[h] = 0.f; q1[h] = 0.f;
#pragma unroll
          for (int i = 0; i < 4; ++i) {
            const u32x4 t = __builtin_amdgcn_raw_buffer_load_b128(wsr, qb + (i * 512 + (2 * hw + h) * 128 + 2 * lane) * 8, 0, 16);
            ok &= ok40(t); q0[h] += __uint_as_float(t[0] << 8); q1[h] += __uint_as_float(t[2] << 8);
          }
        }
        if (__all(ok)) break;
        __builtin_amdgcn_s_sleep(POLLSLP);
        asm volatile("" ::: "memory");
      }
      *(LAS u32*)(L + L_Q + (2 * hw) * 256 + lane * 4) = pk2(q0[0], q1[0]);
      *(LAS u32*)(L + L_Q + (2 * hw + 1) * 256 + lane * 4) = pk2(q0[1], q1[1]);
      LGKM0();
      if (lane == 0) __hip_atomic_fetch_add((LAS u32*)(L + L_FLAG + F_Q), 2u, __ATOMIC_RELAXED, __HIP_MEMORY_SCOPE_WORKGROUP);
      STAMP(4);
      if (hw == 1) {
        { const u32 due = t0 + TGS; while ((int)(rt32() - due) < 0) __builtin_amdgcn_s_sleep(32); }
_Pragma("nounroll")
        for (u32 n = 0; n < SPIN_GLB; ++n) {
          const u32x4 a = __builtin_amdgcn_raw_buffer_load_b128(wsr, WS_START + lane * 16, 0, 16);
          const u32x4 b = __builtin_amdgcn_raw_buffer_load_b128(wsr, WS_START + 1024 + lane * 16, 0, 16);
          const u32 e0 = (u32)ep, e1 = (u32)(ep >> 32);
          const bool ok = a[0] == e0 && a[1] == e1 && a[2] == e0 && a[3] == e1 && b[0] == e0 && b[1] == e1 && b[2] == e0 && b[3] == e1;
          if (__all(ok)) break;
          __builtin_amdgcn_s_sleep(32);
          asm volatile("" ::: "memory");
        }
        STAMP(7);
        if (ADAPT) {
          float gnp[4] = {0.f, 0.f, 0.f, 0.f}, gtp[4] = {0.f, 0.f, 0.f, 0.f};
          _Pragma("nounroll")
          for (int round = 0; round < NROUND; ++round) {
            { const u32 due = t0 + TA0 + round * TAP - (u32)(r * TQR) / 16; while ((int)(rt32() - due) < 0) __builtin_amdgcn_s_sleep(8); }
            const u32 pv = lds_ld(L + L_FLAG + F_PROG);
            if (lane == 0) st_gran(p.ws, WS_PROG + (round * 256 + blk) * 8, t40lo | ((pv >> 8 & 0x1ffffu) << 7) | (pv & 127u), t40hi);
            { const u32 due = t0 + TA0 + TAW + round * TAP - (u32)(r * TQR) / 16; while ((int)(rt32() - due) < 0) __builtin_amdgcn_s_sleep(8); }
            u32x4 a, b;
            bool okk = false;
            _Pragma("nounroll")
            for (int n = 0; n < 3; ++n) {
              a = __builtin_amdgcn_raw_buffer_load_b128(wsr, WS_PROG + round * 2048 + lane * 32, 0, 16);
              b = __builtin_amdgcn_raw_buffer_load_b128(wsr, WS_PROG + round * 2048 + lane * 32 + 16, 0, 16);
              okk = __all(ok40(a) && ok40(b));
              if (okk) break;
              __builtin_amdgcn_s_sleep(32);
              asm volatile("" ::: "memory");
            }
            if (!okk) continue;
            const u32 now = rt32();
            const u32 w4[4] = {a[0], a[2], b[0], b[2]};
            float gn[4], gt[4];
#pragma unroll
            for (int i = 0; i < 4; ++i) {
              float n_ = (float)(w4[i] & 127u), t_ = (float)(((int)(((w4[i] >> 7) - now) << 15)) >> 15);
              n_ += dpp<0x122>(n_); n_ += dpp<0x124>(n_); n_ += dpp<0x128>(n_); gn[i] = xrow_sum(n_) * (1.f / 32.f);
              t_ += dpp<0x122>(t_); t_ += dpp<0x124>(t_); t_ += dpp<0x128>(t_); gt[i] = xrow_sum(t_) * (1.f / 32.f) + (float)(int)(now - t0);
            }
            float sn = (gn[0] + gn[1]) + (gn[2] + gn[3]), st = (gt[0] + gt[1]) + (gt[2] + gt[3]);
            sn = (sn + dpp<0xB1>(sn)) * 0.125f; st = (st + dpp<0xB1>(st)) * 0.125f;
            const float tn = (float)(int)(now - t0);
            float P = 0.f, pm = 0.f;
            if (round > 0 && gtp[0] != 0.f && sn - gnp[0] > 2.f) {
              P = (st - gtp[0]) * __builtin_amdgcn_rcpf(sn - gnp[0]);
              pm = st + (64.f - sn) * P * (ABIAS * 0.001f) - (float)ALAT;
              if (lane == 0 && ADAPT == 1) *(volatile LAS u32*)(L + L_FLAG + F_PACE + 8) = (t0 + (u32)(int)pm) | 1u;
            }
            gnp[0] = sn; gtp[0] = st;
          }
        }
      }
    } else {
      const int h2 = hw - 2;
      u32 nsync2 = 0;
      auto arrive2 = [&]() {
        LGKM0();
        if (lane == 0) __hip_atomic_fetch_add((LAS u32*)(L + L_FLAG + F_SYNC2), 1u, __ATOMIC_RELAXED, __HIP_MEMORY_SCOPE_WORKGROUP);
        nsync2 += 2;
        lds_wait_ge(L + L_FLAG + F_SYNC2, nsync2);
      };
      auto stage2 = [&](const u16* w, int ld, const u16* wbase = nullptr, int tidx = 0) {
        if (FAKEC && wbase) { w = wbase + (size_t)tidx * 8192; ld = 64; }
        const u16* wp = w + (size_t)(h2 * 64 + (lane >> 3)) * ld + (lane & 7) * 8;
#pragma unroll
        for (int i = 0; i < 8; ++i) dma1(lbase + L_STAGE + h2 * 8192 + i * 1024, wp + (size_t)i * 8 * ld);
      };
      LGKM0();
      if (lane == 0) __hip_atomic_fetch_add((LAS u32*)(L + L_FLAG + F_SYNC), 1u, __ATOMIC_RELAXED, __HIP_MEMORY_SCOPE_WORKGROUP);
      if (h2 == 1) {
        const u32x4 z = {0u, ~tag32, 0u, ~tag32};
#pragma unroll
        for (int i = 0; i < 2; ++i)
          __builtin_amdgcn_raw_buffer_store_b128(z, wsr, WS_OP + ((((xg * 4 + 2 * i + (lane >> 5)) * 8 + ab) * 4 + as) * 64) * 8 + (lane & 31) * 16, 0, 16);
        if (lane < 4) __builtin_amdgcn_raw_buffer_store_b128(z, wsr, WS_ML + (((xg * 4 + lane) * 8 + ab) * 4 + as) * 16, 0, 16);
#pragma unroll
        for (int i = 0; i < 4; ++i) {
          const int pc = 8 * i + (lane >> 3), b = pc >> 2, w = pc & 3;
          __builtin_amdgcn_raw_buffer_store_b128(z, wsr, WS_OUT + (((b * 32 + 4 * wcc + w) * 48) + whead) * 128 + (lane & 7) * 16, 0, 16);
          if (r >= 18) __builtin_amdgcn_raw_buffer_store_b128(z, wsr, WS_OUT + (((b * 32 + 4 * xg + w) * 48) + 32 + (r - 18)) * 128 + (lane & 7) * 16, 0, 16);
        }
      }
      lds_wait_ge(L + L_FLAG + F_SYNC, 8);
      const bool f1 = r < 14 && !SKIP_FFN, f15 = r >= 14 && r < 18 && !SKIP_FFN, f2 = r >= 18 && !SKIP_FFN;
      const int t1 = xg * 14 + r, sl1 = t1 >> 2, fq = t1 & 3, ks = r - 18;
      const u16* wo_tile = p.wo + (size_t)whead * 128 * DM + wcc * 64;
      if (f1) stage2(p.wg + (size_t)fq * 128 * FF + sl1 * 64, FF, p.wg, t1);
      else if (f2) stage2(p.wd + (size_t)ks * 128 * DM + xg * 64, DM, p.wd, ks * 8 + xg);
      else stage2(wo_tile, DM, p.wo, r * 8 + xg);
      VMCNT(0);
      if (h2 == 1 && lane == 0) {
        __hip_atomic_store((GAS u64*)(p.ws + WS_START + blk * 8), ep, __ATOMIC_RELAXED, __HIP_MEMORY_SCOPE_AGENT);
        *(volatile LAS u64*)(L + L_FLAG + F_EPOCH) = ep;
      }
      arrive2();
      if (f1) {
        xn_frags(act, fq * 128);
        f32x4 d[2];
        d[0] = tile_mma(act, 2 * h2); d[1] = tile_mma(act, 2 * h2 + 1);
        asm volatile("" : "+v"(d[0]), "+v"(d[1]));
        arrive2();
        stage2(p.wu + (size_t)fq * 128 * FF + sl1 * 64, FF, p.wu, t1);
#pragma unroll
        for (int w = 0; w < 2; ++w) if (g < 2) {
#pragma unroll
          for (int i = 0; i < 4; ++i) st_gran(p.ws, WS_GU + ((((sl1 * 2 + 0) * 4 + fq) * 8 + 4 * g + i) * 64 + 16 * (2 * h2 + w) + c) * 8, f24(d[w][i]), t40hi);
        }
        VMCNT(8);
        arrive2();
        d[0] = tile_mma(act, 2 * h2); d[1] = tile_mma(act, 2 * h2 + 1);
        asm volatile("" : "+v"(d[0]), "+v"(d[1]));
        arrive2();
        stage2(wo_tile, DM, p.wo, r * 8 + xg);
#pragma unroll
        for (int w = 0; w < 2; ++w) if (g < 2) {
#pragma unroll
          for (int i = 0; i < 4; ++i) st_gran(p.ws, WS_GU + ((((sl1 * 2 + 1) * 4 + fq) * 8 + 4 * g + i) * 64 + 16 * (2 * h2 + w) + c) * 8, f24(d[w][i]), t40hi);
        }
        VMCNT(8);
        arrive2();
      } else if (f15) {
        const int sl = xg * 4 + (r - 14);
        if (sl < 28 && !NOPOLL) {
          { const u32 due = t0 + TG15; while ((int)(rt32() - due) < 0) __builtin_amdgcn_s_sleep(16); }
          {
            const u32 so = WS_GU + ((((sl * 2 + 1) * 4 + (lane & 3)) * 8) * 64 + 63) * 8;
_Pragma("nounroll")
            for (u32 n = 0; n < SPIN_GLB; ++n) {
              const u64 t = ld_gran(p.ws, so);
              if (__all((((u32)t ^ t40lo) >> 24) == 0 && (u32)(t >> 32) == t40hi)) break;
              __builtin_amdgcn_s_sleep(16);
            }
          }
          float gs[2][2], us[2][2];
          const int cp = lane & 31;
_Pragma("nounroll")
          for (u32 n = 0; n < SPIN_GLB; ++n) {
            bool ok = true;
#pragma unroll
            for (int bb = 0; bb < 2; ++bb) {
              const int b = 4 * h2 + 2 * bb + (lane >> 5);
              gs[bb][0] = gs[bb][1] = us[bb][0] = us[bb][1] = 0.f;
#pragma unroll
              for (int i = 0; i < 4; ++i) {
                const u32x4 tg = __builtin_amdgcn_raw_buffer_load_b128(wsr, WS_GU + ((((sl * 2 + 0) * 4 + i) * 8 + b) * 64 + 2 * cp) * 8, 0, 16);
                const u32x4 tu = __builtin_amdgcn_raw_buffer_load_b128(wsr, WS_GU + ((((sl * 2 + 1) * 4 + i) * 8 + b) * 64 + 2 * cp) * 8, 0, 16);
                ok &= ok40(tg) && ok40(tu);
                gs[bb][0] += __uint_as_float(tg[0] << 8); gs[bb][1] += __uint_as_float(tg[2] << 8);
                us[bb][0] += __uint_as_float(tu[0] << 8); us[bb][1] += __uint_as_float(tu[2] << 8);
              }
            }
            if (__all(ok)) break;
            __builtin_amdgcn_s_sleep(16);
            asm volatile("" ::: "memory");
          }
#pragma unroll
          for (int bb = 0; bb < 2; ++bb) {
            const int b = 4 * h2 + 2 * bb + (lane >> 5);
            const u32 hp = pk2(rbf(gs[bb][0]) * rbf(us[bb][0]), rbf(gs[bb][1]) * rbf(us[bb][1]));
            st_gran(p.ws, WS_H + (b * FF + sl * 64 + 2 * cp) * 8, t48lo | (hp & 0xffffu), t48hi);
            st_gran(p.ws, WS_H + (b * FF + sl * 64 + 2 * cp + 1) * 8, t48lo | (hp >> 16), t48hi);
          }
        }
      } else if (f2) {
        { const u32 due = t0 + TG2; while ((int)(rt32() - due) < 0) __builtin_amdgcn_s_sleep(16); }
        {
          const u32 so = WS_H + (7 * FF + ks * 128 + 64 * (lane & 1) + 63) * 8;
_Pragma("nounroll")
          for (u32 n = 0; n < (NOPOLL ? 0 : SPIN_GLB); ++n) {
            const u64 t = ld_gran(p.ws, so);
            if (__all((((u32)t ^ t48lo) >> 16) == 0 && (u32)(t >> 32) == t48hi)) break;
            __builtin_amdgcn_s_sleep(32);
          }
        }
        const int b = lane >> 3, k0 = 64 * h2 + 8 * (lane & 7);
        u32x4 hv = {0, 0, 0, 0};
_Pragma("nounroll")
        for (u32 n = 0; n < (NOPOLL ? 0 : SPIN_GLB); ++n) {
          bool ok = true;
#pragma unroll
          for (int i = 0; i < 4; ++i) {
            const u32x4 t = __builtin_amdgcn_raw_buffer_load_b128(wsr, WS_H + (b * FF + ks * 128 + k0 + 2 * i) * 8, 0, 16);
            ok &= ok48(t);
            hv[i] = (t[0] & 0xffffu) | (t[2] << 16);
          }
          if (__all(ok)) break;
          __builtin_amdgcn_s_sleep(16);
          asm volatile("" ::: "memory");
        }
        *(LAS u32x4*)(L + L_O + b * 256 + k0 * 2) = hv;
        arrive2();
#pragma unroll
        for (int s = 0; s < 4; ++s) act[s] = *(LAS u32x4*)(L + L_O + (c & 7) * 256 + 64 * s + 16 * g);
        f32x4 d[2];
        d[0] = tile_mma(act, 2 * h2); d[1] = tile_mma(act, 2 * h2 + 1);
        asm volatile("" : "+v"(d[0]), "+v"(d[1]));
        arrive2();
        stage2(wo_tile, DM, p.wo, r * 8 + xg);
#pragma unroll
        for (int w = 0; w < 2; ++w) if (g < 2) {
#pragma unroll
          for (int i = 0; i < 4; ++i) st_gran(p.ws, WS_OUT + ((((4 * g + i) * 32 + 4 * xg + 2 * h2 + w) * 48) + 32 + ks) * 128 + c * 8, __float_as_uint(d[w][i]), tag32);
        }
        VMCNT(8);
        arrive2();
      }
      STAMP(5);
      if (h2 == 0 && lane == 0) *(volatile LAS u32*)(L + L_FLAG + F_WO) = 1;
      STAMP(6);
    }
_Pragma("nounroll")
    for (u32 n = 0; n < SPIN_LDS; ++n) { if (lds_ld(L + L_FLAG + F_WO) >= 1) break; __builtin_amdgcn_s_sleep(16); }
    asm volatile("" ::: "memory");
  }

  u32x4 wof[4];
  if (wave >= 4) {
    const int tq = c >> 2, tp = c & 3, w = wave - 4;
#pragma unroll
    for (int s = 0; s < 4; ++s) { LAS unsigned char* ap = L + L_STAGE + (32 * s + 8 * g + tq) * 128 + 32 * w + 8 * tp; wof[s] = trread2(ap, ap + 512); }
  }
  int pass0 = (DRY && wave >= 4) ? 0 : 1;
  asm volatile("" : "+v"(pass0));
#pragma nounroll
  for (int pass = __builtin_amdgcn_readfirstlane(pass0); pass < 2; ++pass) {
    const bool real = pass != 0;
    if (real) bar();
    STAMP(8);
    if (STREAMONLY) { STAMP(9); STAMP(10); if (wave < 4 && lane == 0) p.out[blk * 4 + wave] = *(LAS u16*)(L + L_RING + wave * (RING * 4096) + 8); STAMP_FLUSH(); return; }
    const u64 ep = *(volatile LAS u64*)(L + L_FLAG + F_EPOCH);
    const u32 tag32 = (u32)ep;
    const u32 nspin = real ? SPIN_GLB : 1u;
    if (wave < 4 || !real) {
      const int hh = wave & 3, ch = lane >> 3, c0 = (2 * lane) & 15;
      float mw[4], lw[4], v0[4], v1[4];
#pragma unroll
      for (int w = 0; w < 4; ++w) {
        LAS unsigned char* R = L + L_RING + w * (RING * 4096);
        const f32x2 ml = *(LAS f32x2*)(R + 2048 + hh * 8);
        mw[w] = ml[0]; lw[w] = ml[1];
        v0[w] = *(LAS float*)(R + ch * 256 + c0 * 16 + hh * 4);
        v1[w] = *(LAS float*)(R + ch * 256 + c0 * 16 + 16 + hh * 4);
      }
      const float M = fmaxf(fmaxf(mw[0], mw[1]), fmaxf(mw[2], mw[3]));
      float Ls = 0.f, o0 = 0.f, o1 = 0.f;
#pragma unroll
      for (int w = 0; w < 4; ++w) { const float f = __builtin_amdgcn_exp2f(mw[w] - M); Ls = fmaf(f, lw[w], Ls); o0 = fmaf(f, v0[w], o0); o1 = fmaf(f, v1[w], o1); }
      const float inv = __builtin_amdgcn_rcpf(Ls);
      const u32 slot = ((xg * 4 + hh) * 8 + ab) * 4 + as;
      const u32 ov = pk2(o0 * inv, o1 * inv);
      asm volatile("" :: "v"(ov), "v"(Ls), "v"(M));
      if (real) {
        st_gran(p.ws, WS_OP + (slot * 64 + lane) * 8, ov, tag32);
        if (lane < 2) st_gran(p.ws, WS_ML + (slot * 2 + lane) * 8, __float_as_uint(lane ? Ls : M), tag32);
      }
      STAMP(11);
    }
    if (wave >= 4) {
      const int w = wave - 4;
      const u32 slot = (whead * 8 + 2 * w) * 4;
      u64 o[8] = {0, 0, 0, 0, 0, 0, 0, 0}, ml = 0;
      if (H1DLY && real) __builtin_amdgcn_s_sleep(H1DLY);
      {
        u32 have = 0;
_Pragma("nounroll")
        for (u32 n = 0; n < nspin; ++n) {
          u64 t[9];
#pragma unroll
          for (int s = 0; s < 8; ++s) t[s] = ld_gran(p.ws, ((have >> s) & 1) ? WS_DUMMY : WS_OP + ((slot + s) * 64 + lane) * 8);
          t[8] = ld_gran(p.ws, ((have >> 8) & 1) ? WS_DUMMY : WS_ML + (slot * 2 + (lane & 15)) * 8);
#pragma unroll
          for (int s = 0; s < 9; ++s) {
            const bool fresh = !((have >> s) & 1);
            if (s < 8) o[s] = fresh ? t[s] : o[s]; else ml = fresh ? t[8] : ml;
            if (__all((u32)(t[s] >> 32) == tag32)) have |= 1u << s;
          }
          if (have == 0x1ffu) break;
          __builtin_amdgcn_s_sleep(POLLSLP);
        }
      }
      const u32 mlv = (u32)ml;
#pragma unroll
      for (int bb = 0; bb < 2; ++bb) {
        float ms[4], ls[4];
#pragma unroll
        for (int s = 0; s < 4; ++s) { ms[s] = __uint_as_float(__builtin_amdgcn_readlane(mlv, 8 * bb + 2 * s)); ls[s] = __uint_as_float(__builtin_amdgcn_readlane(mlv, 8 * bb + 2 * s + 1)); }
        const float M = fmaxf(fmaxf(ms[0], ms[1]), fmaxf(ms[2], ms[3]));
        float W = 0.f, o0 = 0.f, o1 = 0.f;
#pragma unroll
        for (int s = 0; s < 4; ++s) { const float wt = ls[s] * __builtin_amdgcn_exp2f(ms[s] - M); W += wt; o0 = fmaf(wt, bflo((u32)o[4 * bb + s]), o0); o1 = fmaf(wt, bfhi((u32)o[4 * bb + s]), o1); }
        const float inv = __builtin_amdgcn_rcpf(W);
        *(LAS u32*)(L + L_O + (2 * w + bb) * 256 + lane * 4) = pk2(o0 * inv, o1 * inv);
      }
      STAMP(9);
      LGKM0();
      if (real) {
        if (lane == 0) __hip_atomic_fetch_add((LAS u32*)(L + L_FLAG + F_SYNC3), 1u, __ATOMIC_RELAXED, __HIP_MEMORY_SCOPE_WORKGROUP);
_Pragma("nounroll")
        for (u32 n = 0; n < SPIN_LDS; ++n) { if (lds_ld(L + L_FLAG + F_SYNC3) >= 4) break; }
        asm volatile("" ::: "memory");
      }
      STAMP(12);
      f32x4 a = {0.f, 0.f, 0.f, 0.f}, a2 = {0.f, 0.f, 0.f, 0.f};
      a = mfma32(*(LAS u32x4*)(L + L_O + (c & 7) * 256 + 16 * g), wof[0], a);
      a2 = mfma32(*(LAS u32x4*)(L + L_O + (c & 7) * 256 + 64 + 16 * g), wof[1], a2);
      a = mfma32(*(LAS u32x4*)(L + L_O + (c & 7) * 256 + 128 + 16 * g), wof[2], a);
      a2 = mfma32(*(LAS u32x4*)(L + L_O + (c & 7) * 256 + 192 + 16 * g), wof[3], a2);
      a += a2;
      asm volatile("" :: "v"(a));
      if (real && g < 2) {
#pragma unroll
        for (int i = 0; i < 4; ++i) st_gran(p.ws, WS_OUT + ((((4 * g + i) * 32 + 4 * wcc + w) * 48) + whead) * 128 + c * 8, __float_as_uint(a[i]), tag32);
      }
      STAMP(13);
    }
    if (wave == 0 || !real) {
      const float xres = bflo(p.x[(r >> 2) * DM + 16 * (4 * xg + (r & 3)) + c]);
      const u32 base = WS_OUT + (((r >> 2) * 32 + 4 * xg + (r & 3)) * 48) * 128 + c * 8;
      u64 t[12] = {0, 0, 0, 0, 0, 0, 0, 0, 0, 0, 0, 0};
      if (H2DLY && real) __builtin_amdgcn_s_sleep(H2DLY);
      {
        u32 have = SKIP_FFN ? 0xf00u : 0u;
_Pragma("nounroll")
        for (u32 n = 0; n < nspin; ++n) {
          u64 tt[12];
#pragma unroll
          for (int i = 0; i < 12; ++i) { const int pr = (4 * i + g < 46) ? 4 * i + g : 45; tt[i] = ld_gran(p.ws, ((have >> i) & 1) ? WS_DUMMY : base + pr * 128); }
#pragma unroll
          for (int i = 0; i < 12; ++i) {
            t[i] = ((have >> i) & 1) ? t[i] : tt[i];
            if (__all((u32)(tt[i] >> 32) == tag32)) have |= 1u << i;
          }
          if (have == 0xfffu) break;
          __builtin_amdgcn_s_sleep(POLLSLP);
        }
      }
      float sa = 0.f, sf = 0.f;
#pragma unroll
      for (int i = 0; i < 8; ++i) sa += __uint_as_float((u32)t[i]);
#pragma unroll
      for (int i = 8; i < 12; ++i) sf += (4 * i + g < 46) ? __uint_as_float((u32)t[i]) : 0.f;
      sa = xrow_sum(sa); sf = xrow_sum(sf);
      const float res = rbf(xres + rbf(sa)) + rbf(sf);
      const u32 rv = pk2(res, 0.f);
      asm volatile("" :: "v"(rv));
      if (real) {
        if (HOLD && blk == 0) { while ((int)(rt32() - (t0 + HOLD)) < 0) __builtin_amdgcn_s_sleep(2); }
        if (blk == 0 && lane == 0) __hip_atomic_store((GAS u32*)(p.ws + 1024), rt32(), __ATOMIC_RELAXED, __HIP_MEMORY_SCOPE_AGENT);
        u32 rv2 = rv;
        if (REPORT && blk == 0 && lane == 0) {
          const u32 tt = REPORT == 1 ? rt32() - t0 : lds_ld(L + L_FLAG + 48);
          rv2 = pk2((float)tt * 0.01f, 0.f);
        }
        if (OUTSC1) {
          const u32 nb = __builtin_amdgcn_mov_dpp((int)rv2, 0xB1, 0xf, 0xf, true);
          if (g == 0 && !(c & 1)) __hip_atomic_store((GAS u32*)(p.out + (r >> 2) * DM + 16 * (4 * xg + (r & 3)) + c), (rv2 & 0xffffu) | (nb << 16), __ATOMIC_RELAXED, __HIP_MEMORY_SCOPE_AGENT);
        } else if (g == 0) p.out[(r >> 2) * DM + 16 * (4 * xg + (r & 3)) + c] = (u16)(rv2 & 0xffffu);
        if (blk == 0 && lane == 0) __hip_atomic_store((GAS u64*)(p.ws + WS_EPOCH), ep, __ATOMIC_RELAXED, __HIP_MEMORY_SCOPE_AGENT);
      }
      STAMP(10);
    }
  }
  STAMP_FLUSH();
}

__global__ void __launch_bounds__(64) post_kernel(const unsigned* tend) {
  const u32 t0 = K3CFG == 1 ? __hip_atomic_load((const GAS u32*)tend, __ATOMIC_RELAXED, __HIP_MEMORY_SCOPE_AGENT) : rt32();
  while ((int)(rt32() - (t0 + 3000)) < 0) __builtin_amdgcn_s_sleep(2);
}
extern "C" void kernel_launch(void* const* d_in, const int* in_sizes, int n_in, void* d_out, int out_size, void* d_ws, size_t ws_size, hipStream_t stream) {
  Params p;
  p.x = (const u16*)d_in[0]; p.k = (const u16*)d_in[1]; p.v = (const u16*)d_in[2]; p.wq = (const u16*)d_in[3]; p.wo = (const u16*)d_in[4];
  p.wg = (const u16*)d_in[6]; p.wu = (const u16*)d_in[7]; p.wd = (const u16*)d_in[8];
  p.out = (u16*)d_out; p.ws = (unsigned char*)d_ws;
  hipLaunchKernelGGL(fused_decode_kernel, dim3(NWG), dim3(NTHR), 0, stream, p);
  hipLaunchKernelGGL(post_kernel, dim3(1), dim3(64), 0, stream, (const unsigned*)d_ws + 256);
}
```

```cpp
#include <hip/hip_runtime.h>
#include <stdint.h>

#define GAS __attribute__((address_space(1)))
#define LAS __attribute__((address_space(3)))
typedef unsigned u32;
typedef unsigned long long u64;
typedef unsigned short u16;
typedef u32 u32x4 __attribute__((ext_vector_type(4)));
typedef u32 u32x2 __attribute__((ext_vector_type(2)));
typedef float f32x4 __attribute__((ext_vector_type(4)));
typedef float f32x2 __attribute__((ext_vector_type(2)));
typedef short s16x4 __attribute__((ext_vector_type(4)));
typedef __bf16 bf16x8 __attribute__((ext_vector_type(8)));
typedef __bf16 bf16x2 __attribute__((ext_vector_type(2)));

constexpr int NCODE = 21;
constexpr int PACE10 = 71;
constexpr int PACE_T0 = 250;
constexpr int PACE_FROM = 8;
constexpr int DAHEAD = 3;
constexpr int DPRO = 1;
constexpr int TQ0 = 380, TQR = 48;
constexpr int TGS = 1000;
constexpr int TG15 = 950;
constexpr int TG2 = 1350;
constexpr int POLLSLP = 4, H1DLY = 18, H2DLY = 14;

constexpr int DM = 512, SK = 8192, DH = 128, FF = 1792;
constexpr int NWG = 256, NTHR = 512;
constexpr int RING = 8;
constexpr int NUNIT = 64;
constexpr float SC = 0.08838834764831845f * 1.4426950408889634f;
constexpr float THR_RAW = 8.0f / SC;

constexpr u32 WS_EPOCH = 0;
constexpr u32 WS_DUMMY = 2048;
constexpr u32 WS_START = 4096;
constexpr u32 WS_T0 = 8192;
constexpr u32 WS_Q = 65536;
constexpr u32 WS_GU = WS_Q + 8 * 8 * 4 * 512 * 8;
constexpr u32 WS_H = WS_GU + 28 * 2 * 4 * 8 * 64 * 8;
constexpr u32 WS_OP = WS_H + 8 * 1792 * 8;
constexpr u32 WS_ML = WS_OP + 32 * 8 * 4 * 64 * 8;
constexpr u32 WS_OUT = WS_ML + 32 * 8 * 4 * 2 * 8;
constexpr u32 WS_END = WS_OUT + 8 * 32 * 48 * 16 * 8;
static_assert(WS_END <= (8u << 20), "ws map");

constexpr u32 L_RING = 0;
constexpr u32 L_STAGE = 4 * RING * 4096;
constexpr u32 L_XN = L_STAGE + 16384;
constexpr u32 XN_STR = 1056;
constexpr u32 L_Q = L_XN + 8 * XN_STR;
constexpr u32 L_O = L_Q + 1024;
constexpr u32 L_FLAG = L_O + 2048;
constexpr u32 L_SCR = L_FLAG + 256;
constexpr u32 L_END = L_SCR + 1024;
static_assert(L_END <= 163840, "lds");
constexpr u32 F_SYNC = 0, F_Q = 4, F_SYNC2 = 8, F_WO = 12, F_EPOCH = 16, F_SYNC3 = 24, F_ANCH = 28, F_OUTP = 32;

constexpr u32 PER256 = (u32)(4096.0 * 1024.0 / (PACE10 * 0.1e12) * 1e8 * 256.0);
constexpr u32 SPIN_LDS = 1u << 22, SPIN_GLB = 1u << 17;

struct Params {
  const u16* x; const u16* k; const u16* v; const u16* wq; const u16* wo; const u16* wg; const u16* wu; const u16* wd;
  u16* out; unsigned char* ws;
};

__device__ __forceinline__ u32 rt32() { return (u32)__builtin_amdgcn_s_memrealtime(); }
__device__ __forceinline__ float bflo(u32 w) { return __uint_as_float(w << 16); }
__device__ __forceinline__ float bfhi(u32 w) { return __uint_as_float(w & 0xffff0000u); }
__device__ __forceinline__ u32 pk2(float lo, float hi) { f32x2 t = {lo, hi}; return __builtin_bit_cast(u32, __builtin_convertvector(t, bf16x2)); }
__device__ __forceinline__ float rbf(float f) { return bflo(pk2(f, f)); }
template <int CTRL> __device__ __forceinline__ float dpp(float x) {
  return __builtin_bit_cast(float, __builtin_amdgcn_mov_dpp(__builtin_bit_cast(int, x), CTRL, 0xf, 0xf, true));
}
__device__ __forceinline__ float xrow_max(float x) {
  auto s = __builtin_amdgcn_permlane16_swap(__float_as_uint(x), __float_as_uint(x), false, false);
  x = fmaxf(__uint_as_float(s[0]), __uint_as_float(s[1]));
  auto t = __builtin_amdgcn_permlane32_swap(__float_as_uint(x), __float_as_uint(x), false, false);
  return fmaxf(__uint_as_float(t[0]), __uint_as_float(t[1]));
}
__device__ __forceinline__ float xrow_sum(float x) {
  auto s = __builtin_amdgcn_permlane16_swap(__float_as_uint(x), __float_as_uint(x), false, false);
  x = __uint_as_float(s[0]) + __uint_as_float(s[1]);
  auto t = __builtin_amdgcn_permlane32_swap(__float_as_uint(x), __float_as_uint(x), false, false);
  return __uint_as_float(t[0]) + __uint_as_float(t[1]);
}
__device__ __forceinline__ float wave_sum(float x) {
  x += dpp<0xB1>(x); x += dpp<0x4E>(x); x += dpp<0x141>(x); x += dpp<0x128>(x);
  return xrow_sum(x);
}
__device__ __forceinline__ f32x4 mfma32(u32x4 a, u32x4 b, f32x4 c) {
  return __builtin_amdgcn_mfma_f32_16x16x32_bf16(__builtin_bit_cast(bf16x8, a), __builtin_bit_cast(bf16x8, b), c, 0, 0, 0);
}
__device__ __forceinline__ f32x4 mfma16(u32x2 a, u32x2 b, f32x4 c) {
  return __builtin_amdgcn_mfma_f32_16x16x16bf16_1k(__builtin_bit_cast(s16x4, a), __builtin_bit_cast(s16x4, b), c, 0, 0, 0);
}
__device__ __forceinline__ u32x2 trread(LAS unsigned char* p) {
  return __builtin_bit_cast(u32x2, __builtin_amdgcn_ds_read_tr16_b64_v4i16((LAS s16x4*)p));
}
__device__ __forceinline__ u32x4 trread2(LAS unsigned char* p0, LAS unsigned char* p1) {
  const u32x2 a = trread(p0), b = trread(p1); return u32x4{a.x, a.y, b.x, b.y};
}
__device__ __forceinline__ void dma4(u32 ldst, u32 v0, u32 v1, u32 v2, u32 v3, __amdgpu_buffer_rsrc_t srd, u32 soff) {
  u32 keep;
  asm volatile("s_waitcnt lgkmcnt(0)\n\ts_mov_b32 %0, m0\n\ts_mov_b32 m0, %1\n\ts_nop 0\n\t"
               "buffer_load_dwordx4 %2, %6, %7 offen nt lds\n\t"
               "buffer_load_dwordx4 %3, %6, %7 offen offset:1024 nt lds\n\t"
               "buffer_load_dwordx4 %4, %6, %7 offen offset:2048 nt lds\n\t"
               "buffer_load_dwordx4 %5, %6, %7 offen offset:3072 nt lds\n\t"
               "s_mov_b32 m0, %0"
               : "=&s"(keep) : "s"(ldst), "v"(v0), "v"(v1), "v"(v2), "v"(v3), "s"(srd), "s"(soff) : "memory");
}
__device__ __forceinline__ void dma1(u32 ldst, const void* gp) {
  u32 keep;
  asm volatile("s_mov_b32 %0, m0\n\ts_mov_b32 m0, %1\n\ts_nop 0\n\tglobal_load_lds_dwordx4 %2, off nt\n\ts_mov_b32 m0, %0"
               : "=&s"(keep) : "s"(ldst), "v"(gp) : "memory");
}
__device__ __forceinline__ void dma1p(u32 ldst, const void* gp) {
  u32 keep;
  asm volatile("s_mov_b32 %0, m0\n\ts_mov_b32 m0, %1\n\ts_nop 0\n\tglobal_load_lds_dwordx4 %2, off\n\ts_mov_b32 m0, %0"
               : "=&s"(keep) : "s"(ldst), "v"(gp) : "memory");
}
#define VMCNT(N) asm volatile("s_waitcnt vmcnt(" #N ")" ::: "memory")
#define LGKM0() asm volatile("s_waitcnt lgkmcnt(0)" ::: "memory")
#define NOUNROLL _Pragma("nounroll")
__device__ __forceinline__ void wait_units(int n) {
  switch (n) {
    case 0: VMCNT(0); break;   case 1: VMCNT(4); break;   case 2: VMCNT(8); break;   case 3: VMCNT(12); break;
    case 4: VMCNT(16); break;  case 5: VMCNT(20); break;  case 6: VMCNT(24); break;  default: VMCNT(28); break;
  }
}
__device__ __forceinline__ void bar() { LGKM0(); __builtin_amdgcn_s_barrier(); asm volatile("" ::: "memory"); }
__device__ __forceinline__ u32 lds_ld(LAS unsigned char* p) { return *(volatile LAS u32*)p; }
__device__ __forceinline__ void lds_add(LAS unsigned char* p, u32 v) { __hip_atomic_fetch_add((LAS u32*)p, v, __ATOMIC_RELAXED, __HIP_MEMORY_SCOPE_WORKGROUP); }
__device__ __forceinline__ void lds_wait_ge(LAS unsigned char* p, u32 want) {
  NOUNROLL
  for (u32 n = 0; n < SPIN_LDS; ++n) { if (lds_ld(p) >= want) break; __builtin_amdgcn_s_sleep(1); }
  asm volatile("" ::: "memory");
}
__device__ __forceinline__ void wait_until(u32 due) { while ((int)(rt32() - due) < 0) __builtin_amdgcn_s_sleep(4); }
__device__ __forceinline__ void st_gran(unsigned char* ws, u32 off, u32 lo, u32 hi) {
  __hip_atomic_store((GAS u64*)(ws + off), ((u64)hi << 32) | lo, __ATOMIC_RELAXED, __HIP_MEMORY_SCOPE_AGENT);
}
__device__ __forceinline__ u64 ld_gran(unsigned char* ws, u32 off) {
  return __hip_atomic_load((GAS u64*)(ws + off), __ATOMIC_RELAXED, __HIP_MEMORY_SCOPE_AGENT);
}

__global__ void __launch_bounds__(NTHR, 2) fused_decode_kernel(Params p) {
  __shared__ __attribute__((aligned(16))) unsigned char lds_raw[L_END];
  LAS unsigned char* L = (LAS unsigned char*)lds_raw;
  const u32 lbase = (u32)(size_t)L;
  const int tid = threadIdx.x, lane = tid & 63, wave = __builtin_amdgcn_readfirstlane(tid >> 6);
  const int g = lane >> 4, c = lane & 15;
  const int blk = blockIdx.x, xg = blk & 7, r = blk >> 3;
  const u32 t0 = rt32();
  u64 pc0; asm volatile("s_getpc_b64 %0" : "=s"(pc0));
  const __amdgpu_buffer_rsrc_t wsr = __builtin_amdgcn_make_buffer_rsrc(p.ws, 0, WS_END, 0x00020000);
  const int as = r & 3, ab = r >> 2, akv = xg, apair = ab * 8 + akv;
  const int whead = 4 * xg + (r & 3), wcc = r >> 2;

  if (wave < 4) {
    const size_t pbase = (size_t)apair * SK * DH;
    const __amdgpu_buffer_rsrc_t ksr = __builtin_amdgcn_make_buffer_rsrc((void*)(p.k + pbase), 0, SK * DH * 2, 0x00020000);
    const __amdgpu_buffer_rsrc_t vsr = __builtin_amdgcn_make_buffer_rsrc((void*)(p.v + pbase), 0, SK * DH * 2, 0x00020000);
    const u32 vk0 = g * 256 + 16 * (c ^ g), vk1 = g * 256 + 16 * (c ^ (4 + g)), vk2 = g * 256 + 16 * (c ^ (8 + g)), vk3 = g * 256 + 16 * (c ^ (12 + g));
    const u32 vv0 = g * 256 + 16 * (c ^ (g << 1)), vv1 = g * 256 + 16 * (c ^ ((4 + g) << 1));
    const u32 ring = lbase + L_RING + wave * (RING * 4096);
    int nis = 0;
    int landed = 0;
    u32 tnow = t0, pA = t0 + PACE_T0;
    const u32 sbase = as * 16384u + wave * 4096u;
    auto issue = [&]() {
      if (nis >= PACE_FROM) {
        if (nis < PACE_FROM + 6) { const u32 an = __builtin_amdgcn_readfirstlane(lds_ld(L + L_FLAG + F_ANCH)); if (an) pA = an + PACE_T0; }
        const u32 due = pA + (((u32)nis * PER256) >> 8);
        if ((int)(tnow - due) < 0) {
          NOUNROLL
          for (u32 n = 0; n < SPIN_LDS; ++n) { tnow = rt32(); if ((int)(tnow - due) >= 0) break; __builtin_amdgcn_s_sleep(1); }
        }
      }
      const u32 soff = (u32)(nis >> 1) * 65536u + sbase, ldst = ring + (u32)(nis & (RING - 1)) * 4096u;
      if (nis & 1) dma4(ldst, vv0, vv1, vv0, vv1, vsr, soff); else dma4(ldst, vk0, vk1, vk2, vk3, ksr, soff);
      ++nis;
    };
    issue();
    bar();
    NOUNROLL
    while (nis < RING) {
      if (nis >= DPRO) { wait_units(DPRO - 1); landed = nis - (DPRO - 1); }
      if (lds_ld(L + L_FLAG + F_Q) >= 4) break;
      issue();
    }
    auto refill = [&](int cons) { while (nis - landed < DAHEAD && nis < cons + RING && nis < NUNIT) issue(); };
    lds_wait_ge(L + L_FLAG + F_Q, 4);
    u32x4 qf[4];
#pragma unroll
    for (int s = 0; s < 4; ++s) qf[s] = *(LAS u32x4*)(L + L_Q + (c & 3) * 256 + 64 * s + 16 * g);
    refill(0);
    LAS unsigned char* R = L + L_RING + wave * (RING * 4096);
    const u32 ka = c * 256 + 16 * (g ^ c);
    const u32 va = (4 * g + (c >> 2)) * 256 + 32 * ((4 * g + (c >> 2)) & 7) + 8 * (c & 3);
    float m = -INFINITY, lsum = 0.f;
    f32x4 acc[8];
#pragma unroll
    for (int i = 0; i < 8; ++i) acc[i] = f32x4{0.f, 0.f, 0.f, 0.f};
    NOUNROLL
    for (int j = 0; j < NUNIT / 2; ++j) {
      wait_units(nis - 2 * j - 1);
      landed = landed > 2 * j + 1 ? landed : 2 * j + 1;
      refill(2 * j);
      LAS unsigned char* Kt = R + ((2 * j) & (RING - 1)) * 4096;
      LAS unsigned char* Vt = R + ((2 * j + 1) & (RING - 1)) * 4096;
      f32x4 sa = {0.f, 0.f, 0.f, 0.f}, sb = {0.f, 0.f, 0.f, 0.f};
      {
        const u32x4 k0 = *(LAS u32x4*)(Kt + ka), k1 = *(LAS u32x4*)(Kt + (ka ^ 64)), k2 = *(LAS u32x4*)(Kt + (ka ^ 128)), k3 = *(LAS u32x4*)(Kt + (ka ^ 192));
        sa = mfma32(k0, qf[0], sa); sb = mfma32(k1, qf[1], sb); sa = mfma32(k2, qf[2], sa); sb = mfma32(k3, qf[3], sb);
      }
      const f32x4 sc = sa + sb;
      const float tmax = fmaxf(fmaxf(sc[0], sc[1]), fmaxf(sc[2], sc[3]));
      if (__builtin_amdgcn_ballot_w64(tmax > m + THR_RAW) != 0ull) {
        const float mn = fmaxf(m, xrow_max(tmax));
        const float al = __builtin_amdgcn_exp2f((m - mn) * SC);
        m = mn; lsum *= al;
        const float a0 = dpp<0x00>(al), a1 = dpp<0x55>(al), a2 = dpp<0xAA>(al), a3 = dpp<0xFF>(al);
#pragma unroll
        for (int i = 0; i < 8; ++i) { acc[i][0] *= a0; acc[i][1] *= a1; acc[i][2] *= a2; acc[i][3] *= a3; }
      }
      const float mc = m * SC;
      const float p0 = __builtin_amdgcn_exp2f(fmaf(sc[0], SC, -mc)), p1 = __builtin_amdgcn_exp2f(fmaf(sc[1], SC, -mc));
      const float p2 = __builtin_amdgcn_exp2f(fmaf(sc[2], SC, -mc)), p3 = __builtin_amdgcn_exp2f(fmaf(sc[3], SC, -mc));
      lsum += (p0 + p1) + (p2 + p3);
      const u32x2 pa = {pk2(p0, p1), pk2(p2, p3)};
      wait_units(nis - 2 * j - 2);
      landed = landed > 2 * j + 2 ? landed : 2 * j + 2;
      refill(2 * j + 1);
#pragma unroll
      for (int i = 0; i < 8; ++i) acc[i] = mfma16(pa, trread(Vt + (va ^ (32 * i))), acc[i]);
      refill(2 * j + 2);
    }

    lsum = xrow_sum(lsum);
    if (g == 0) {
#pragma unroll
      for (int i = 0; i < 8; ++i) *(LAS f32x4*)(R + i * 256 + c * 16) = acc[i];
      if (c < 4) *(LAS f32x2*)(R + 2048 + c * 8) = f32x2{m * SC, lsum};
    }
  } else {
    const int hw = wave - 4;
    __builtin_amdgcn_s_setprio(3);
    u32x4 xa, xb; u32x2 ev;
    if (hw == 0 && lane < 16) *(LAS u32x4*)(L + L_FLAG + lane * 16) = u32x4{0, 0, 0, 0};
    const int kq = r & 3;
    if (NCODE && r * 4 + hw < NCODE) {
      dma1p(lbase + L_SCR, (const char*)(pc0 & ~255ull) + (r * 4 + hw) * 1024 + lane * 16);
    }
    {
      const u16* xp = p.x + (2 * hw) * DM + lane * 8;
      const u16* wp = p.wq + (size_t)(kq * 128 + hw * 32 + (lane >> 3)) * 4096 + xg * 512 + (r >> 2) * 64 + (lane & 7) * 8;
      const u32 ldst = lbase + L_STAGE + hw * 4096;
      u32 keep;
      asm volatile("global_load_dwordx4 %0, %4, off\n\tglobal_load_dwordx4 %1, %4, off offset:1024\n\tglobal_load_dwordx2 %2, %5, off\n\t"
                   "s_mov_b32 %3, m0\n\ts_mov_b32 m0, %10\n\ts_nop 0\n\t"
                   "global_load_lds_dwordx4 %6, off nt\n\ts_add_u32 m0, m0, 0x400\n\ts_nop 0\n\t"
                   "global_load_lds_dwordx4 %7, off nt\n\ts_add_u32 m0, m0, 0x400\n\ts_nop 0\n\t"
                   "global_load_lds_dwordx4 %8, off nt\n\ts_add_u32 m0, m0, 0x400\n\ts_nop 0\n\t"
                   "global_load_lds_dwordx4 %9, off nt\n\ts_mov_b32 m0, %3\n\t"
                   "s_waitcnt lgkmcnt(0)\n\ts_barrier\n\ts_waitcnt vmcnt(4)"
                   : "=&v"(xa), "=&v"(xb), "=&v"(ev), "=&s"(keep)
                   : "v"(xp), "v"(p.ws + WS_EPOCH), "v"(wp), "v"(wp + (size_t)8 * 4096), "v"(wp + (size_t)16 * 4096), "v"(wp + (size_t)24 * 4096), "s"(ldst)
                   : "memory", "scc");
    }
    const u64 ep = (((u64)ev.y << 32) | ev.x) + 1ull;
    const u32 tag32 = (u32)ep, t48lo = ((u32)ep) << 16, t48hi = (u32)(ep >> 16), t40lo = ((u32)ep) << 24, t40hi = (u32)(ep >> 8);
#pragma unroll
    for (int q = 0; q < 2; ++q) {
      const u32x4 xv = q ? xb : xa;
      float v[8];
#pragma unroll
      for (int i = 0; i < 4; ++i) { v[2 * i] = bflo(xv[i]); v[2 * i + 1] = bfhi(xv[i]); }
      const float mean = wave_sum(((v[0] + v[1]) + (v[2] + v[3])) + ((v[4] + v[5]) + (v[6] + v[7]))) * (1.f / DM);
      float q2 = 0.f;
#pragma unroll
      for (int i = 0; i < 8; ++i) { const float d = v[i] - mean; q2 = fmaf(d, d, q2); }
      const float var = rbf(wave_sum(q2) * (1.f / DM));
      const float rden = __builtin_amdgcn_rcpf(rbf(__builtin_amdgcn_sqrtf(rbf(var + 1e-5f))));
      const float mb = rbf(mean);
      u32x4 o;
#pragma unroll
      for (int i = 0; i < 4; ++i) { const u32 d = pk2(v[2 * i] - mb, v[2 * i + 1] - mb); o[i] = pk2(bflo(d) * rden, bfhi(d) * rden); }
      *(LAS u32x4*)(L + L_XN + (2 * hw + q) * XN_STR + lane * 16) = o;
    }
    const int tq = c >> 2, tp = c & 3;
    auto tile_mma = [&](const u32x4 (&act)[4], int cg) {
      f32x4 a0 = {0.f, 0.f, 0.f, 0.f}, a1 = {0.f, 0.f, 0.f, 0.f};
      LAS unsigned char* ap = L + L_STAGE + (8 * g + tq) * 128 + 32 * cg + 8 * tp;
      a0 = mfma32(act[0], trread2(ap, ap + 512), a0);
      a1 = mfma32(act[1], trread2(ap + 4096, ap + 4096 + 512), a1);
      a0 = mfma32(act[2], trread2(ap + 8192, ap + 8192 + 512), a0);
      a1 = mfma32(act[3], trread2(ap + 12288, ap + 12288 + 512), a1);
      return a0 + a1;
    };
    auto xn_frags = [&](u32x4 (&act)[4], int k0) {
#pragma unroll
      for (int s = 0; s < 4; ++s) act[s] = *(LAS u32x4*)(L + L_XN + (c & 7) * XN_STR + 2 * k0 + 64 * s + 16 * g);
    };
    auto f24 = [&](float f) { const u32 u = __float_as_uint(f); return t40lo | ((u + 0x7fu + ((u >> 8) & 1u)) >> 8); };
    auto ok40 = [&](const u32x4& t) { return ((t[0] ^ t40lo) >> 24) == 0 && t[1] == t40hi && ((t[2] ^ t40lo) >> 24) == 0 && t[3] == t40hi; };
    auto ok48 = [&](const u32x4& t) { return ((t[0] ^ t48lo) >> 16) == 0 && t[1] == t48hi && ((t[2] ^ t48lo) >> 16) == 0 && t[3] == t48hi; };
    u32x4 act[4];
    asm volatile("s_waitcnt vmcnt(0) lgkmcnt(0)" ::: "memory");
    if (lane == 0) lds_add(L + L_FLAG + F_SYNC, 1u);
    lds_wait_ge(L + L_FLAG + F_SYNC, 4);
    xn_frags(act, kq * 128);
    {
      const f32x4 d = tile_mma(act, hw);
      if (g < 2) {
#pragma unroll
        for (int i = 0; i < 4; ++i) st_gran(p.ws, WS_Q + ((((xg * 8 + 4 * g + i) * 4 + kq) * 512) + (r >> 2) * 64 + 16 * hw + c) * 8, f24(d[i]), t40hi);
      }
    }
    if (hw == 1 && lane == 0) st_gran(p.ws, WS_T0 + blk * 8, t40lo | (t0 & 0xffffffu), t40hi);
    LGKM0();
    if (lane == 0) lds_add(L + L_FLAG + F_SYNC, 1u);
    if (hw < 2) {
      const u32 qb = WS_Q + ((akv * 8 + ab) * 4) * 512 * 8;
      wait_until(t0 + TQ0 - (u32)(r * TQR) / 16);
      float q0[2], q1[2];
      NOUNROLL
      for (u32 n = 0; n < SPIN_GLB; ++n) {
        bool ok = true;
#pragma unroll
        for (int h = 0; h < 2; ++h) {
          q0[h] = 0.f; q1[h] = 0.f;
#pragma unroll
          for (int i = 0; i < 4; ++i) {
            const u32x4 t = __builtin_amdgcn_raw_buffer_load_b128(wsr, qb + (i * 512 + (2 * hw + h) * 128 + 2 * lane) * 8, 0, 16);
            ok &= ok40(t); q0[h] += __uint_as_float(t[0] << 8); q1[h] += __uint_as_float(t[2] << 8);
          }
        }
        if (__all(ok)) break;
        __builtin_amdgcn_s_sleep(POLLSLP);
        asm volatile("" ::: "memory");
      }
      *(LAS u32*)(L + L_Q + (2 * hw) * 256 + lane * 4) = pk2(q0[0], q1[0]);
      *(LAS u32*)(L + L_Q + (2 * hw + 1) * 256 + lane * 4) = pk2(q0[1], q1[1]);
      LGKM0();
      if (lane == 0) lds_add(L + L_FLAG + F_Q, 2u);
      if (hw == 0) {
        u64 ts = 0;
        bool ok = false;
        NOUNROLL
        for (u32 n = 0; n < 64 && !ok; ++n) {
          ts = ld_gran(p.ws, WS_T0 + (xg + 8 * (lane & 31)) * 8);
          ok = __all((((u32)ts ^ t40lo) >> 24) == 0 && (u32)(ts >> 32) == t40hi);
          if (!ok) __builtin_amdgcn_s_sleep(8);
        }
        int d = ((int)(((u32)ts - t0) << 8)) >> 8;
        d = min(d, __builtin_amdgcn_mov_dpp(d, 0xB1, 0xf, 0xf, true)); d = min(d, __builtin_amdgcn_mov_dpp(d, 0x4E, 0xf, 0xf, true));
        d = min(d, __builtin_amdgcn_mov_dpp(d, 0x141, 0xf, 0xf, true)); d = min(d, __builtin_amdgcn_mov_dpp(d, 0x128, 0xf, 0xf, true));
        int dm = min(min(__builtin_amdgcn_readlane(d, 0), __builtin_amdgcn_readlane(d, 16)), 0);
        dm = dm < -400 ? -400 : dm;
        if (ok && lane == 0) *(volatile LAS u32*)(L + L_FLAG + F_ANCH) = (t0 + dm) | 1u;
      }
      if (hw == 1) {
        wait_until(t0 + TGS);
        NOUNROLL
        for (u32 n = 0; n < SPIN_GLB; ++n) {
          const u32x4 a = __builtin_amdgcn_raw_buffer_load_b128(wsr, WS_START + lane * 16, 0, 16);
          const u32x4 b = __builtin_amdgcn_raw_buffer_load_b128(wsr, WS_START + 1024 + lane * 16, 0, 16);
          const u32 e0 = (u32)ep, e1 = (u32)(ep >> 32);
          const bool ok = a[0] == e0 && a[1] == e1 && a[2] == e0 && a[3] == e1 && b[0] == e0 && b[1] == e1 && b[2] == e0 && b[3] == e1;
          if (__all(ok)) break;
          __builtin_amdgcn_s_sleep(32);
          asm volatile("" ::: "memory");
        }
      }
    } else {
      const int h2 = hw - 2;
      u32 nsync2 = 0;
      auto arrive2 = [&]() {
        LGKM0();
        if (lane == 0) lds_add(L + L_FLAG + F_SYNC2, 1u);
        nsync2 += 2;
        lds_wait_ge(L + L_FLAG + F_SYNC2, nsync2);
      };
      auto stage2 = [&](const u16* w, int ld) {
        const u16* wp = w + (size_t)(h2 * 64 + (lane >> 3)) * ld + (lane & 7) * 8;
#pragma unroll
        for (int i = 0; i < 8; ++i) dma1(lbase + L_STAGE + h2 * 8192 + i * 1024, wp + (size_t)i * 8 * ld);
      };
      if (h2 == 1) {
        const u32x4 z = {0u, ~tag32, 0u, ~tag32};
#pragma unroll
        for (int i = 0; i < 2; ++i)
          __builtin_amdgcn_raw_buffer_store_b128(z, wsr, WS_OP + ((((akv * 4 + 2 * i + (lane >> 5)) * 8 + ab) * 4 + as) * 64) * 8 + (lane & 31) * 16, 0, 16);
        if (lane < 4) __builtin_amdgcn_raw_buffer_store_b128(z, wsr, WS_ML + (((akv * 4 + lane) * 8 + ab) * 4 + as) * 16, 0, 16);
      }
      lds_wait_ge(L + L_FLAG + F_SYNC, 8);
      const bool f1 = r < 14, f15 = r >= 14 && r < 18, f2 = r >= 18;
      const int t1 = xg * 14 + r, sl1 = t1 >> 2, fq = t1 & 3, ks = r - 18;
      const u16* wo_tile = p.wo + (size_t)whead * 128 * DM + wcc * 64;
      if (f1) stage2(p.wg + (size_t)fq * 128 * FF + sl1 * 64, FF);
      else if (f2) stage2(p.wd + (size_t)ks * 128 * DM + xg * 64, DM);
      else stage2(wo_tile, DM);
      VMCNT(0);
      if (h2 == 1 && lane == 0) {
        __hip_atomic_store((GAS u64*)(p.ws + WS_START + blk * 8), ep, __ATOMIC_RELAXED, __HIP_MEMORY_SCOPE_AGENT);
        *(volatile LAS u64*)(L + L_FLAG + F_EPOCH) = ep;
      }
      arrive2();
      if (f1) {
        xn_frags(act, fq * 128);
        f32x4 d[2];
        d[0] = tile_mma(act, 2 * h2); d[1] = tile_mma(act, 2 * h2 + 1);
        asm volatile("" : "+v"(d[0]), "+v"(d[1]));
        arrive2();
        stage2(p.wu + (size_t)fq * 128 * FF + sl1 * 64, FF);
#pragma unroll
        for (int w = 0; w < 2; ++w) if (g < 2) {
#pragma unroll
          for (int i = 0; i < 4; ++i) st_gran(p.ws, WS_GU + ((((sl1 * 2 + 0) * 4 + fq) * 8 + 4 * g + i) * 64 + 16 * (2 * h2 + w) + c) * 8, f24(d[w][i]), t40hi);
        }
        VMCNT(8);
        arrive2();
        d[0] = tile_mma(act, 2 * h2); d[1] = tile_mma(act, 2 * h2 + 1);
        asm volatile("" : "+v"(d[0]), "+v"(d[1]));
        arrive2();
        stage2(wo_tile, DM);
#pragma unroll
        for (int w = 0; w < 2; ++w) if (g < 2) {
#pragma unroll
          for (int i = 0; i < 4; ++i) st_gran(p.ws, WS_GU + ((((sl1 * 2 + 1) * 4 + fq) * 8 + 4 * g + i) * 64 + 16 * (2 * h2 + w) + c) * 8, f24(d[w][i]), t40hi);
        }
        VMCNT(8);
        arrive2();
      } else if (f15) {
        const int sl = xg * 4 + (r - 14);
        if (sl < 28) {
          wait_until(t0 + TG15);
          {
            const u32 so = WS_GU + ((((sl * 2 + 1) * 4 + (lane & 3)) * 8) * 64 + 63) * 8;
            NOUNROLL
            for (u32 n = 0; n < SPIN_GLB; ++n) {
              const u64 t = ld_gran(p.ws, so);
              if (__all((((u32)t ^ t40lo) >> 24) == 0 && (u32)(t >> 32) == t40hi)) break;
              __builtin_amdgcn_s_sleep(16);
            }
          }
          float gs[2][2], us[2][2];
          const int cp = lane & 31;
          NOUNROLL
          for (u32 n = 0; n < SPIN_GLB; ++n) {
            bool ok = true;
#pragma unroll
            for (int bb = 0; bb < 2; ++bb) {
              const int b = 4 * h2 + 2 * bb + (lane >> 5);
              gs[bb][0] = gs[bb][1] = us[bb][0] = us[bb][1] = 0.f;
#pragma unroll
              for (int i = 0; i < 4; ++i) {
                const u32x4 tg = __builtin_amdgcn_raw_buffer_load_b128(wsr, WS_GU + ((((sl * 2 + 0) * 4 + i) * 8 + b) * 64 + 2 * cp) * 8, 0, 16);
                const u32x4 tu = __builtin_amdgcn_raw_buffer_load_b128(wsr, WS_GU + ((((sl * 2 + 1) * 4 + i) * 8 + b) * 64 + 2 * cp) * 8, 0, 16);
                ok &= ok40(tg) && ok40(tu);
                gs[bb][0] += __uint_as_float(tg[0] << 8); gs[bb][1] += __uint_as_float(tg[2] << 8);
                us[bb][0] += __uint_as_float(tu[0] << 8); us[bb][1] += __uint_as_float(tu[2] << 8);
              }
            }
            if (__all(ok)) break;
            __builtin_amdgcn_s_sleep(16);
            asm volatile("" ::: "memory");
          }
#pragma unroll
          for (int bb = 0; bb < 2; ++bb) {
            const int b = 4 * h2 + 2 * bb + (lane >> 5);
            const u32 hp = pk2(rbf(gs[bb][0]) * rbf(us[bb][0]), rbf(gs[bb][1]) * rbf(us[bb][1]));
            st_gran(p.ws, WS_H + (b * FF + sl * 64 + 2 * cp) * 8, t48lo | (hp & 0xffffu), t48hi);
            st_gran(p.ws, WS_H + (b * FF + sl * 64 + 2 * cp + 1) * 8, t48lo | (hp >> 16), t48hi);
          }
        }
      } else {
        wait_until(t0 + TG2);
        {
          const u32 so = WS_H + (7 * FF + ks * 128 + 64 * (lane & 1) + 63) * 8;
          NOUNROLL
          for (u32 n = 0; n < SPIN_GLB; ++n) {
            const u64 t = ld_gran(p.ws, so);
            if (__all((((u32)t ^ t48lo) >> 16) == 0 && (u32)(t >> 32) == t48hi)) break;
            __builtin_amdgcn_s_sleep(32);
          }
        }
        const int b = lane >> 3, k0 = 64 * h2 + 8 * (lane & 7);
        u32x4 hv = {0, 0, 0, 0};
        NOUNROLL
        for (u32 n = 0; n < SPIN_GLB; ++n) {
          bool ok = true;
#pragma unroll
          for (int i = 0; i < 4; ++i) {
            const u32x4 t = __builtin_amdgcn_raw_buffer_load_b128(wsr, WS_H + (b * FF + ks * 128 + k0 + 2 * i) * 8, 0, 16);
            ok &= ok48(t);
            hv[i] = (t[0] & 0xffffu) | (t[2] << 16);
          }
          if (__all(ok)) break;
          __builtin_amdgcn_s_sleep(16);
          asm volatile("" ::: "memory");
        }
        *(LAS u32x4*)(L + L_O + b * 256 + k0 * 2) = hv;
        arrive2();
#pragma unroll
        for (int s = 0; s < 4; ++s) act[s] = *(LAS u32x4*)(L + L_O + (c & 7) * 256 + 64 * s + 16 * g);
        f32x4 d[2];
        d[0] = tile_mma(act, 2 * h2); d[1] = tile_mma(act, 2 * h2 + 1);
        asm volatile("" : "+v"(d[0]), "+v"(d[1]));
        arrive2();
        stage2(wo_tile, DM);
#pragma unroll
        for (int w = 0; w < 2; ++w) if (g < 2) {
#pragma unroll
          for (int i = 0; i < 4; ++i) st_gran(p.ws, WS_OUT + ((((4 * g + i) * 32 + 4 * xg + 2 * h2 + w) * 48) + 32 + ks) * 128 + c * 8, f24(d[w][i]), t40hi);
        }
        VMCNT(8);
        arrive2();
      }
      if (h2 == 0 && lane == 0) *(volatile LAS u32*)(L + L_FLAG + F_WO) = 1;
    }
    NOUNROLL
    for (u32 n = 0; n < SPIN_LDS; ++n) { if (lds_ld(L + L_FLAG + F_WO) >= 1) break; __builtin_amdgcn_s_sleep(16); }
    asm volatile("" ::: "memory");
  }

  u32x4 wof[4];
  if (wave >= 4) {
    const int tq = c >> 2, tp = c & 3, w = wave - 4;
#pragma unroll
    for (int s = 0; s < 4; ++s) { LAS unsigned char* ap = L + L_STAGE + (32 * s + 8 * g + tq) * 128 + 32 * w + 8 * tp; wof[s] = trread2(ap, ap + 512); }
  }
  bar();
  const u64 ep = *(volatile LAS u64*)(L + L_FLAG + F_EPOCH);
  const u32 tag32 = (u32)ep, t40lo = ((u32)ep) << 24, t40hi = (u32)(ep >> 8);
  if (wave < 4) {
    const int hh = wave, ch = lane >> 3, c0 = (2 * lane) & 15;
    float mw[4], lw[4], v0[4], v1[4];
#pragma unroll
    for (int w = 0; w < 4; ++w) {
      LAS unsigned char* R = L + L_RING + w * (RING * 4096);
      const f32x2 ml = *(LAS f32x2*)(R + 2048 + hh * 8);
      mw[w] = ml[0]; lw[w] = ml[1];
      v0[w] = *(LAS float*)(R + ch * 256 + c0 * 16 + hh * 4);
      v1[w] = *(LAS float*)(R + ch * 256 + c0 * 16 + 16 + hh * 4);
    }
    const float M = fmaxf(fmaxf(mw[0], mw[1]), fmaxf(mw[2], mw[3]));
    float Ls = 0.f, o0 = 0.f, o1 = 0.f;
#pragma unroll
    for (int w = 0; w < 4; ++w) { const float f = __builtin_amdgcn_exp2f(mw[w] - M); Ls = fmaf(f, lw[w], Ls); o0 = fmaf(f, v0[w], o0); o1 = fmaf(f, v1[w], o1); }
    const float inv = __builtin_amdgcn_rcpf(Ls);
    const u32 slot = ((akv * 4 + hh) * 8 + ab) * 4 + as;
    st_gran(p.ws, WS_OP + (slot * 64 + lane) * 8, pk2(o0 * inv, o1 * inv), tag32);
    if (lane < 2) st_gran(p.ws, WS_ML + (slot * 2 + lane) * 8, __float_as_uint(lane ? Ls : M), tag32);
  } else {
    const int w = wave - 4;
    const u32 slot = (whead * 8 + 2 * w) * 4;
    u64 o[8] = {0, 0, 0, 0, 0, 0, 0, 0}, ml = 0;
    __builtin_amdgcn_s_sleep(H1DLY);
    {
      u32 have = 0;
      NOUNROLL
      for (u32 n = 0; n < SPIN_GLB; ++n) {
        u64 t[9];
#pragma unroll
        for (int s = 0; s < 8; ++s) t[s] = ld_gran(p.ws, ((have >> s) & 1) ? WS_DUMMY : WS_OP + ((slot + s) * 64 + lane) * 8);
        t[8] = ld_gran(p.ws, ((have >> 8) & 1) ? WS_DUMMY : WS_ML + (slot * 2 + (lane & 15)) * 8);
#pragma unroll
        for (int s = 0; s < 9; ++s) {
          const bool fresh = !((have >> s) & 1);
          if (s < 8) o[s] = fresh ? t[s] : o[s]; else ml = fresh ? t[8] : ml;
          if (__all((u32)(t[s] >> 32) == tag32)) have |= 1u << s;
        }
        if (have == 0x1ffu) break;
        __builtin_amdgcn_s_sleep(POLLSLP);
      }
    }
    const u32 mlv = (u32)ml;
#pragma unroll
    for (int bb = 0; bb < 2; ++bb) {
      float ms[4], ls[4];
#pragma unroll
      for (int s = 0; s < 4; ++s) { ms[s] = __uint_as_float(__builtin_amdgcn_readlane(mlv, 8 * bb + 2 * s)); ls[s] = __uint_as_float(__builtin_amdgcn_readlane(mlv, 8 * bb + 2 * s + 1)); }
      const float M = fmaxf(fmaxf(ms[0], ms[1]), fmaxf(ms[2], ms[3]));
      float W = 0.f, o0 = 0.f, o1 = 0.f;
#pragma unroll
      for (int s = 0; s < 4; ++s) { const float wt = ls[s] * __builtin_amdgcn_exp2f(ms[s] - M); W += wt; o0 = fmaf(wt, bflo((u32)o[4 * bb + s]), o0); o1 = fmaf(wt, bfhi((u32)o[4 * bb + s]), o1); }
      const float inv = __builtin_amdgcn_rcpf(W);
      *(LAS u32*)(L + L_O + (2 * w + bb) * 256 + lane * 4) = pk2(o0 * inv, o1 * inv);
    }
    LGKM0();
    if (lane == 0) lds_add(L + L_FLAG + F_SYNC3, 1u);
    NOUNROLL
    for (u32 n = 0; n < SPIN_LDS; ++n) { if (lds_ld(L + L_FLAG + F_SYNC3) >= 4) break; }
    asm volatile("" ::: "memory");
    f32x4 a = {0.f, 0.f, 0.f, 0.f}, a2 = {0.f, 0.f, 0.f, 0.f};
    a = mfma32(*(LAS u32x4*)(L + L_O + (c & 7) * 256 + 16 * g), wof[0], a);
    a2 = mfma32(*(LAS u32x4*)(L + L_O + (c & 7) * 256 + 64 + 16 * g), wof[1], a2);
    a = mfma32(*(LAS u32x4*)(L + L_O + (c & 7) * 256 + 128 + 16 * g), wof[2], a);
    a2 = mfma32(*(LAS u32x4*)(L + L_O + (c & 7) * 256 + 192 + 16 * g), wof[3], a2);
    a += a2;
    if (g < 2) {
#pragma unroll
      for (int i = 0; i < 4; ++i) {
        const u32 u = __float_as_uint(a[i]);
        st_gran(p.ws, WS_OUT + ((((4 * g + i) * 32 + 4 * wcc + w) * 48) + whead) * 128 + c * 8, t40lo | ((u + 0x7fu + ((u >> 8) & 1u)) >> 8), t40hi);
      }
    }
    if (lane == 0) lds_add(L + L_FLAG + F_OUTP, 1u);
  }
  if (wave == 0) {
    const float xres = bflo(p.x[(r >> 2) * DM + 16 * (4 * xg + (r & 3)) + c]);
    const u32 base = WS_OUT + (((r >> 2) * 32 + 4 * xg + (r & 3)) * 48) * 128 + c * 8;
    u64 t[12] = {0, 0, 0, 0, 0, 0, 0, 0, 0, 0, 0, 0};
    lds_wait_ge(L + L_FLAG + F_OUTP, 4);
    __builtin_amdgcn_s_sleep(H2DLY);
    {
      u32 have = 0;
      NOUNROLL
      for (u32 n = 0; n < SPIN_GLB; ++n) {
        u64 tt[12];
#pragma unroll
        for (int i = 0; i < 12; ++i) { const int pr = (4 * i + g < 46) ? 4 * i + g : 45; tt[i] = ld_gran(p.ws, ((have >> i) & 1) ? WS_DUMMY : base + pr * 128); }
#pragma unroll
        for (int i = 0; i < 12; ++i) {
          t[i] = ((have >> i) & 1) ? t[i] : tt[i];
          if (__all((((u32)tt[i] ^ t40lo) >> 24) == 0 && (u32)(tt[i] >> 32) == t40hi)) have |= 1u << i;
        }
        if (have == 0xfffu) break;
        __builtin_amdgcn_s_sleep(POLLSLP);
      }
    }
    float sa = 0.f, sf = 0.f;
#pragma unroll
    for (int i = 0; i < 8; ++i) sa += __uint_as_float((u32)t[i] << 8);
#pragma unroll
    for (int i = 8; i < 12; ++i) sf += (4 * i + g < 46) ? __uint_as_float((u32)t[i] << 8) : 0.f;
    sa = xrow_sum(sa); sf = xrow_sum(sf);
    const float res = rbf(xres + rbf(sa)) + rbf(sf);
    if (g == 0) p.out[(r >> 2) * DM + 16 * (4 * xg + (r & 3)) + c] = (u16)(pk2(res, 0.f) & 0xffffu);
    if (blk == 0 && lane == 0) __hip_atomic_store((GAS u64*)(p.ws + WS_EPOCH), ep, __ATOMIC_RELAXED, __HIP_MEMORY_SCOPE_AGENT);
  }
}

extern "C" void kernel_launch(void* const* d_in, const int* in_sizes, int n_in, void* d_out, int out_size, void* d_ws, size_t ws_size, hipStream_t stream) {
  Params p;
  p.x = (const u16*)d_in[0]; p.k = (const u16*)d_in[1]; p.v = (const u16*)d_in[2]; p.wq = (const u16*)d_in[3]; p.wo = (const u16*)d_in[4];
  p.wg = (const u16*)d_in[6]; p.wu = (const u16*)d_in[7]; p.wd = (const u16*)d_in[8];
  p.out = (u16*)d_out; p.ws = (unsigned char*)d_ws;
  hipLaunchKernelGGL(fused_decode_kernel, dim3(NWG), dim3(NTHR), 0, stream, p);
}
```

```cpp
#include <hip/hip_runtime.h>
#include <stdint.h>

#define GAS __attribute__((address_space(1)))
#define LAS __attribute__((address_space(3)))
typedef unsigned u32;
typedef unsigned long long u64;
typedef unsigned short u16;
typedef u32 u32x4 __attribute__((ext_vector_type(4)));
typedef u32 u32x2 __attribute__((ext_vector_type(2)));
typedef float f32x4 __attribute__((ext_vector_type(4)));
typedef float f32x2 __attribute__((ext_vector_type(2)));
typedef short s16x4 __attribute__((ext_vector_type(4)));
typedef __bf16 bf16x8 __attribute__((ext_vector_type(8)));
typedef __bf16 bf16x2 __attribute__((ext_vector_type(2)));

constexpr int NCODE = 21;
constexpr int PACE10 = 71;
constexpr int PACE_T0 = 250;
constexpr int PACE_FROM = 8;
constexpr int TOUCH = 1;
constexpr int DAHEAD = 3;
constexpr int DPRO = 1;
constexpr int TQ0 = 380, TQR = 48;
constexpr int TGS = 1000;
constexpr int TG15 = 950;
constexpr int TG2 = 1350;
constexpr int POLLSLP = 4, H1DLY = 18, H2DLY = 14;

constexpr int DM = 512, SK = 8192, DH = 128, FF = 1792;
constexpr int NWG = 256, NTHR = 512;
constexpr int RING = 8;
constexpr int NUNIT = 64;
constexpr float SC = 0.08838834764831845f * 1.4426950408889634f;
constexpr float THR_RAW = 8.0f / SC;

constexpr u32 WS_EPOCH = 0;
constexpr u32 WS_DUMMY = 2048;
constexpr u32 WS_START = 4096;
constexpr u32 WS_T0 = 8192;
constexpr u32 WS_Q = 65536;
constexpr u32 WS_GU = WS_Q + 8 * 8 * 4 * 512 * 8;
constexpr u32 WS_H = WS_GU + 28 * 2 * 4 * 8 * 64 * 8;
constexpr u32 WS_OP = WS_H + 8 * 1792 * 8;
constexpr u32 WS_ML = WS_OP + 32 * 8 * 4 * 64 * 8;
constexpr u32 WS_OUT = WS_ML + 32 * 8 * 4 * 2 * 8;
constexpr u32 WS_END = WS_OUT + 8 * 32 * 48 * 16 * 8;
static_assert(WS_END <= (8u << 20), "ws map");

constexpr u32 L_RING = 0;
constexpr u32 L_STAGE = 4 * RING * 4096;
constexpr u32 L_XN = L_STAGE + 16384;
constexpr u32 XN_STR = 1056;
constexpr u32 L_Q = L_XN + 8 * XN_STR;
constexpr u32 L_O = L_Q + 1024;
constexpr u32 L_FLAG = L_O + 2048;
constexpr u32 L_SCR = L_FLAG + 256;
constexpr u32 L_END = L_SCR + 1024;
static_assert(L_END <= 163840, "lds");
constexpr u32 F_SYNC = 0, F_Q = 4, F_SYNC2 = 8, F_WO = 12, F_EPOCH = 16, F_SYNC3 = 24, F_ANCH = 28, F_OUTP = 32;

constexpr u32 PER256 = (u32)(4096.0 * 1024.0 / (PACE10 * 0.1e12) * 1e8 * 256.0);
constexpr u32 SPIN_LDS = 1u << 22, SPIN_GLB = 1u << 17;

struct Params {
  const u16* x; const u16* k; const u16* v; const u16* wq; const u16* wo; const u16* wg; const u16* wu; const u16* wd;
  u16* out; unsigned char* ws;
};

__device__ __forceinline__ u32 rt32() { return (u32)__builtin_amdgcn_s_memrealtime(); }
__device__ __forceinline__ float bflo(u32 w) { return __uint_as_float(w << 16); }
__device__ __forceinline__ float bfhi(u32 w) { return __uint_as_float(w & 0xffff0000u); }
__device__ __forceinline__ u32 pk2(float lo, float hi) { f32x2 t = {lo, hi}; return __builtin_bit_cast(u32, __builtin_convertvector(t, bf16x2)); }
__device__ __forceinline__ float rbf(float f) { return bflo(pk2(f, f)); }
template <int CTRL> __device__ __forceinline__ float dpp(float x) {
  return __builtin_bit_cast(float, __builtin_amdgcn_mov_dpp(__builtin_bit_cast(int, x), CTRL, 0xf, 0xf, true));
}
__device__ __forceinline__ float xrow_max(float x) {
  auto s = __builtin_amdgcn_permlane16_swap(__float_as_uint(x), __float_as_uint(x), false, false);
  x = fmaxf(__uint_as_float(s[0]), __uint_as_float(s[1]));
  auto t = __builtin_amdgcn_permlane32_swap(__float_as_uint(x), __float_as_uint(x), false, false);
  return fmaxf(__uint_as_float(t[0]), __uint_as_float(t[1]));
}
__device__ __forceinline__ float xrow_sum(float x) {
  auto s = __builtin_amdgcn_permlane16_swap(__float_as_uint(x), __float_as_uint(x), false, false);
  x = __uint_as_float(s[0]) + __uint_as_float(s[1]);
  auto t = __builtin_amdgcn_permlane32_swap(__float_as_uint(x), __float_as_uint(x), false, false);
  return __uint_as_float(t[0]) + __uint_as_float(t[1]);
}
__device__ __forceinline__ float wave_sum(float x) {
  x += dpp<0xB1>(x); x += dpp<0x4E>(x); x += dpp<0x141>(x); x += dpp<0x128>(x);
  return xrow_sum(x);
}
__device__ __forceinline__ f32x4 mfma32(u32x4 a, u32x4 b, f32x4 c) {
  return __builtin_amdgcn_mfma_f32_16x16x32_bf16(__builtin_bit_cast(bf16x8, a), __builtin_bit_cast(bf16x8, b), c, 0, 0, 0);
}
__device__ __forceinline__ f32x4 mfma16(u32x2 a, u32x2 b, f32x4 c) {
  return __builtin_amdgcn_mfma_f32_16x16x16bf16_1k(__builtin_bit_cast(s16x4, a), __builtin_bit_cast(s16x4, b), c, 0, 0, 0);
}
__device__ __forceinline__ u32x2 trread(LAS unsigned char* p) {
  return __builtin_bit_cast(u32x2, __builtin_amdgcn_ds_read_tr16_b64_v4i16((LAS s16x4*)p));
}
__device__ __forceinline__ u32x4 trread2(LAS unsigned char* p0, LAS unsigned char* p1) {
  const u32x2 a = trread(p0), b = trread(p1); return u32x4{a.x, a.y, b.x, b.y};
}
__device__ __forceinline__ void dma4(u32 ldst, u32 v0, u32 v1, u32 v2, u32 v3, __amdgpu_buffer_rsrc_t srd, u32 soff) {
  u32 keep;
  asm volatile("s_waitcnt lgkmcnt(0)\n\ts_mov_b32 %0, m0\n\ts_mov_b32 m0, %1\n\ts_nop 0\n\t"
               "buffer_load_dwordx4 %2, %6, %7 offen nt lds\n\t"
               "buffer_load_dwordx4 %3, %6, %7 offen offset:1024 nt lds\n\t"
               "buffer_load_dwordx4 %4, %6, %7 offen offset:2048 nt lds\n\t"
               "buffer_load_dwordx4 %5, %6, %7 offen offset:3072 nt lds\n\t"
               "s_mov_b32 m0, %0"
               : "=&s"(keep) : "s"(ldst), "v"(v0), "v"(v1), "v"(v2), "v"(v3), "s"(srd), "s"(soff) : "memory");
}
__device__ __forceinline__ void dma1(u32 ldst, const void* gp) {
  u32 keep;
  asm volatile("s_mov_b32 %0, m0\n\ts_mov_b32 m0, %1\n\ts_nop 0\n\tglobal_load_lds_dwordx4 %2, off nt\n\ts_mov_b32 m0, %0"
               : "=&s"(keep) : "s"(ldst), "v"(gp) : "memory");
}
__device__ __forceinline__ void dma1p(u32 ldst, const void* gp) {
  u32 keep;
  asm volatile("s_mov_b32 %0, m0\n\ts_mov_b32 m0, %1\n\ts_nop 0\n\tglobal_load_lds_dwordx4 %2, off\n\ts_mov_b32 m0, %0"
               : "=&s"(keep) : "s"(ldst), "v"(gp) : "memory");
}
#define VMCNT(N) asm volatile("s_waitcnt vmcnt(" #N ")" ::: "memory")
#define LGKM0() asm volatile("s_waitcnt lgkmcnt(0)" ::: "memory")
#define NOUNROLL _Pragma("nounroll")
__device__ __forceinline__ void wait_units(int n) {
  switch (n) {
    case 0: VMCNT(0); break;   case 1: VMCNT(4); break;   case 2: VMCNT(8); break;   case 3: VMCNT(12); break;
    case 4: VMCNT(16); break;  case 5: VMCNT(20); break;  case 6: VMCNT(24); break;  default: VMCNT(28); break;
  }
}
__device__ __forceinline__ void bar() { LGKM0(); __builtin_amdgcn_s_barrier(); asm volatile("" ::: "memory"); }
__device__ __forceinline__ u32 lds_ld(LAS unsigned char* p) { return *(volatile LAS u32*)p; }
__device__ __forceinline__ void lds_add(LAS unsigned char* p, u32 v) { __hip_atomic_fetch_add((LAS u32*)p, v, __ATOMIC_RELAXED, __HIP_MEMORY_SCOPE_WORKGROUP); }
__device__ __forceinline__ void lds_wait_ge(LAS unsigned char* p, u32 want) {
  NOUNROLL
  for (u32 n = 0; n < SPIN_LDS; ++n) { if (lds_ld(p) >= want) break; __builtin_amdgcn_s_sleep(1); }
  asm volatile("" ::: "memory");
}
__device__ __forceinline__ void wait_until(u32 due) { while ((int)(rt32() - due) < 0) __builtin_amdgcn_s_sleep(4); }
__device__ __forceinline__ void st_gran(unsigned char* ws, u32 off, u32 lo, u32 hi) {
  __hip_atomic_store((GAS u64*)(ws + off), ((u64)hi << 32) | lo, __ATOMIC_RELAXED, __HIP_MEMORY_SCOPE_AGENT);
}
__device__ __forceinline__ u64 ld_gran(unsigned char* ws, u32 off) {
  return __hip_atomic_load((GAS u64*)(ws + off), __ATOMIC_RELAXED, __HIP_MEMORY_SCOPE_AGENT);
}

__global__ void __launch_bounds__(NTHR, 2) fused_decode_kernel(Params p) {
  __shared__ __attribute__((aligned(16))) unsigned char lds_raw[L_END];
  LAS unsigned char* L = (LAS unsigned char*)lds_raw;
  const u32 lbase = (u32)(size_t)L;
  const int tid = threadIdx.x, lane = tid & 63, wave = __builtin_amdgcn_readfirstlane(tid >> 6);
  const int g = lane >> 4, c = lane & 15;
  const int blk = blockIdx.x, xg = blk & 7, r = blk >> 3;
  const u32 t0 = rt32();
  u64 pc0; asm volatile("s_getpc_b64 %0" : "=s"(pc0));
  const __amdgpu_buffer_rsrc_t wsr = __builtin_amdgcn_make_buffer_rsrc(p.ws, 0, WS_END, 0x00020000);
  const int as = r & 3, ab = r >> 2, akv = xg, apair = ab * 8 + akv;
  const int whead = 4 * xg + (r & 3), wcc = r >> 2;

  if (wave < 4) {
    const size_t pbase = (size_t)apair * SK * DH;
    const __amdgpu_buffer_rsrc_t ksr = __builtin_amdgcn_make_buffer_rsrc((void*)(p.k + pbase), 0, SK * DH * 2, 0x00020000);
    const __amdgpu_buffer_rsrc_t vsr = __builtin_amdgcn_make_buffer_rsrc((void*)(p.v + pbase), 0, SK * DH * 2, 0x00020000);
    const u32 vk0 = g * 256 + 16 * (c ^ g), vk1 = g * 256 + 16 * (c ^ (4 + g)), vk2 = g * 256 + 16 * (c ^ (8 + g)), vk3 = g * 256 + 16 * (c ^ (12 + g));
    const u32 vv0 = g * 256 + 16 * (c ^ (g << 1)), vv1 = g * 256 + 16 * (c ^ ((4 + g) << 1));
    const u32 ring = lbase + L_RING + wave * (RING * 4096);
    int nis = 0;
    int landed = 0;
    u32 tnow = t0, pA = t0 + PACE_T0;
    const u32 sbase = as * 16384u + wave * 4096u;
    auto issue = [&]() {
      if (nis >= PACE_FROM) {
        if (nis < PACE_FROM + 6) { const u32 an = __builtin_amdgcn_readfirstlane(lds_ld(L + L_FLAG + F_ANCH)); if (an) pA = an + PACE_T0; }
        const u32 due = pA + (((u32)nis * PER256) >> 8);
        if ((int)(tnow - due) < 0) {
          NOUNROLL
          for (u32 n = 0; n < SPIN_LDS; ++n) { tnow = rt32(); if ((int)(tnow - due) >= 0) break; __builtin_amdgcn_s_sleep(1); }
        }
      }
      const u32 soff = (u32)(nis >> 1) * 65536u + sbase, ldst = ring + (u32)(nis & (RING - 1)) * 4096u;
      if (nis & 1) dma4(ldst, vv0, vv1, vv0, vv1, vsr, soff); else dma4(ldst, vk0, vk1, vk2, vk3, ksr, soff);
      ++nis;
    };
    issue();
    bar();
    NOUNROLL
    while (nis < RING) {
      if (nis >= DPRO) { wait_units(DPRO - 1); landed = nis - (DPRO - 1); }
      if (lds_ld(L + L_FLAG + F_Q) >= 4) break;
      issue();
    }
    auto refill = [&](int cons) { while (nis - landed < DAHEAD && nis < cons + RING && nis < NUNIT) issue(); };
    lds_wait_ge(L + L_FLAG + F_Q, 4);
    u32x4 qf[4];
#pragma unroll
    for (int s = 0; s < 4; ++s) qf[s] = *(LAS u32x4*)(L + L_Q + (c & 3) * 256 + 64 * s + 16 * g);
    refill(0);
    LAS unsigned char* R = L + L_RING + wave * (RING * 4096);
    const u32 ka = c * 256 + 16 * (g ^ c);
    const u32 va = (4 * g + (c >> 2)) * 256 + 32 * ((4 * g + (c >> 2)) & 7) + 8 * (c & 3);
    float m = -INFINITY, lsum = 0.f;
    f32x4 acc[8];
#pragma unroll
    for (int i = 0; i < 8; ++i) acc[i] = f32x4{0.f, 0.f, 0.f, 0.f};
    NOUNROLL
    for (int j = 0; j < NUNIT / 2; ++j) {
      wait_units(nis - 2 * j - 1);
      landed = landed > 2 * j + 1 ? landed : 2 * j + 1;
      refill(2 * j);
      LAS unsigned char* Kt = R + ((2 * j) & (RING - 1)) * 4096;
      LAS unsigned char* Vt = R + ((2 * j + 1) & (RING - 1)) * 4096;
      f32x4 sa = {0.f, 0.f, 0.f, 0.f}, sb = {0.f, 0.f, 0.f, 0.f};
      {
        const u32x4 k0 = *(LAS u32x4*)(Kt + ka), k1 = *(LAS u32x4*)(Kt + (ka ^ 64)), k2 = *(LAS u32x4*)(Kt + (ka ^ 128)), k3 = *(LAS u32x4*)(Kt + (ka ^ 192));
        sa = mfma32(k0, qf[0], sa); sb = mfma32(k1, qf[1], sb); sa = mfma32(k2, qf[2], sa); sb = mfma32(k3, qf[3], sb);
      }
      const f32x4 sc = sa + sb;
      const float tmax = fmaxf(fmaxf(sc[0], sc[1]), fmaxf(sc[2], sc[3]));
      if (__builtin_amdgcn_ballot_w64(tmax > m + THR_RAW) != 0ull) {
        const float mn = fmaxf(m, xrow_max(tmax));
        const float al = __builtin_amdgcn_exp2f((m - mn) * SC);
        m = mn; lsum *= al;
        const float a0 = dpp<0x00>(al), a1 = dpp<0x55>(al), a2 = dpp<0xAA>(al), a3 = dpp<0xFF>(al);
#pragma unroll
        for (int i = 0; i < 8; ++i) { acc[i][0] *= a0; acc[i][1] *= a1; acc[i][2] *= a2; acc[i][3] *= a3; }
      }
      const float mc = m * SC;
      const float p0 = __builtin_amdgcn_exp2f(fmaf(sc[0], SC, -mc)), p1 = __builtin_amdgcn_exp2f(fmaf(sc[1], SC, -mc));
      const float p2 = __builtin_amdgcn_exp2f(fmaf(sc[2], SC, -mc)), p3 = __builtin_amdgcn_exp2f(fmaf(sc[3], SC, -mc));
      lsum += (p0 + p1) + (p2 + p3);
      const u32x2 pa = {pk2(p0, p1), pk2(p2, p3)};
      wait_units(nis - 2 * j - 2);
      landed = landed > 2 * j + 2 ? landed : 2 * j + 2;
      refill(2 * j + 1);
#pragma unroll
      for (int i = 0; i < 8; ++i) acc[i] = mfma16(pa, trread(Vt + (va ^ (32 * i))), acc[i]);
      refill(2 * j + 2);
    }

    lsum = xrow_sum(lsum);
    if (g == 0) {
#pragma unroll
      for (int i = 0; i < 8; ++i) *(LAS f32x4*)(R + i * 256 + c * 16) = acc[i];
      if (c < 4) *(LAS f32x2*)(R + 2048 + c * 8) = f32x2{m * SC, lsum};
    }
  } else {
    const int hw = wave - 4;
    __builtin_amdgcn_s_setprio(3);
    u32x4 xa, xb; u32x2 ev;
    if (hw == 0 && lane < 16) *(LAS u32x4*)(L + L_FLAG + lane * 16) = u32x4{0, 0, 0, 0};
    const int kq = r & 3;
    if (NCODE && r * 4 + hw < NCODE) {
      dma1p(lbase + L_SCR, (const char*)(pc0 & ~255ull) + (r * 4 + hw) * 1024 + lane * 16);
    }
    {
      const u16* xp = p.x + (2 * hw) * DM + lane * 8;
      const u16* wp = p.wq + (size_t)(kq * 128 + hw * 32 + (lane >> 3)) * 4096 + xg * 512 + (r >> 2) * 64 + (lane & 7) * 8;
      const u32 ldst = lbase + L_STAGE + hw * 4096;
      u32 keep;
      asm volatile("global_load_dwordx4 %0, %4, off\n\tglobal_load_dwordx4 %1, %4, off offset:1024\n\tglobal_load_dwordx2 %2, %5, off\n\t"
                   "s_mov_b32 %3, m0\n\ts_mov_b32 m0, %10\n\ts_nop 0\n\t"
                   "global_load_lds_dwordx4 %6, off nt\n\ts_add_u32 m0, m0, 0x400\n\ts_nop 0\n\t"
                   "global_load_lds_dwordx4 %7, off nt\n\ts_add_u32 m0, m0, 0x400\n\ts_nop 0\n\t"
                   "global_load_lds_dwordx4 %8, off nt\n\ts_add_u32 m0, m0, 0x400\n\ts_nop 0\n\t"
                   "global_load_lds_dwordx4 %9, off nt\n\ts_mov_b32 m0, %3\n\t"
                   "s_waitcnt lgkmcnt(0)\n\ts_barrier\n\ts_waitcnt vmcnt(4)"
                   : "=&v"(xa), "=&v"(xb), "=&v"(ev), "=&s"(keep)
                   : "v"(xp), "v"(p.ws + WS_EPOCH), "v"(wp), "v"(wp + (size_t)8 * 4096), "v"(wp + (size_t)16 * 4096), "v"(wp + (size_t)24 * 4096), "s"(ldst)
                   : "memory", "scc");
    }
    const u64 ep = (((u64)ev.y << 32) | ev.x) + 1ull;
    const u32 tag32 = (u32)ep, t48lo = ((u32)ep) << 16, t48hi = (u32)(ep >> 16), t40lo = ((u32)ep) << 24, t40hi = (u32)(ep >> 8);
#pragma unroll
    for (int q = 0; q < 2; ++q) {
      const u32x4 xv = q ? xb : xa;
      float v[8];
#pragma unroll
      for (int i = 0; i < 4; ++i) { v[2 * i] = bflo(xv[i]); v[2 * i + 1] = bfhi(xv[i]); }
      const float mean = wave_sum(((v[0] + v[1]) + (v[2] + v[3])) + ((v[4] + v[5]) + (v[6] + v[7]))) * (1.f / DM);
      float q2 = 0.f;
#pragma unroll
      for (int i = 0; i < 8; ++i) { const float d = v[i] - mean; q2 = fmaf(d, d, q2); }
      const float var = rbf(wave_sum(q2) * (1.f / DM));
      const float rden = __builtin_amdgcn_rcpf(rbf(__builtin_amdgcn_sqrtf(rbf(var + 1e-5f))));
      const float mb = rbf(mean);
      u32x4 o;
#pragma unroll
      for (int i = 0; i < 4; ++i) { const u32 d = pk2(v[2 * i] - mb, v[2 * i + 1] - mb); o[i] = pk2(bflo(d) * rden, bfhi(d) * rden); }
      *(LAS u32x4*)(L + L_XN + (2 * hw + q) * XN_STR + lane * 16) = o;
    }
    const int tq = c >> 2, tp = c & 3;
    auto tile_mma = [&](const u32x4 (&act)[4], int cg) {
      f32x4 a0 = {0.f, 0.f, 0.f, 0.f}, a1 = {0.f, 0.f, 0.f, 0.f};
      LAS unsigned char* ap = L + L_STAGE + (8 * g + tq) * 128 + 32 * cg + 8 * tp;
      a0 = mfma32(act[0], trread2(ap, ap + 512), a0);
      a1 = mfma32(act[1], trread2(ap + 4096, ap + 4096 + 512), a1);
      a0 = mfma32(act[2], trread2(ap + 8192, ap + 8192 + 512), a0);
      a1 = mfma32(act[3], trread2(ap + 12288, ap + 12288 + 512), a1);
      return a0 + a1;
    };
    auto xn_frags = [&](u32x4 (&act)[4], int k0) {
#pragma unroll
      for (int s = 0; s < 4; ++s) act[s] = *(LAS u32x4*)(L + L_XN + (c & 7) * XN_STR + 2 * k0 + 64 * s + 16 * g);
    };
    auto f24 = [&](float f) { const u32 u = __float_as_uint(f); return t40lo | ((u + 0x7fu + ((u >> 8) & 1u)) >> 8); };
    auto ok40 = [&](const u32x4& t) { return ((t[0] ^ t40lo) >> 24) == 0 && t[1] == t40hi && ((t[2] ^ t40lo) >> 24) == 0 && t[3] == t40hi; };
    auto ok48 = [&](const u32x4& t) { return ((t[0] ^ t48lo) >> 16) == 0 && t[1] == t48hi && ((t[2] ^ t48lo) >> 16) == 0 && t[3] == t48hi; };
    u32x4 act[4];
    asm volatile("s_waitcnt vmcnt(0) lgkmcnt(0)" ::: "memory");
    if (lane == 0) lds_add(L + L_FLAG + F_SYNC, 1u);
    lds_wait_ge(L + L_FLAG + F_SYNC, 4);
    xn_frags(act, kq * 128);
    {
      const f32x4 d = tile_mma(act, hw);
      if (g < 2) {
#pragma unroll
        for (int i = 0; i < 4; ++i) st_gran(p.ws, WS_Q + ((((xg * 8 + 4 * g + i) * 4 + kq) * 512) + (r >> 2) * 64 + 16 * hw + c) * 8, f24(d[i]), t40hi);
      }
    }
    if (hw == 1 && lane == 0) st_gran(p.ws, WS_T0 + blk * 8, t40lo | (t0 & 0xffffffu), t40hi);
    LGKM0();
    if (lane == 0) lds_add(L + L_FLAG + F_SYNC, 1u);
    if (hw < 2) {
      const u32 qb = WS_Q + ((akv * 8 + ab) * 4) * 512 * 8;
      wait_until(t0 + TQ0 - (u32)(r * TQR) / 16);
      float q0[2], q1[2];
      NOUNROLL
      for (u32 n = 0; n < SPIN_GLB; ++n) {
        bool ok = true;
#pragma unroll
        for (int h = 0; h < 2; ++h) {
          q0[h] = 0.f; q1[h] = 0.f;
#pragma unroll
          for (int i = 0; i < 4; ++i) {
            const u32x4 t = __builtin_amdgcn_raw_buffer_load_b128(wsr, qb + (i * 512 + (2 * hw + h) * 128 + 2 * lane) * 8, 0, 16);
            ok &= ok40(t); q0[h] += __uint_as_float(t[0] << 8); q1[h] += __uint_as_float(t[2] << 8);
          }
        }
        if (__all(ok)) break;
        __builtin_amdgcn_s_sleep(POLLSLP);
        asm volatile("" ::: "memory");
      }
      *(LAS u32*)(L + L_Q + (2 * hw) * 256 + lane * 4) = pk2(q0[0], q1[0]);
      *(LAS u32*)(L + L_Q + (2 * hw + 1) * 256 + lane * 4) = pk2(q0[1], q1[1]);
      LGKM0();
      if (lane == 0) lds_add(L + L_FLAG + F_Q, 2u);
      if (TOUCH && hw == 1) {
        const u16* tp = (lane < 32 ? p.k : p.v) + (size_t)apair * SK * DH + (size_t)(lane & 31) * 32768 + as * 8192 + 4096;
        u32 keep; const u32 ld_ = lbase + L_SCR;
        if ((lane & 31) >= 4)
          asm volatile("s_mov_b32 %0, m0\n\ts_mov_b32 m0, %1\n\ts_nop 0\n\tglobal_load_lds_dword %2, off nt\n\ts_mov_b32 m0, %0" : "=&s"(keep) : "s"(ld_), "v"(tp) : "memory");
      }
      if (hw == 0) {
        u64 ts = 0;
        bool ok = false;
        NOUNROLL
        for (u32 n = 0; n < 64 && !ok; ++n) {
          ts = ld_gran(p.ws, WS_T0 + (xg + 8 * (lane & 31)) * 8);
          ok = __all((((u32)ts ^ t40lo) >> 24) == 0 && (u32)(ts >> 32) == t40hi);
          if (!ok) __builtin_amdgcn_s_sleep(8);
        }
        int d = ((int)(((u32)ts - t0) << 8)) >> 8;
        d = min(d, __builtin_amdgcn_mov_dpp(d, 0xB1, 0xf, 0xf, true)); d = min(d, __builtin_amdgcn_mov_dpp(d, 0x4E, 0xf, 0xf, true));
        d = min(d, __builtin_amdgcn_mov_dpp(d, 0x141, 0xf, 0xf, true)); d = min(d, __builtin_amdgcn_mov_dpp(d, 0x128, 0xf, 0xf, true));
        int dm = min(min(__builtin_amdgcn_readlane(d, 0), __builtin_amdgcn_readlane(d, 16)), 0);
        dm = dm < -400 ? -400 : dm;
        if (ok && lane == 0) *(volatile LAS u32*)(L + L_FLAG + F_ANCH) = (t0 + dm) | 1u;
      }
      if (hw == 1) {
        wait_until(t0 + TGS);
        NOUNROLL
        for (u32 n = 0; n < SPIN_GLB; ++n) {
          const u32x4 a = __builtin_amdgcn_raw_buffer_load_b128(wsr, WS_START + lane * 16, 0, 16);
          const u32x4 b = __builtin_amdgcn_raw_buffer_load_b128(wsr, WS_START + 1024 + lane * 16, 0, 16);
          const u32 e0 = (u32)ep, e1 = (u32)(ep >> 32);
          const bool ok = a[0] == e0 && a[1] == e1 && a[2] == e0 && a[3] == e1 && b[0] == e0 && b[1] == e1 && b[2] == e0 && b[3] == e1;
          if (__all(ok)) break;
          __builtin_amdgcn_s_sleep(32);
          asm volatile("" ::: "memory");
        }
      }
    } else {
      const int h2 = hw - 2;
      u32 nsync2 = 0;
      auto arrive2 = [&]() {
        LGKM0();
        if (lane == 0) lds_add(L + L_FLAG + F_SYNC2, 1u);
        nsync2 += 2;
        lds_wait_ge(L + L_FLAG + F_SYNC2, nsync2);
      };
      auto stage2 = [&](const u16* w, int ld) {
        const u16* wp = w + (size_t)(h2 * 64 + (lane >> 3)) * ld + (lane & 7) * 8;
#pragma unroll
        for (int i = 0; i < 8; ++i) dma1(lbase + L_STAGE + h2 * 8192 + i * 1024, wp + (size_t)i * 8 * ld);
      };
      if (h2 == 1) {
        const u32x4 z = {0u, ~tag32, 0u, ~tag32};
#pragma unroll
        for (int i = 0; i < 2; ++i)
          __builtin_amdgcn_raw_buffer_store_b128(z, wsr, WS_OP + ((((akv * 4 + 2 * i + (lane >> 5)) * 8 + ab) * 4 + as) * 64) * 8 + (lane & 31) * 16, 0, 16);
        if (lane < 4) __builtin_amdgcn_raw_buffer_store_b128(z, wsr, WS_ML + (((akv * 4 + lane) * 8 + ab) * 4 + as) * 16, 0, 16);
      }
      lds_wait_ge(L + L_FLAG + F_SYNC, 8);
      const bool f1 = r < 14, f15 = r >= 14 && r < 18, f2 = r >= 18;
      const int t1 = xg * 14 + r, sl1 = t1 >> 2, fq = t1 & 3, ks = r - 18;
      const u16* wo_tile = p.wo + (size_t)whead * 128 * DM + wcc * 64;
      if (f1) stage2(p.wg + (size_t)fq * 128 * FF + sl1 * 64, FF);
      else if (f2) stage2(p.wd + (size_t)ks * 128 * DM + xg * 64, DM);
      else stage2(wo_tile, DM);
      VMCNT(0);
      if (h2 == 1 && lane == 0) {
        __hip_atomic_store((GAS u64*)(p.ws + WS_START + blk * 8), ep, __ATOMIC_RELAXED, __HIP_MEMORY_SCOPE_AGENT);
        *(volatile LAS u64*)(L + L_FLAG + F_EPOCH) = ep;
      }
      arrive2();
      if (f1) {
        xn_frags(act, fq * 128);
        f32x4 d[2];
        d[0] = tile_mma(act, 2 * h2); d[1] = tile_mma(act, 2 * h2 + 1);
        asm volatile("" : "+v"(d[0]), "+v"(d[1]));
        arrive2();
        stage2(p.wu + (size_t)fq * 128 * FF + sl1 * 64, FF);
#pragma unroll
        for (int w = 0; w < 2; ++w) if (g < 2) {
#pragma unroll
          for (int i = 0; i < 4; ++i) st_gran(p.ws, WS_GU + ((((sl1 * 2 + 0) * 4 + fq) * 8 + 4 * g + i) * 64 + 16 * (2 * h2 + w) + c) * 8, f24(d[w][i]), t40hi);
        }
        VMCNT(8);
        arrive2();
        d[0] = tile_mma(act, 2 * h2); d[1] = tile_mma(act, 2 * h2 + 1);
        asm volatile("" : "+v"(d[0]), "+v"(d[1]));
        arrive2();
        stage2(wo_tile, DM);
#pragma unroll
        for (int w = 0; w < 2; ++w) if (g < 2) {
#pragma unroll
          for (int i = 0; i < 4; ++i) st_gran(p.ws, WS_GU + ((((sl1 * 2 + 1) * 4 + fq) * 8 + 4 * g + i) * 64 + 16 * (2 * h2 + w) + c) * 8, f24(d[w][i]), t40hi);
        }
        VMCNT(8);
        arrive2();
      } else if (f15) {
        const int sl = xg * 4 + (r - 14);
        if (sl < 28) {
          wait_until(t0 + TG15);
          {
            const u32 so = WS_GU + ((((sl * 2 + 1) * 4 + (lane & 3)) * 8) * 64 + 63) * 8;
            NOUNROLL
            for (u32 n = 0; n < SPIN_GLB; ++n) {
              const u64 t = ld_gran(p.ws, so);
              if (__all((((u32)t ^ t40lo) >> 24) == 0 && (u32)(t >> 32) == t40hi)) break;
              __builtin_amdgcn_s_sleep(16);
            }
          }
          float gs[2][2], us[2][2];
          const int cp = lane & 31;
          NOUNROLL
          for (u32 n = 0; n < SPIN_GLB; ++n) {
            bool ok = true;
#pragma unroll
            for (int bb = 0; bb < 2; ++bb) {
              const int b = 4 * h2 + 2 * bb + (lane >> 5);
              gs[bb][0] = gs[bb][1] = us[bb][0] = us[bb][1] = 0.f;
#pragma unroll
              for (int i = 0; i < 4; ++i) {
                const u32x4 tg = __builtin_amdgcn_raw_buffer_load_b128(wsr, WS_GU + ((((sl * 2 + 0) * 4 + i) * 8 + b) * 64 + 2 * cp) * 8, 0, 16);
                const u32x4 tu = __builtin_amdgcn_raw_buffer_load_b128(wsr, WS_GU + ((((sl * 2 + 1) * 4 + i) * 8 + b) * 64 + 2 * cp) * 8, 0, 16);
                ok &= ok40(tg) && ok40(tu);
                gs[bb][0] += __uint_as_float(tg[0] << 8); gs[bb][1] += __uint_as_float(tg[2] << 8);
                us[bb][0] += __uint_as_float(tu[0] << 8); us[bb][1] += __uint_as_float(tu[2] << 8);
              }
            }
            if (__all(ok)) break;
            __builtin_amdgcn_s_sleep(16);
            asm volatile("" ::: "memory");
          }
#pragma unroll
          for (int bb = 0; bb < 2; ++bb) {
            const int b = 4 * h2 + 2 * bb + (lane >> 5);
            const u32 hp = pk2(rbf(gs[bb][0]) * rbf(us[bb][0]), rbf(gs[bb][1]) * rbf(us[bb][1]));
            st_gran(p.ws, WS_H + (b * FF + sl * 64 + 2 * cp) * 8, t48lo | (hp & 0xffffu), t48hi);
            st_gran(p.ws, WS_H + (b * FF + sl * 64 + 2 * cp + 1) * 8, t48lo | (hp >> 16), t48hi);
          }
        }
      } else {
        wait_until(t0 + TG2);
        {
          const u32 so = WS_H + (7 * FF + ks * 128 + 64 * (lane & 1) + 63) * 8;
          NOUNROLL
          for (u32 n = 0; n < SPIN_GLB; ++n) {
            const u64 t = ld_gran(p.ws, so);
            if (__all((((u32)t ^ t48lo) >> 16) == 0 && (u32)(t >> 32) == t48hi)) break;
            __builtin_amdgcn_s_sleep(32);
          }
        }
        const int b = lane >> 3, k0 = 64 * h2 + 8 * (lane & 7);
        u32x4 hv = {0, 0, 0, 0};
        NOUNROLL
        for (u32 n = 0; n < SPIN_GLB; ++n) {
          bool ok = true;
#pragma unroll
          for (int i = 0; i < 4; ++i) {
            const u32x4 t = __builtin_amdgcn_raw_buffer_load_b128(wsr, WS_H + (b * FF + ks * 128 + k0 + 2 * i) * 8, 0, 16);
            ok &= ok48(t);
            hv[i] = (t[0] & 0xffffu) | (t[2] << 16);
          }
          if (__all(ok)) break;
          __builtin_amdgcn_s_sleep(16);
          asm volatile("" ::: "memory");
        }
        *(LAS u32x4*)(L + L_O + b * 256 + k0 * 2) = hv;
        arrive2();
#pragma unroll
        for (int s = 0; s < 4; ++s) act[s] = *(LAS u32x4*)(L + L_O + (c & 7) * 256 + 64 * s + 16 * g);
        f32x4 d[2];
        d[0] = tile_mma(act, 2 * h2); d[1] = tile_mma(act, 2 * h2 + 1);
        asm volatile("" : "+v"(d[0]), "+v"(d[1]));
        arrive2();
        stage2(wo_tile, DM);
#pragma unroll
        for (int w = 0; w < 2; ++w) if (g < 2) {
#pragma unroll
          for (int i = 0; i < 4; ++i) st_gran(p.ws, WS_OUT + ((((4 * g + i) * 32 + 4 * xg + 2 * h2 + w) * 48) + 32 + ks) * 128 + c * 8, f24(d[w][i]), t40hi);
        }
        VMCNT(8);
        arrive2();
      }
      if (h2 == 0 && lane == 0) *(volatile LAS u32*)(L + L_FLAG + F_WO) = 1;
    }
    NOUNROLL
    for (u32 n = 0; n < SPIN_LDS; ++n) { if (lds_ld(L + L_FLAG + F_WO) >= 1) break; __builtin_amdgcn_s_sleep(16); }
    asm volatile("" ::: "memory");
  }

  u32x4 wof[4];
  if (wave >= 4) {
    const int tq = c >> 2, tp = c & 3, w = wave - 4;
#pragma unroll
    for (int s = 0; s < 4; ++s) { LAS unsigned char* ap = L + L_STAGE + (32 * s + 8 * g + tq) * 128 + 32 * w + 8 * tp; wof[s] = trread2(ap, ap + 512); }
  }
  bar();
  const u64 ep = *(volatile LAS u64*)(L + L_FLAG + F_EPOCH);
  const u32 tag32 = (u32)ep, t40lo = ((u32)ep) << 24, t40hi = (u32)(ep >> 8);
  if (wave < 4) {
    const int hh = wave, ch = lane >> 3, c0 = (2 * lane) & 15;
    float mw[4], lw[4], v0[4], v1[4];
#pragma unroll
    for (int w = 0; w < 4; ++w) {
      LAS unsigned char* R = L + L_RING + w * (RING * 4096);
      const f32x2 ml = *(LAS f32x2*)(R + 2048 + hh * 8);
      mw[w] = ml[0]; lw[w] = ml[1];
      v0[w] = *(LAS float*)(R + ch * 256 + c0 * 16 + hh * 4);
      v1[w] = *(LAS float*)(R + ch * 256 + c0 * 16 + 16 + hh * 4);
    }
    const float M = fmaxf(fmaxf(mw[0], mw[1]), fmaxf(mw[2], mw[3]));
    float Ls = 0.f, o0 = 0.f, o1 = 0.f;
#pragma unroll
    for (int w = 0; w < 4; ++w) { const float f = __builtin_amdgcn_exp2f(mw[w] - M); Ls = fmaf(f, lw[w], Ls); o0 = fmaf(f, v0[w], o0); o1 = fmaf(f, v1[w], o1); }
    const float inv = __builtin_amdgcn_rcpf(Ls);
    const u32 slot = ((akv * 4 + hh) * 8 + ab) * 4 + as;
    st_gran(p.ws, WS_OP + (slot * 64 + lane) * 8, pk2(o0 * inv, o1 * inv), tag32);
    if (lane < 2) st_gran(p.ws, WS_ML + (slot * 2 + lane) * 8, __float_as_uint(lane ? Ls : M), tag32);
  } else {
    const int w = wave - 4;
    const u32 slot = (whead * 8 + 2 * w) * 4;
    u64 o[8] = {0, 0, 0, 0, 0, 0, 0, 0}, ml = 0;
    __builtin_amdgcn_s_sleep(H1DLY);
    {
      u32 have = 0;
      NOUNROLL
      for (u32 n = 0; n < SPIN_GLB; ++n) {
        u64 t[9];
#pragma unroll
        for (int s = 0; s < 8; ++s) t[s] = ld_gran(p.ws, ((have >> s) & 1) ? WS_DUMMY : WS_OP + ((slot + s) * 64 + lane) * 8);
        t[8] = ld_gran(p.ws, ((have >> 8) & 1) ? WS_DUMMY : WS_ML + (slot * 2 + (lane & 15)) * 8);
#pragma unroll
        for (int s = 0; s < 9; ++s) {
          const bool fresh = !((have >> s) & 1);
          if (s < 8) o[s] = fresh ? t[s] : o[s]; else ml = fresh ? t[8] : ml;
          if (__all((u32)(t[s] >> 32) == tag32)) have |= 1u << s;
        }
        if (have == 0x1ffu) break;
        __builtin_amdgcn_s_sleep(POLLSLP);
      }
    }
    const u32 mlv = (u32)ml;
#pragma unroll
    for (int bb = 0; bb < 2; ++bb) {
      float ms[4], ls[4];
#pragma unroll
      for (int s = 0; s < 4; ++s) { ms[s] = __uint_as_float(__builtin_amdgcn_readlane(mlv, 8 * bb + 2 * s)); ls[s] = __uint_as_float(__builtin_amdgcn_readlane(mlv, 8 * bb + 2 * s + 1)); }
      const float M = fmaxf(fmaxf(ms[0], ms[1]), fmaxf(ms[2], ms[3]));
      float W = 0.f, o0 = 0.f, o1 = 0.f;
#pragma unroll
      for (int s = 0; s < 4; ++s) { const float wt = ls[s] * __builtin_amdgcn_exp2f(ms[s] - M); W += wt; o0 = fmaf(wt, bflo((u32)o[4 * bb + s]), o0); o1 = fmaf(wt, bfhi((u32)o[4 * bb + s]), o1); }
      const float inv = __builtin_amdgcn_rcpf(W);
      *(LAS u32*)(L + L_O + (2 * w + bb) * 256 + lane * 4) = pk2(o0 * inv, o1 * inv);
    }
    LGKM0();
    if (lane == 0) lds_add(L + L_FLAG + F_SYNC3, 1u);
    NOUNROLL
    for (u32 n = 0; n < SPIN_LDS; ++n) { if (lds_ld(L + L_FLAG + F_SYNC3) >= 4) break; }
    asm volatile("" ::: "memory");
    f32x4 a = {0.f, 0.f, 0.f, 0.f}, a2 = {0.f, 0.f, 0.f, 0.f};
    a = mfma32(*(LAS u32x4*)(L + L_O + (c & 7) * 256 + 16 * g), wof[0], a);
    a2 = mfma32(*(LAS u32x4*)(L + L_O + (c & 7) * 256 + 64 + 16 * g), wof[1], a2);
    a = mfma32(*(LAS u32x4*)(L + L_O + (c & 7) * 256 + 128 + 16 * g), wof[2], a);
    a2 = mfma32(*(LAS u32x4*)(L + L_O + (c & 7) * 256 + 192 + 16 * g), wof[3], a2);
    a += a2;
    if (g < 2) {
#pragma unroll
      for (int i = 0; i < 4; ++i) {
        const u32 u = __float_as_uint(a[i]);
        st_gran(p.ws, WS_OUT + ((((4 * g + i) * 32 + 4 * wcc + w) * 48) + whead) * 128 + c * 8, t40lo | ((u + 0x7fu + ((u >> 8) & 1u)) >> 8), t40hi);
      }
    }
    if (lane == 0) lds_add(L + L_FLAG + F_OUTP, 1u);
  }
  if (wave == 0) {
    const float xres = bflo(p.x[(r >> 2) * DM + 16 * (4 * xg + (r & 3)) + c]);
    const u32 base = WS_OUT + (((r >> 2) * 32 + 4 * xg + (r & 3)) * 48) * 128 + c * 8;
    u64 t[12] = {0, 0, 0, 0, 0, 0, 0, 0, 0, 0, 0, 0};
    lds_wait_ge(L + L_FLAG + F_OUTP, 4);
    __builtin_amdgcn_s_sleep(H2DLY);
    {
      u32 have = 0;
      NOUNROLL
      for (u32 n = 0; n < SPIN_GLB; ++n) {
        u64 tt[12];
#pragma unroll
        for (int i = 0; i < 12; ++i) { const int pr = (4 * i + g < 46) ? 4 * i + g : 45; tt[i] = ld_gran(p.ws, ((have >> i) & 1) ? WS_DUMMY : base + pr * 128); }
#pragma unroll
        for (int i = 0; i < 12; ++i) {
          t[i] = ((have >> i) & 1) ? t[i] : tt[i];
          if (__all((((u32)tt[i] ^ t40lo) >> 24) == 0 && (u32)(tt[i] >> 32) == t40hi)) have |= 1u << i;
        }
        if (have == 0xfffu) break;
        __builtin_amdgcn_s_sleep(POLLSLP);
      }
    }
    float sa = 0.f, sf = 0.f;
#pragma unroll
    for (int i = 0; i < 8; ++i) sa += __uint_as_float((u32)t[i] << 8);
#pragma unroll
    for (int i = 8; i < 12; ++i) sf += (4 * i + g < 46) ? __uint_as_float((u32)t[i] << 8) : 0.f;
    sa = xrow_sum(sa); sf = xrow_sum(sf);
    const float res = rbf(xres + rbf(sa)) + rbf(sf);
    if (g == 0) p.out[(r >> 2) * DM + 16 * (4 * xg + (r & 3)) + c] = (u16)(pk2(res, 0.f) & 0xffffu);
    if (blk == 0 && lane == 0) __hip_atomic_store((GAS u64*)(p.ws + WS_EPOCH), ep, __ATOMIC_RELAXED, __HIP_MEMORY_SCOPE_AGENT);
  }
}

extern "C" void kernel_launch(void* const* d_in, const int* in_sizes, int n_in, void* d_out, int out_size, void* d_ws, size_t ws_size, hipStream_t stream) {
  Params p;
  p.x = (const u16*)d_in[0]; p.k = (const u16*)d_in[1]; p.v = (const u16*)d_in[2]; p.wq = (const u16*)d_in[3]; p.wo = (const u16*)d_in[4];
  p.wg = (const u16*)d_in[6]; p.wu = (const u16*)d_in[7]; p.wd = (const u16*)d_in[8];
  p.out = (u16*)d_out; p.ws = (unsigned char*)d_ws;
  hipLaunchKernelGGL(fused_decode_kernel, dim3(NWG), dim3(NTHR), 0, stream, p);
}
```

```cpp
#define TOUCHM 1
#define TLK 4
#include <hip/hip_runtime.h>
#include <stdint.h>

#define GAS __attribute__((address_space(1)))
#define LAS __attribute__((address_space(3)))
typedef unsigned u32;
typedef unsigned long long u64;
typedef unsigned short u16;
typedef u32 u32x4 __attribute__((ext_vector_type(4)));
typedef u32 u32x2 __attribute__((ext_vector_type(2)));
typedef float f32x4 __attribute__((ext_vector_type(4)));
typedef float f32x2 __attribute__((ext_vector_type(2)));
typedef short s16x4 __attribute__((ext_vector_type(4)));
typedef __bf16 bf16x8 __attribute__((ext_vector_type(8)));
typedef __bf16 bf16x2 __attribute__((ext_vector_type(2)));

constexpr int NCODE = 21;
constexpr int PACE10 = 71;
constexpr int PACE_T0 = 250;
constexpr int PACE_FROM = 8;
constexpr int TOUCH = TOUCHM, TLOOK = TLK;
constexpr int DAHEAD = 3;
constexpr int DPRO = 1;
constexpr int TQ0 = 380, TQR = 48;
constexpr int TGS = 1000;
constexpr int TG15 = 950;
constexpr int TG2 = 1350;
constexpr int POLLSLP = 4, H1DLY = 18, H2DLY = 14;

constexpr int DM = 512, SK = 8192, DH = 128, FF = 1792;
constexpr int NWG = 256, NTHR = 512;
constexpr int RING = 8;
constexpr int NUNIT = 64;
constexpr float SC = 0.08838834764831845f * 1.4426950408889634f;
constexpr float THR_RAW = 8.0f / SC;

constexpr u32 WS_EPOCH = 0;
constexpr u32 WS_DUMMY = 2048;
constexpr u32 WS_START = 4096;
constexpr u32 WS_T0 = 8192;
constexpr u32 WS_Q = 65536;
constexpr u32 WS_GU = WS_Q + 8 * 8 * 4 * 512 * 8;
constexpr u32 WS_H = WS_GU + 28 * 2 * 4 * 8 * 64 * 8;
constexpr u32 WS_OP = WS_H + 8 * 1792 * 8;
constexpr u32 WS_ML = WS_OP + 32 * 8 * 4 * 64 * 8;
constexpr u32 WS_OUT = WS_ML + 32 * 8 * 4 * 2 * 8;
constexpr u32 WS_END = WS_OUT + 8 * 32 * 48 * 16 * 8;
static_assert(WS_END <= (8u << 20), "ws map");

constexpr u32 L_RING = 0;
constexpr u32 L_STAGE = 4 * RING * 4096;
constexpr u32 L_XN = L_STAGE + 16384;
constexpr u32 XN_STR = 1056;
constexpr u32 L_Q = L_XN + 8 * XN_STR;
constexpr u32 L_O = L_Q + 1024;
constexpr u32 L_FLAG = L_O + 2048;
constexpr u32 L_SCR = L_FLAG + 256;
constexpr u32 L_END = L_SCR + 1024;
static_assert(L_END <= 163840, "lds");
constexpr u32 F_SYNC = 0, F_Q = 4, F_SYNC2 = 8, F_WO = 12, F_EPOCH = 16, F_SYNC3 = 24, F_ANCH = 28, F_OUTP = 32;

constexpr u32 PER256 = (u32)(4096.0 * 1024.0 / (PACE10 * 0.1e12) * 1e8 * 256.0);
constexpr u32 SPIN_LDS = 1u << 22, SPIN_GLB = 1u << 17;

struct Params {
  const u16* x; const u16* k; const u16* v; const u16* wq; const u16* wo; const u16* wg; const u16* wu; const u16* wd;
  u16* out; unsigned char* ws;
};

__device__ __forceinline__ u32 rt32() { return (u32)__builtin_amdgcn_s_memrealtime(); }
__device__ __forceinline__ float bflo(u32 w) { return __uint_as_float(w << 16); }
__device__ __forceinline__ float bfhi(u32 w) { return __uint_as_float(w & 0xffff0000u); }
__device__ __forceinline__ u32 pk2(float lo, float hi) { f32x2 t = {lo, hi}; return __builtin_bit_cast(u32, __builtin_convertvector(t, bf16x2)); }
__device__ __forceinline__ float rbf(float f) { return bflo(pk2(f, f)); }
template <int CTRL> __device__ __forceinline__ float dpp(float x) {
  return __builtin_bit_cast(float, __builtin_amdgcn_mov_dpp(__builtin_bit_cast(int, x), CTRL, 0xf, 0xf, true));
}
__device__ __forceinline__ float xrow_max(float x) {
  auto s = __builtin_amdgcn_permlane16_swap(__float_as_uint(x), __float_as_uint(x), false, false);
  x = fmaxf(__uint_as_float(s[0]), __uint_as_float(s[1]));
  auto t = __builtin_amdgcn_permlane32_swap(__float_as_uint(x), __float_as_uint(x), false, false);
  return fmaxf(__uint_as_float(t[0]), __uint_as_float(t[1]));
}
__device__ __forceinline__ float xrow_sum(float x) {
  auto s = __builtin_amdgcn_permlane16_swap(__float_as_uint(x), __float_as_uint(x), false, false);
  x = __uint_as_float(s[0]) + __uint_as_float(s[1]);
  auto t = __builtin_amdgcn_permlane32_swap(__float_as_uint(x), __float_as_uint(x), false, false);
  return __uint_as_float(t[0]) + __uint_as_float(t[1]);
}
__device__ __forceinline__ float wave_sum(float x) {
  x += dpp<0xB1>(x); x += dpp<0x4E>(x); x += dpp<0x141>(x); x += dpp<0x128>(x);
  return xrow_sum(x);
}
__device__ __forceinline__ f32x4 mfma32(u32x4 a, u32x4 b, f32x4 c) {
  return __builtin_amdgcn_mfma_f32_16x16x32_bf16(__builtin_bit_cast(bf16x8, a), __builtin_bit_cast(bf16x8, b), c, 0, 0, 0);
}
__device__ __forceinline__ f32x4 mfma16(u32x2 a, u32x2 b, f32x4 c) {
  return __builtin_amdgcn_mfma_f32_16x16x16bf16_1k(__builtin_bit_cast(s16x4, a), __builtin_bit_cast(s16x4, b), c, 0, 0, 0);
}
__device__ __forceinline__ u32x2 trread(LAS unsigned char* p) {
  return __builtin_bit_cast(u32x2, __builtin_amdgcn_ds_read_tr16_b64_v4i16((LAS s16x4*)p));
}
__device__ __forceinline__ u32x4 trread2(LAS unsigned char* p0, LAS unsigned char* p1) {
  const u32x2 a = trread(p0), b = trread(p1); return u32x4{a.x, a.y, b.x, b.y};
}
__device__ __forceinline__ void dma4(u32 ldst, u32 v0, u32 v1, u32 v2, u32 v3, __amdgpu_buffer_rsrc_t srd, u32 soff) {
  u32 keep;
  asm volatile("s_waitcnt lgkmcnt(0)\n\ts_mov_b32 %0, m0\n\ts_mov_b32 m0, %1\n\ts_nop 0\n\t"
               "buffer_load_dwordx4 %2, %6, %7 offen nt lds\n\t"
               "buffer_load_dwordx4 %3, %6, %7 offen offset:1024 nt lds\n\t"
               "buffer_load_dwordx4 %4, %6, %7 offen offset:2048 nt lds\n\t"
               "buffer_load_dwordx4 %5, %6, %7 offen offset:3072 nt lds\n\t"
               "s_mov_b32 m0, %0"
               : "=&s"(keep) : "s"(ldst), "v"(v0), "v"(v1), "v"(v2), "v"(v3), "s"(srd), "s"(soff) : "memory");
}
__device__ __forceinline__ void dma1(u32 ldst, const void* gp) {
  u32 keep;
  asm volatile("s_mov_b32 %0, m0\n\ts_mov_b32 m0, %1\n\ts_nop 0\n\tglobal_load_lds_dwordx4 %2, off nt\n\ts_mov_b32 m0, %0"
               : "=&s"(keep) : "s"(ldst), "v"(gp) : "memory");
}
__device__ __forceinline__ void dma1p(u32 ldst, const void* gp) {
  u32 keep;
  asm volatile("s_mov_b32 %0, m0\n\ts_mov_b32 m0, %1\n\ts_nop 0\n\tglobal_load_lds_dwordx4 %2, off\n\ts_mov_b32 m0, %0"
               : "=&s"(keep) : "s"(ldst), "v"(gp) : "memory");
}
#define VMCNT(N) asm volatile("s_waitcnt vmcnt(" #N ")" ::: "memory")
#define LGKM0() asm volatile("s_waitcnt lgkmcnt(0)" ::: "memory")
#define NOUNROLL _Pragma("nounroll")
__device__ __forceinline__ void wait_units(int n) {
  switch (n) {
    case 0: VMCNT(0); break;   case 1: VMCNT(4); break;   case 2: VMCNT(8); break;   case 3: VMCNT(12); break;
    case 4: VMCNT(16); break;  case 5: VMCNT(20); break;  case 6: VMCNT(24); break;  default: VMCNT(28); break;
  }
}
__device__ __forceinline__ void bar() { LGKM0(); __builtin_amdgcn_s_barrier(); asm volatile("" ::: "memory"); }
__device__ __forceinline__ u32 lds_ld(LAS unsigned char* p) { return *(volatile LAS u32*)p; }
__device__ __forceinline__ void lds_add(LAS unsigned char* p, u32 v) { __hip_atomic_fetch_add((LAS u32*)p, v, __ATOMIC_RELAXED, __HIP_MEMORY_SCOPE_WORKGROUP); }
__device__ __forceinline__ void lds_wait_ge(LAS unsigned char* p, u32 want) {
  NOUNROLL
  for (u32 n = 0; n < SPIN_LDS; ++n) { if (lds_ld(p) >= want) break; __builtin_amdgcn_s_sleep(1); }
  asm volatile("" ::: "memory");
}
__device__ __forceinline__ void wait_until(u32 due) { while ((int)(rt32() - due) < 0) __builtin_amdgcn_s_sleep(4); }
__device__ __forceinline__ void st_gran(unsigned char* ws, u32 off, u32 lo, u32 hi) {
  __hip_atomic_store((GAS u64*)(ws + off), ((u64)hi << 32) | lo, __ATOMIC_RELAXED, __HIP_MEMORY_SCOPE_AGENT);
}
__device__ __forceinline__ u64 ld_gran(unsigned char* ws, u32 off) {
  return __hip_atomic_load((GAS u64*)(ws + off), __ATOMIC_RELAXED, __HIP_MEMORY_SCOPE_AGENT);
}

__global__ void __launch_bounds__(NTHR, 2) fused_decode_kernel(Params p) {
  __shared__ __attribute__((aligned(16))) unsigned char lds_raw[L_END];
  LAS unsigned char* L = (LAS unsigned char*)lds_raw;
  const u32 lbase = (u32)(size_t)L;
  const int tid = threadIdx.x, lane = tid & 63, wave = __builtin_amdgcn_readfirstlane(tid >> 6);
  const int g = lane >> 4, c = lane & 15;
  const int blk = blockIdx.x, xg = blk & 7, r = blk >> 3;
  const u32 t0 = rt32();
  u64 pc0; asm volatile("s_getpc_b64 %0" : "=s"(pc0));
  const __amdgpu_buffer_rsrc_t wsr = __builtin_amdgcn_make_buffer_rsrc(p.ws, 0, WS_END, 0x00020000);
  const int as = r & 3, ab = r >> 2, akv = xg, apair = ab * 8 + akv;
  const int whead = 4 * xg + (r & 3), wcc = r >> 2;

  if (wave < 4) {
    const size_t pbase = (size_t)apair * SK * DH;
    const __amdgpu_buffer_rsrc_t ksr = __builtin_amdgcn_make_buffer_rsrc((void*)(p.k + pbase), 0, SK * DH * 2, 0x00020000);
    const __amdgpu_buffer_rsrc_t vsr = __builtin_amdgcn_make_buffer_rsrc((void*)(p.v + pbase), 0, SK * DH * 2, 0x00020000);
    const u32 vk0 = g * 256 + 16 * (c ^ g), vk1 = g * 256 + 16 * (c ^ (4 + g)), vk2 = g * 256 + 16 * (c ^ (8 + g)), vk3 = g * 256 + 16 * (c ^ (12 + g));
    const u32 vv0 = g * 256 + 16 * (c ^ (g << 1)), vv1 = g * 256 + 16 * (c ^ ((4 + g) << 1));
    const u32 ring = lbase + L_RING + wave * (RING * 4096);
    int nis = 0;
    int landed = 0;
    u32 tnow = t0, pA = t0 + PACE_T0;
    const u32 sbase = as * 16384u + wave * 4096u;
    auto issue = [&]() {
      if (nis >= PACE_FROM) {
        if (nis < PACE_FROM + 6) { const u32 an = __builtin_amdgcn_readfirstlane(lds_ld(L + L_FLAG + F_ANCH)); if (an) pA = an + PACE_T0; }
        const u32 due = pA + (((u32)nis * PER256) >> 8);
        if ((int)(tnow - due) < 0) {
          NOUNROLL
          for (u32 n = 0; n < SPIN_LDS; ++n) { tnow = rt32(); if ((int)(tnow - due) >= 0) break; __builtin_amdgcn_s_sleep(1); }
        }
      }
      const u32 soff = (u32)(nis >> 1) * 65536u + sbase, ldst = ring + (u32)(nis & (RING - 1)) * 4096u;
      if (nis & 1) dma4(ldst, vv0, vv1, vv0, vv1, vsr, soff); else dma4(ldst, vk0, vk1, vk2, vk3, ksr, soff);
      ++nis;
    };
    issue();
    bar();
    NOUNROLL
    while (nis < RING) {
      if (nis >= DPRO) { wait_units(DPRO - 1); landed = nis - (DPRO - 1); }
      if (lds_ld(L + L_FLAG + F_Q) >= 4) break;
      issue();
    }
    auto refill = [&](int cons) { while (nis - landed < DAHEAD && nis < cons + RING && nis < NUNIT) issue(); };
    lds_wait_ge(L + L_FLAG + F_Q, 4);
    u32x4 qf[4];
#pragma unroll
    for (int s = 0; s < 4; ++s) qf[s] = *(LAS u32x4*)(L + L_Q + (c & 3) * 256 + 64 * s + 16 * g);
    refill(0);
    LAS unsigned char* R = L + L_RING + wave * (RING * 4096);
    const u32 ka = c * 256 + 16 * (g ^ c);
    const u32 va = (4 * g + (c >> 2)) * 256 + 32 * ((4 * g + (c >> 2)) & 7) + 8 * (c & 3);
    float m = -INFINITY, lsum = 0.f;
    f32x4 acc[8];
#pragma unroll
    for (int i = 0; i < 8; ++i) acc[i] = f32x4{0.f, 0.f, 0.f, 0.f};
    NOUNROLL
    for (int j = 0; j < NUNIT / 2; ++j) {
      wait_units(nis - 2 * j - 1);
      landed = landed > 2 * j + 1 ? landed : 2 * j + 1;
      refill(2 * j);
      LAS unsigned char* Kt = R + ((2 * j) & (RING - 1)) * 4096;
      LAS unsigned char* Vt = R + ((2 * j + 1) & (RING - 1)) * 4096;
      f32x4 sa = {0.f, 0.f, 0.f, 0.f}, sb = {0.f, 0.f, 0.f, 0.f};
      {
        const u32x4 k0 = *(LAS u32x4*)(Kt + ka), k1 = *(LAS u32x4*)(Kt + (ka ^ 64)), k2 = *(LAS u32x4*)(Kt + (ka ^ 128)), k3 = *(LAS u32x4*)(Kt + (ka ^ 192));
        sa = mfma32(k0, qf[0], sa); sb = mfma32(k1, qf[1], sb); sa = mfma32(k2, qf[2], sa); sb = mfma32(k3, qf[3], sb);
      }
      const f32x4 sc = sa + sb;
      const float tmax = fmaxf(fmaxf(sc[0], sc[1]), fmaxf(sc[2], sc[3]));
      if (__builtin_amdgcn_ballot_w64(tmax > m + THR_RAW) != 0ull) {
        const float mn = fmaxf(m, xrow_max(tmax));
        const float al = __builtin_amdgcn_exp2f((m - mn) * SC);
        m = mn; lsum *= al;
        const float a0 = dpp<0x00>(al), a1 = dpp<0x55>(al), a2 = dpp<0xAA>(al), a3 = dpp<0xFF>(al);
#pragma unroll
        for (int i = 0; i < 8; ++i) { acc[i][0] *= a0; acc[i][1] *= a1; acc[i][2] *= a2; acc[i][3] *= a3; }
      }
      const float mc = m * SC;
      const float p0 = __builtin_amdgcn_exp2f(fmaf(sc[0], SC, -mc)), p1 = __builtin_amdgcn_exp2f(fmaf(sc[1], SC, -mc));
      const float p2 = __builtin_amdgcn_exp2f(fmaf(sc[2], SC, -mc)), p3 = __builtin_amdgcn_exp2f(fmaf(sc[3], SC, -mc));
      lsum += (p0 + p1) + (p2 + p3);
      const u32x2 pa = {pk2(p0, p1), pk2(p2, p3)};
      wait_units(nis - 2 * j - 2);
      landed = landed > 2 * j + 2 ? landed : 2 * j + 2;
      refill(2 * j + 1);
#pragma unroll
      for (int i = 0; i < 8; ++i) acc[i] = mfma16(pa, trread(Vt + (va ^ (32 * i))), acc[i]);
      refill(2 * j + 2);
    }

    lsum = xrow_sum(lsum);
    if (g == 0) {
#pragma unroll
      for (int i = 0; i < 8; ++i) *(LAS f32x4*)(R + i * 256 + c * 16) = acc[i];
      if (c < 4) *(LAS f32x2*)(R + 2048 + c * 8) = f32x2{m * SC, lsum};
    }
  } else {
    const int hw = wave - 4;
    __builtin_amdgcn_s_setprio(3);
    u32x4 xa, xb; u32x2 ev;
    if (hw == 0 && lane < 16) *(LAS u32x4*)(L + L_FLAG + lane * 16) = u32x4{0, 0, 0, 0};
    const int kq = r & 3;
    if (NCODE && r * 4 + hw < NCODE) {
      dma1p(lbase + L_SCR, (const char*)(pc0 & ~255ull) + (r * 4 + hw) * 1024 + lane * 16);
    }
    {
      const u16* xp = p.x + (2 * hw) * DM + lane * 8;
      const u16* wp = p.wq + (size_t)(kq * 128 + hw * 32 + (lane >> 3)) * 4096 + xg * 512 + (r >> 2) * 64 + (lane & 7) * 8;
      const u32 ldst = lbase + L_STAGE + hw * 4096;
      u32 keep;
      asm volatile("global_load_dwordx4 %0, %4, off\n\tglobal_load_dwordx4 %1, %4, off offset:1024\n\tglobal_load_dwordx2 %2, %5, off\n\t"
                   "s_mov_b32 %3, m0\n\ts_mov_b32 m0, %10\n\ts_nop 0\n\t"
                   "global_load_lds_dwordx4 %6, off nt\n\ts_add_u32 m0, m0, 0x400\n\ts_nop 0\n\t"
                   "global_load_lds_dwordx4 %7, off nt\n\ts_add_u32 m0, m0, 0x400\n\ts_nop 0\n\t"
                   "global_load_lds_dwordx4 %8, off nt\n\ts_add_u32 m0, m0, 0x400\n\ts_nop 0\n\t"
                   "global_load_lds_dwordx4 %9, off nt\n\ts_mov_b32 m0, %3\n\t"
                   "s_waitcnt lgkmcnt(0)\n\ts_barrier\n\ts_waitcnt vmcnt(4)"
                   : "=&v"(xa), "=&v"(xb), "=&v"(ev), "=&s"(keep)
                   : "v"(xp), "v"(p.ws + WS_EPOCH), "v"(wp), "v"(wp + (size_t)8 * 4096), "v"(wp + (size_t)16 * 4096), "v"(wp + (size_t)24 * 4096), "s"(ldst)
                   : "memory", "scc");
    }
    const u64 ep = (((u64)ev.y << 32) | ev.x) + 1ull;
    const u32 tag32 = (u32)ep, t48lo = ((u32)ep) << 16, t48hi = (u32)(ep >> 16), t40lo = ((u32)ep) << 24, t40hi = (u32)(ep >> 8);
#pragma unroll
    for (int q = 0; q < 2; ++q) {
      const u32x4 xv = q ? xb : xa;
      float v[8];
#pragma unroll
      for (int i = 0; i < 4; ++i) { v[2 * i] = bflo(xv[i]); v[2 * i + 1] = bfhi(xv[i]); }
      const float mean = wave_sum(((v[0] + v[1]) + (v[2] + v[3])) + ((v[4] + v[5]) + (v[6] + v[7]))) * (1.f / DM);
      float q2 = 0.f;
#pragma unroll
      for (int i = 0; i < 8; ++i) { const float d = v[i] - mean; q2 = fmaf(d, d, q2); }
      const float var = rbf(wave_sum(q2) * (1.f / DM));
      const float rden = __builtin_amdgcn_rcpf(rbf(__builtin_amdgcn_sqrtf(rbf(var + 1e-5f))));
      const float mb = rbf(mean);
      u32x4 o;
#pragma unroll
      for (int i = 0; i < 4; ++i) { const u32 d = pk2(v[2 * i] - mb, v[2 * i + 1] - mb); o[i] = pk2(bflo(d) * rden, bfhi(d) * rden); }
      *(LAS u32x4*)(L + L_XN + (2 * hw + q) * XN_STR + lane * 16) = o;
    }
    const int tq = c >> 2, tp = c & 3;
    auto tile_mma = [&](const u32x4 (&act)[4], int cg) {
      f32x4 a0 = {0.f, 0.f, 0.f, 0.f}, a1 = {0.f, 0.f, 0.f, 0.f};
      LAS unsigned char* ap = L + L_STAGE + (8 * g + tq) * 128 + 32 * cg + 8 * tp;
      a0 = mfma32(act[0], trread2(ap, ap + 512), a0);
      a1 = mfma32(act[1], trread2(ap + 4096, ap + 4096 + 512), a1);
      a0 = mfma32(act[2], trread2(ap + 8192, ap + 8192 + 512), a0);
      a1 = mfma32(act[3], trread2(ap + 12288, ap + 12288 + 512), a1);
      return a0 + a1;
    };
    auto xn_frags = [&](u32x4 (&act)[4], int k0) {
#pragma unroll
      for (int s = 0; s < 4; ++s) act[s] = *(LAS u32x4*)(L + L_XN + (c & 7) * XN_STR + 2 * k0 + 64 * s + 16 * g);
    };
    auto f24 = [&](float f) { const u32 u = __float_as_uint(f); return t40lo | ((u + 0x7fu + ((u >> 8) & 1u)) >> 8); };
    auto ok40 = [&](const u32x4& t) { return ((t[0] ^ t40lo) >> 24) == 0 && t[1] == t40hi && ((t[2] ^ t40lo) >> 24) == 0 && t[3] == t40hi; };
    auto ok48 = [&](const u32x4& t) { return ((t[0] ^ t48lo) >> 16) == 0 && t[1] == t48hi && ((t[2] ^ t48lo) >> 16) == 0 && t[3] == t48hi; };
    u32x4 act[4];
    asm volatile("s_waitcnt vmcnt(0) lgkmcnt(0)" ::: "memory");
    if (lane == 0) lds_add(L + L_FLAG + F_SYNC, 1u);
    lds_wait_ge(L + L_FLAG + F_SYNC, 4);
    xn_frags(act, kq * 128);
    {
      const f32x4 d = tile_mma(act, hw);
      if (g < 2) {
#pragma unroll
        for (int i = 0; i < 4; ++i) st_gran(p.ws, WS_Q + ((((xg * 8 + 4 * g + i) * 4 + kq) * 512) + (r >> 2) * 64 + 16 * hw + c) * 8, f24(d[i]), t40hi);
      }
    }
    if (hw == 1 && lane == 0) st_gran(p.ws, WS_T0 + blk * 8, t40lo | (t0 & 0xffffffu), t40hi);
    LGKM0();
    if (lane == 0) lds_add(L + L_FLAG + F_SYNC, 1u);
    if (hw < 2) {
      const u32 qb = WS_Q + ((akv * 8 + ab) * 4) * 512 * 8;
      wait_until(t0 + TQ0 - (u32)(r * TQR) / 16);
      float q0[2], q1[2];
      NOUNROLL
      for (u32 n = 0; n < SPIN_GLB; ++n) {
        bool ok = true;
#pragma unroll
        for (int h = 0; h < 2; ++h) {
          q0[h] = 0.f; q1[h] = 0.f;
#pragma unroll
          for (int i = 0; i < 4; ++i) {
            const u32x4 t = __builtin_amdgcn_raw_buffer_load_b128(wsr, qb + (i * 512 + (2 * hw + h) * 128 + 2 * lane) * 8, 0, 16);
            ok &= ok40(t); q0[h] += __uint_as_float(t[0] << 8); q1[h] += __uint_as_float(t[2] << 8);
          }
        }
        if (__all(ok)) break;
        __builtin_amdgcn_s_sleep(POLLSLP);
        asm volatile("" ::: "memory");
      }
      *(LAS u32*)(L + L_Q + (2 * hw) * 256 + lane * 4) = pk2(q0[0], q1[0]);
      *(LAS u32*)(L + L_Q + (2 * hw + 1) * 256 + lane * 4) = pk2(q0[1], q1[1]);
      LGKM0();
      if (lane == 0) lds_add(L + L_FLAG + F_Q, 2u);
      if (hw == 0) {
        u64 ts = 0;
        bool ok = false;
        NOUNROLL
        for (u32 n = 0; n < 64 && !ok; ++n) {
          ts = ld_gran(p.ws, WS_T0 + (xg + 8 * (lane & 31)) * 8);
          ok = __all((((u32)ts ^ t40lo) >> 24) == 0 && (u32)(ts >> 32) == t40hi);
          if (!ok) __builtin_amdgcn_s_sleep(8);
        }
        int d = ((int)(((u32)ts - t0) << 8)) >> 8;
        d = min(d, __builtin_amdgcn_mov_dpp(d, 0xB1, 0xf, 0xf, true)); d = min(d, __builtin_amdgcn_mov_dpp(d, 0x4E, 0xf, 0xf, true));
        d = min(d, __builtin_amdgcn_mov_dpp(d, 0x141, 0xf, 0xf, true)); d = min(d, __builtin_amdgcn_mov_dpp(d, 0x128, 0xf, 0xf, true));
        int dm = min(min(__builtin_amdgcn_readlane(d, 0), __builtin_amdgcn_readlane(d, 16)), 0);
        dm = dm < -400 ? -400 : dm;
        if (ok && lane == 0) *(volatile LAS u32*)(L + L_FLAG + F_ANCH) = (t0 + dm) | 1u;
      }
      if (hw == 1) {
        if (TOUCH) {
          const int nl = TOUCH == 1 ? 2 : 8;
          const u16* tb = ((lane & 1) ? p.v : p.k) + (size_t)apair * SK * DH + as * 8192 + ((lane >> 1) & 3) * 2048 + 1024;
          const u32 ld_ = lbase + L_SCR;
          NOUNROLL
          for (int j = 3; j < NUNIT / 2; ++j) {
            if (j > TLOOK + 3) wait_until(t0 + PACE_T0 + (((u32)(2 * (j - TLOOK)) * PER256) >> 8));
            const u16* tp = tb + (size_t)j * 32768;
            u32 keep;
            if (lane < nl)
              asm volatile("s_mov_b32 %0, m0\n\ts_mov_b32 m0, %1\n\ts_nop 0\n\tglobal_load_lds_dword %2, off nt\n\ts_mov_b32 m0, %0" : "=&s"(keep) : "s"(ld_), "v"(tp) : "memory");
          }
        }
        wait_until(t0 + TGS);
        NOUNROLL
        for (u32 n = 0; n < SPIN_GLB; ++n) {
          const u32x4 a = __builtin_amdgcn_raw_buffer_load_b128(wsr, WS_START + lane * 16, 0, 16);
          const u32x4 b = __builtin_amdgcn_raw_buffer_load_b128(wsr, WS_START + 1024 + lane * 16, 0, 16);
          const u32 e0 = (u32)ep, e1 = (u32)(ep >> 32);
          const bool ok = a[0] == e0 && a[1] == e1 && a[2] == e0 && a[3] == e1 && b[0] == e0 && b[1] == e1 && b[2] == e0 && b[3] == e1;
          if (__all(ok)) break;
          __builtin_amdgcn_s_sleep(32);
          asm volatile("" ::: "memory");
        }
      }
    } else {
      const int h2 = hw - 2;
      u32 nsync2 = 0;
      auto arrive2 = [&]() {
        LGKM0();
        if (lane == 0) lds_add(L + L_FLAG + F_SYNC2, 1u);
        nsync2 += 2;
        lds_wait_ge(L + L_FLAG + F_SYNC2, nsync2);
      };
      auto stage2 = [&](const u16* w, int ld) {
        const u16* wp = w + (size_t)(h2 * 64 + (lane >> 3)) * ld + (lane & 7) * 8;
#pragma unroll
        for (int i = 0; i < 8; ++i) dma1(lbase + L_STAGE + h2 * 8192 + i * 1024, wp + (size_t)i * 8 * ld);
      };
      if (h2 == 1) {
        const u32x4 z = {0u, ~tag32, 0u, ~tag32};
#pragma unroll
        for (int i = 0; i < 2; ++i)
          __builtin_amdgcn_raw_buffer_store_b128(z, wsr, WS_OP + ((((akv * 4 + 2 * i + (lane >> 5)) * 8 + ab) * 4 + as) * 64) * 8 + (lane & 31) * 16, 0, 16);
        if (lane < 4) __builtin_amdgcn_raw_buffer_store_b128(z, wsr, WS_ML + (((akv * 4 + lane) * 8 + ab) * 4 + as) * 16, 0, 16);
      }
      lds_wait_ge(L + L_FLAG + F_SYNC, 8);
      const bool f1 = r < 14, f15 = r >= 14 && r < 18, f2 = r >= 18;
      const int t1 = xg * 14 + r, sl1 = t1 >> 2, fq = t1 & 3, ks = r - 18;
      const u16* wo_tile = p.wo + (size_t)whead * 128 * DM + wcc * 64;
      if (f1) stage2(p.wg + (size_t)fq * 128 * FF + sl1 * 64, FF);
      else if (f2) stage2(p.wd + (size_t)ks * 128 * DM + xg * 64, DM);
      else stage2(wo_tile, DM);
      VMCNT(0);
      if (h2 == 1 && lane == 0) {
        __hip_atomic_store((GAS u64*)(p.ws + WS_START + blk * 8), ep, __ATOMIC_RELAXED, __HIP_MEMORY_SCOPE_AGENT);
        *(volatile LAS u64*)(L + L_FLAG + F_EPOCH) = ep;
      }
      arrive2();
      if (f1) {
        xn_frags(act, fq * 128);
        f32x4 d[2];
        d[0] = tile_mma(act, 2 * h2); d[1] = tile_mma(act, 2 * h2 + 1);
        asm volatile("" : "+v"(d[0]), "+v"(d[1]));
        arrive2();
        stage2(p.wu + (size_t)fq * 128 * FF + sl1 * 64, FF);
#pragma unroll
        for (int w = 0; w < 2; ++w) if (g < 2) {
#pragma unroll
          for (int i = 0; i < 4; ++i) st_gran(p.ws, WS_GU + ((((sl1 * 2 + 0) * 4 + fq) * 8 + 4 * g + i) * 64 + 16 * (2 * h2 + w) + c) * 8, f24(d[w][i]), t40hi);
        }
        VMCNT(8);
        arrive2();
        d[0] = tile_mma(act, 2 * h2); d[1] = tile_mma(act, 2 * h2 + 1);
        asm volatile("" : "+v"(d[0]), "+v"(d[1]));
        arrive2();
        stage2(wo_tile, DM);
#pragma unroll
        for (int w = 0; w < 2; ++w) if (g < 2) {
#pragma unroll
          for (int i = 0; i < 4; ++i) st_gran(p.ws, WS_GU + ((((sl1 * 2 + 1) * 4 + fq) * 8 + 4 * g + i) * 64 + 16 * (2 * h2 + w) + c) * 8, f24(d[w][i]), t40hi);
        }
        VMCNT(8);
        arrive2();
      } else if (f15) {
        const int sl = xg * 4 + (r - 14);
        if (sl < 28) {
          wait_until(t0 + TG15);
          {
            const u32 so = WS_GU + ((((sl * 2 + 1) * 4 + (lane & 3)) * 8) * 64 + 63) * 8;
            NOUNROLL
            for (u32 n = 0; n < SPIN_GLB; ++n) {
              const u64 t = ld_gran(p.ws, so);
              if (__all((((u32)t ^ t40lo) >> 24) == 0 && (u32)(t >> 32) == t40hi)) break;
              __builtin_amdgcn_s_sleep(16);
            }
          }
          float gs[2][2], us[2][2];
          const int cp = lane & 31;
          NOUNROLL
          for (u32 n = 0; n < SPIN_GLB; ++n) {
            bool ok = true;
#pragma unroll
            for (int bb = 0; bb < 2; ++bb) {
              const int b = 4 * h2 + 2 * bb + (lane >> 5);
              gs[bb][0] = gs[bb][1] = us[bb][0] = us[bb][1] = 0.f;
#pragma unroll
              for (int i = 0; i < 4; ++i) {
                const u32x4 tg = __builtin_amdgcn_raw_buffer_load_b128(wsr, WS_GU + ((((sl * 2 + 0) * 4 + i) * 8 + b) * 64 + 2 * cp) * 8, 0, 16);
                const u32x4 tu = __builtin_amdgcn_raw_buffer_load_b128(wsr, WS_GU + ((((sl * 2 + 1) * 4 + i) * 8 + b) * 64 + 2 * cp) * 8, 0, 16);
                ok &= ok40(tg) && ok40(tu);
                gs[bb][0] += __uint_as_float(tg[0] << 8); gs[bb][1] += __uint_as_float(tg[2] << 8);
                us[bb][0] += __uint_as_float(tu[0] << 8); us[bb][1] += __uint_as_float(tu[2] << 8);
              }
            }
            if (__all(ok)) break;
            __builtin_amdgcn_s_sleep(16);
            asm volatile("" ::: "memory");
          }
#pragma unroll
          for (int bb = 0; bb < 2; ++bb) {
            const int b = 4 * h2 + 2 * bb + (lane >> 5);
            const u32 hp = pk2(rbf(gs[bb][0]) * rbf(us[bb][0]), rbf(gs[bb][1]) * rbf(us[bb][1]));
            st_gran(p.ws, WS_H + (b * FF + sl * 64 + 2 * cp) * 8, t48lo | (hp & 0xffffu), t48hi);
            st_gran(p.ws, WS_H + (b * FF + sl * 64 + 2 * cp + 1) * 8, t48lo | (hp >> 16), t48hi);
          }
        }
      } else {
        wait_until(t0 + TG2);
        {
          const u32 so = WS_H + (7 * FF + ks * 128 + 64 * (lane & 1) + 63) * 8;
          NOUNROLL
          for (u32 n = 0; n < SPIN_GLB; ++n) {
            const u64 t = ld_gran(p.ws, so);
            if (__all((((u32)t ^ t48lo) >> 16) == 0 && (u32)(t >> 32) == t48hi)) break;
            __builtin_amdgcn_s_sleep(32);
          }
        }
        const int b = lane >> 3, k0 = 64 * h2 + 8 * (lane & 7);
        u32x4 hv = {0, 0, 0, 0};
        NOUNROLL
        for (u32 n = 0; n < SPIN_GLB; ++n) {
          bool ok = true;
#pragma unroll
          for (int i = 0; i < 4; ++i) {
            const u32x4 t = __builtin_amdgcn_raw_buffer_load_b128(wsr, WS_H + (b * FF + ks * 128 + k0 + 2 * i) * 8, 0, 16);
            ok &= ok48(t);
            hv[i] = (t[0] & 0xffffu) | (t[2] << 16);
          }
          if (__all(ok)) break;
          __builtin_amdgcn_s_sleep(16);
          asm volatile("" ::: "memory");
        }
        *(LAS u32x4*)(L + L_O + b * 256 + k0 * 2) = hv;
        arrive2();
#pragma unroll
        for (int s = 0; s < 4; ++s) act[s] = *(LAS u32x4*)(L + L_O + (c & 7) * 256 + 64 * s + 16 * g);
        f32x4 d[2];
        d[0] = tile_mma(act, 2 * h2); d[1] = tile_mma(act, 2 * h2 + 1);
        asm volatile("" : "+v"(d[0]), "+v"(d[1]));
        arrive2();
        stage2(wo_tile, DM);
#pragma unroll
        for (int w = 0; w < 2; ++w) if (g < 2) {
#pragma unroll
          for (int i = 0; i < 4; ++i) st_gran(p.ws, WS_OUT + ((((4 * g + i) * 32 + 4 * xg + 2 * h2 + w) * 48) + 32 + ks) * 128 + c * 8, f24(d[w][i]), t40hi);
        }
        VMCNT(8);
        arrive2();
      }
      if (h2 == 0 && lane == 0) *(volatile LAS u32*)(L + L_FLAG + F_WO) = 1;
    }
    NOUNROLL
    for (u32 n = 0; n < SPIN_LDS; ++n) { if (lds_ld(L + L_FLAG + F_WO) >= 1) break; __builtin_amdgcn_s_sleep(16); }
    asm volatile("" ::: "memory");
  }

  u32x4 wof[4];
  if (wave >= 4) {
    const int tq = c >> 2, tp = c & 3, w = wave - 4;
#pragma unroll
    for (int s = 0; s < 4; ++s) { LAS unsigned char* ap = L + L_STAGE + (32 * s + 8 * g + tq) * 128 + 32 * w + 8 * tp; wof[s] = trread2(ap, ap + 512); }
  }
  bar();
  const u64 ep = *(volatile LAS u64*)(L + L_FLAG + F_EPOCH);
  const u32 tag32 = (u32)ep, t40lo = ((u32)ep) << 24, t40hi = (u32)(ep >> 8);
  if (wave < 4) {
    const int hh = wave, ch = lane >> 3, c0 = (2 * lane) & 15;
    float mw[4], lw[4], v0[4], v1[4];
#pragma unroll
    for (int w = 0; w < 4; ++w) {
      LAS unsigned char* R = L + L_RING + w * (RING * 4096);
      const f32x2 ml = *(LAS f32x2*)(R + 2048 + hh * 8);
      mw[w] = ml[0]; lw[w] = ml[1];
      v0[w] = *(LAS float*)(R + ch * 256 + c0 * 16 + hh * 4);
      v1[w] = *(LAS float*)(R + ch * 256 + c0 * 16 + 16 + hh * 4);
    }
    const float M = fmaxf(fmaxf(mw[0], mw[1]), fmaxf(mw[2], mw[3]));
    float Ls = 0.f, o0 = 0.f, o1 = 0.f;
#pragma unroll
    for (int w = 0; w < 4; ++w) { const float f = __builtin_amdgcn_exp2f(mw[w] - M); Ls = fmaf(f, lw[w], Ls); o0 = fmaf(f, v0[w], o0); o1 = fmaf(f, v1[w], o1); }
    const float inv = __builtin_amdgcn_rcpf(Ls);
    const u32 slot = ((akv * 4 + hh) * 8 + ab) * 4 + as;
    st_gran(p.ws, WS_OP + (slot * 64 + lane) * 8, pk2(o0 * inv, o1 * inv), tag32);
    if (lane < 2) st_gran(p.ws, WS_ML + (slot * 2 + lane) * 8, __float_as_uint(lane ? Ls : M), tag32);
  } else {
    const int w = wave - 4;
    const u32 slot = (whead * 8 + 2 * w) * 4;
    u64 o[8] = {0, 0, 0, 0, 0, 0, 0, 0}, ml = 0;
    __builtin_amdgcn_s_sleep(H1DLY);
    {
      u32 have = 0;
      NOUNROLL
      for (u32 n = 0; n < SPIN_GLB; ++n) {
        u64 t[9];
#pragma unroll
        for (int s = 0; s < 8; ++s) t[s] = ld_gran(p.ws, ((have >> s) & 1) ? WS_DUMMY : WS_OP + ((slot + s) * 64 + lane) * 8);
        t[8] = ld_gran(p.ws, ((have >> 8) & 1) ? WS_DUMMY : WS_ML + (slot * 2 + (lane & 15)) * 8);
#pragma unroll
        for (int s = 0; s < 9; ++s) {
          const bool fresh = !((have >> s) & 1);
          if (s < 8) o[s] = fresh ? t[s] : o[s]; else ml = fresh ? t[8] : ml;
          if (__all((u32)(t[s] >> 32) == tag32)) have |= 1u << s;
        }
        if (have == 0x1ffu) break;
        __builtin_amdgcn_s_sleep(POLLSLP);
      }
    }
    const u32 mlv = (u32)ml;
#pragma unroll
    for (int bb = 0; bb < 2; ++bb) {
      float ms[4], ls[4];
#pragma unroll
      for (int s = 0; s < 4; ++s) { ms[s] = __uint_as_float(__builtin_amdgcn_readlane(mlv, 8 * bb + 2 * s)); ls[s] = __uint_as_float(__builtin_amdgcn_readlane(mlv, 8 * bb + 2 * s + 1)); }
      const float M = fmaxf(fmaxf(ms[0], ms[1]), fmaxf(ms[2], ms[3]));
      float W = 0.f, o0 = 0.f, o1 = 0.f;
#pragma unroll
      for (int s = 0; s < 4; ++s) { const float wt = ls[s] * __builtin_amdgcn_exp2f(ms[s] - M); W += wt; o0 = fmaf(wt, bflo((u32)o[4 * bb + s]), o0); o1 = fmaf(wt, bfhi((u32)o[4 * bb + s]), o1); }
      const float inv = __builtin_amdgcn_rcpf(W);
      *(LAS u32*)(L + L_O + (2 * w + bb) * 256 + lane * 4) = pk2(o0 * inv, o1 * inv);
    }
    LGKM0();
    if (lane == 0) lds_add(L + L_FLAG + F_SYNC3, 1u);
    NOUNROLL
    for (u32 n = 0; n < SPIN_LDS; ++n) { if (lds_ld(L + L_FLAG + F_SYNC3) >= 4) break; }
    asm volatile("" ::: "memory");
    f32x4 a = {0.f, 0.f, 0.f, 0.f}, a2 = {0.f, 0.f, 0.f, 0.f};
    a = mfma32(*(LAS u32x4*)(L + L_O + (c & 7) * 256 + 16 * g), wof[0], a);
    a2 = mfma32(*(LAS u32x4*)(L + L_O + (c & 7) * 256 + 64 + 16 * g), wof[1], a2);
    a = mfma32(*(LAS u32x4*)(L + L_O + (c & 7) * 256 + 128 + 16 * g), wof[2], a);
    a2 = mfma32(*(LAS u32x4*)(L + L_O + (c & 7) * 256 + 192 + 16 * g), wof[3], a2);
    a += a2;
    if (g < 2) {
#pragma unroll
      for (int i = 0; i < 4; ++i) {
        const u32 u = __float_as_uint(a[i]);
        st_gran(p.ws, WS_OUT + ((((4 * g + i) * 32 + 4 * wcc + w) * 48) + whead) * 128 + c * 8, t40lo | ((u + 0x7fu + ((u >> 8) & 1u)) >> 8), t40hi);
      }
    }
    if (lane == 0) lds_add(L + L_FLAG + F_OUTP, 1u);
  }
  if (wave == 0) {
    const float xres = bflo(p.x[(r >> 2) * DM + 16 * (4 * xg + (r & 3)) + c]);
    const u32 base = WS_OUT + (((r >> 2) * 32 + 4 * xg + (r & 3)) * 48) * 128 + c * 8;
    u64 t[12] = {0, 0, 0, 0, 0, 0, 0, 0, 0, 0, 0, 0};
    lds_wait_ge(L + L_FLAG + F_OUTP, 4);
    __builtin_amdgcn_s_sleep(H2DLY);
    {
      u32 have = 0;
      NOUNROLL
      for (u32 n = 0; n < SPIN_GLB; ++n) {
        u64 tt[12];
#pragma unroll
        for (int i = 0; i < 12; ++i) { const int pr = (4 * i + g < 46) ? 4 * i + g : 45; tt[i] = ld_gran(p.ws, ((have >> i) & 1) ? WS_DUMMY : base + pr * 128); }
#pragma unroll
        for (int i = 0; i < 12; ++i) {
          t[i] = ((have >> i) & 1) ? t[i] : tt[i];
          if (__all((((u32)tt[i] ^ t40lo) >> 24) == 0 && (u32)(tt[i] >> 32) == t40hi)) have |= 1u << i;
        }
        if (have == 0xfffu) break;
        __builtin_amdgcn_s_sleep(POLLSLP);
      }
    }
    float sa = 0.f, sf = 0.f;
#pragma unroll
    for (int i = 0; i < 8; ++i) sa += __uint_as_float((u32)t[i] << 8);
#pragma unroll
    for (int i = 8; i < 12; ++i) sf += (4 * i + g < 46) ? __uint_as_float((u32)t[i] << 8) : 0.f;
    sa = xrow_sum(sa); sf = xrow_sum(sf);
    const float res = rbf(xres + rbf(sa)) + rbf(sf);
    if (g == 0) p.out[(r >> 2) * DM + 16 * (4 * xg + (r & 3)) + c] = (u16)(pk2(res, 0.f) & 0xffffu);
    if (blk == 0 && lane == 0) __hip_atomic_store((GAS u64*)(p.ws + WS_EPOCH), ep, __ATOMIC_RELAXED, __HIP_MEMORY_SCOPE_AGENT);
  }
}

extern "C" void kernel_launch(void* const* d_in, const int* in_sizes, int n_in, void* d_out, int out_size, void* d_ws, size_t ws_size, hipStream_t stream) {
  Params p;
  p.x = (const u16*)d_in[0]; p.k = (const u16*)d_in[1]; p.v = (const u16*)d_in[2]; p.wq = (const u16*)d_in[3]; p.wo = (const u16*)d_in[4];
  p.wg = (const u16*)d_in[6]; p.wu = (const u16*)d_in[7]; p.wd = (const u16*)d_in[8];
  p.out = (u16*)d_out; p.ws = (unsigned char*)d_ws;
  hipLaunchKernelGGL(fused_decode_kernel, dim3(NWG), dim3(NTHR), 0, stream, p);
}
```

```cpp
#define TOUCH2 1
#include <hip/hip_runtime.h>
#include <stdint.h>

#define GAS __attribute__((address_space(1)))
#define LAS __attribute__((address_space(3)))
typedef unsigned u32;
typedef unsigned long long u64;
typedef unsigned short u16;
typedef u32 u32x4 __attribute__((ext_vector_type(4)));
typedef u32 u32x2 __attribute__((ext_vector_type(2)));
typedef float f32x4 __attribute__((ext_vector_type(4)));
typedef float f32x2 __attribute__((ext_vector_type(2)));
typedef short s16x4 __attribute__((ext_vector_type(4)));
typedef __bf16 bf16x8 __attribute__((ext_vector_type(8)));
typedef __bf16 bf16x2 __attribute__((ext_vector_type(2)));

constexpr int NCODE = 21;
constexpr int PACE10 = 71;
constexpr int PACE_T0 = 250;
constexpr int PACE_FROM = 8;
constexpr int DAHEAD = 3;
constexpr int DPRO = 1;
constexpr int TQ0 = 380, TQR = 48;
constexpr int TGS = 1000;
constexpr int TG15 = 950;
constexpr int TG2 = 1350;
constexpr int POLLSLP = 4, H1DLY = 18, H2DLY = 14;

constexpr int DM = 512, SK = 8192, DH = 128, FF = 1792;
constexpr int NWG = 256, NTHR = 512;
constexpr int RING = 8;
constexpr int NUNIT = 64;
constexpr float SC = 0.08838834764831845f * 1.4426950408889634f;
constexpr float THR_RAW = 8.0f / SC;

constexpr u32 WS_EPOCH = 0;
constexpr u32 WS_DUMMY = 2048;
constexpr u32 WS_START = 4096;
constexpr u32 WS_T0 = 8192;
constexpr u32 WS_Q = 65536;
constexpr u32 WS_GU = WS_Q + 8 * 8 * 4 * 512 * 8;
constexpr u32 WS_H = WS_GU + 28 * 2 * 4 * 8 * 64 * 8;
constexpr u32 WS_OP = WS_H + 8 * 1792 * 8;
constexpr u32 WS_ML = WS_OP + 32 * 8 * 4 * 64 * 8;
constexpr u32 WS_OUT = WS_ML + 32 * 8 * 4 * 2 * 8;
constexpr u32 WS_END = WS_OUT + 8 * 32 * 48 * 16 * 8;
static_assert(WS_END <= (8u << 20), "ws map");

constexpr u32 L_RING = 0;
constexpr u32 L_STAGE = 4 * RING * 4096;
constexpr u32 L_XN = L_STAGE + 16384;
constexpr u32 XN_STR = 1056;
constexpr u32 L_Q = L_XN + 8 * XN_STR;
constexpr u32 L_O = L_Q + 1024;
constexpr u32 L_FLAG = L_O + 2048;
constexpr u32 L_SCR = L_FLAG + 256;
constexpr u32 L_END = L_SCR + 1024;
static_assert(L_END <= 163840, "lds");
constexpr u32 F_SYNC = 0, F_Q = 4, F_SYNC2 = 8, F_WO = 12, F_EPOCH = 16, F_SYNC3 = 24, F_ANCH = 28, F_OUTP = 32;

constexpr u32 PER256 = (u32)(4096.0 * 1024.0 / (PACE10 * 0.1e12) * 1e8 * 256.0);
constexpr u32 SPIN_LDS = 1u << 22, SPIN_GLB = 1u << 17;

struct Params {
  const u16* x; const u16* k; const u16* v; const u16* wq; const u16* wo; const u16* wg; const u16* wu; const u16* wd;
  u16* out; unsigned char* ws;
};

__device__ __forceinline__ u32 rt32() { return (u32)__builtin_amdgcn_s_memrealtime(); }
__device__ __forceinline__ float bflo(u32 w) { return __uint_as_float(w << 16); }
__device__ __forceinline__ float bfhi(u32 w) { return __uint_as_float(w & 0xffff0000u); }
__device__ __forceinline__ u32 pk2(float lo, float hi) { f32x2 t = {lo, hi}; return __builtin_bit_cast(u32, __builtin_convertvector(t, bf16x2)); }
__device__ __forceinline__ float rbf(float f) { return bflo(pk2(f, f)); }
template <int CTRL> __device__ __forceinline__ float dpp(float x) {
  return __builtin_bit_cast(float, __builtin_amdgcn_mov_dpp(__builtin_bit_cast(int, x), CTRL, 0xf, 0xf, true));
}
__device__ __forceinline__ float xrow_max(float x) {
  auto s = __builtin_amdgcn_permlane16_swap(__float_as_uint(x), __float_as_uint(x), false, false);
  x = fmaxf(__uint_as_float(s[0]), __uint_as_float(s[1]));
  auto t = __builtin_amdgcn_permlane32_swap(__float_as_uint(x), __float_as_uint(x), false, false);
  return fmaxf(__uint_as_float(t[0]), __uint_as_float(t[1]));
}
__device__ __forceinline__ float xrow_sum(float x) {
  auto s = __builtin_amdgcn_permlane16_swap(__float_as_uint(x), __float_as_uint(x), false, false);
  x = __uint_as_float(s[0]) + __uint_as_float(s[1]);
  auto t = __builtin_amdgcn_permlane32_swap(__float_as_uint(x), __float_as_uint(x), false, false);
  return __uint_as_float(t[0]) + __uint_as_float(t[1]);
}
__device__ __forceinline__ float wave_sum(float x) {
  x += dpp<0xB1>(x); x += dpp<0x4E>(x); x += dpp<0x141>(x); x += dpp<0x128>(x);
  return xrow_sum(x);
}
__device__ __forceinline__ f32x4 mfma32(u32x4 a, u32x4 b, f32x4 c) {
  return __builtin_amdgcn_mfma_f32_16x16x32_bf16(__builtin_bit_cast(bf16x8, a), __builtin_bit_cast(bf16x8, b), c, 0, 0, 0);
}
__device__ __forceinline__ f32x4 mfma16(u32x2 a, u32x2 b, f32x4 c) {
  return __builtin_amdgcn_mfma_f32_16x16x16bf16_1k(__builtin_bit_cast(s16x4, a), __builtin_bit_cast(s16x4, b), c, 0, 0, 0);
}
__device__ __forceinline__ u32x2 trread(LAS unsigned char* p) {
  return __builtin_bit_cast(u32x2, __builtin_amdgcn_ds_read_tr16_b64_v4i16((LAS s16x4*)p));
}
__device__ __forceinline__ u32x4 trread2(LAS unsigned char* p0, LAS unsigned char* p1) {
  const u32x2 a = trread(p0), b = trread(p1); return u32x4{a.x, a.y, b.x, b.y};
}
__device__ __forceinline__ void dma4(u32 ldst, u32 v0, u32 v1, u32 v2, u32 v3, __amdgpu_buffer_rsrc_t srd, u32 soff) {
  u32 keep;
  asm volatile("s_waitcnt lgkmcnt(0)\n\ts_mov_b32 %0, m0\n\ts_mov_b32 m0, %1\n\ts_nop 0\n\t"
               "buffer_load_dwordx4 %2, %6, %7 offen nt lds\n\t"
               "buffer_load_dwordx4 %3, %6, %7 offen offset:1024 nt lds\n\t"
               "buffer_load_dwordx4 %4, %6, %7 offen offset:2048 nt lds\n\t"
               "buffer_load_dwordx4 %5, %6, %7 offen offset:3072 nt lds\n\t"
               "s_mov_b32 m0, %0"
               : "=&s"(keep) : "s"(ldst), "v"(v0), "v"(v1), "v"(v2), "v"(v3), "s"(srd), "s"(soff) : "memory");
}
__device__ __forceinline__ void dma1(u32 ldst, const void* gp) {
  u32 keep;
  asm volatile("s_mov_b32 %0, m0\n\ts_mov_b32 m0, %1\n\ts_nop 0\n\tglobal_load_lds_dwordx4 %2, off nt\n\ts_mov_b32 m0, %0"
               : "=&s"(keep) : "s"(ldst), "v"(gp) : "memory");
}
__device__ __forceinline__ void dma1p(u32 ldst, const void* gp) {
  u32 keep;
  asm volatile("s_mov_b32 %0, m0\n\ts_mov_b32 m0, %1\n\ts_nop 0\n\tglobal_load_lds_dwordx4 %2, off\n\ts_mov_b32 m0, %0"
               : "=&s"(keep) : "s"(ldst), "v"(gp) : "memory");
}
#define VMCNT(N) asm volatile("s_waitcnt vmcnt(" #N ")" ::: "memory")
#define LGKM0() asm volatile("s_waitcnt lgkmcnt(0)" ::: "memory")
#define NOUNROLL _Pragma("nounroll")
__device__ __forceinline__ void wait_units(int n) {
  switch (n) {
    case 0: VMCNT(0); break;   case 1: VMCNT(4); break;   case 2: VMCNT(8); break;   case 3: VMCNT(12); break;
    case 4: VMCNT(16); break;  case 5: VMCNT(20); break;  case 6: VMCNT(24); break;  default: VMCNT(28); break;
  }
}
__device__ __forceinline__ void bar() { LGKM0(); __builtin_amdgcn_s_barrier(); asm volatile("" ::: "memory"); }
__device__ __forceinline__ u32 lds_ld(LAS unsigned char* p) { return *(volatile LAS u32*)p; }
__device__ __forceinline__ void lds_add(LAS unsigned char* p, u32 v) { __hip_atomic_fetch_add((LAS u32*)p, v, __ATOMIC_RELAXED, __HIP_MEMORY_SCOPE_WORKGROUP); }
__device__ __forceinline__ void lds_wait_ge(LAS unsigned char* p, u32 want) {
  NOUNROLL
  for (u32 n = 0; n < SPIN_LDS; ++n) { if (lds_ld(p) >= want) break; __builtin_amdgcn_s_sleep(1); }
  asm volatile("" ::: "memory");
}
__device__ __forceinline__ void wait_until(u32 due) { while ((int)(rt32() - due) < 0) __builtin_amdgcn_s_sleep(4); }
__device__ __forceinline__ void st_gran(unsigned char* ws, u32 off, u32 lo, u32 hi) {
  __hip_atomic_store((GAS u64*)(ws + off), ((u64)hi << 32) | lo, __ATOMIC_RELAXED, __HIP_MEMORY_SCOPE_AGENT);
}
__device__ __forceinline__ u64 ld_gran(unsigned char* ws, u32 off) {
  return __hip_atomic_load((GAS u64*)(ws + off), __ATOMIC_RELAXED, __HIP_MEMORY_SCOPE_AGENT);
}

__global__ void __launch_bounds__(NTHR, 2) fused_decode_kernel(Params p) {
  __shared__ __attribute__((aligned(16))) unsigned char lds_raw[L_END];
  LAS unsigned char* L = (LAS unsigned char*)lds_raw;
  const u32 lbase = (u32)(size_t)L;
  const int tid = threadIdx.x, lane = tid & 63, wave = __builtin_amdgcn_readfirstlane(tid >> 6);
  const int g = lane >> 4, c = lane & 15;
  const int blk = blockIdx.x, xg = blk & 7, r = blk >> 3;
  const u32 t0 = rt32();
  u64 pc0; asm volatile("s_getpc_b64 %0" : "=s"(pc0));
  const __amdgpu_buffer_rsrc_t wsr = __builtin_amdgcn_make_buffer_rsrc(p.ws, 0, WS_END, 0x00020000);
  const int as = r & 3, ab = r >> 2, akv = xg, apair = ab * 8 + akv;
  const int whead = 4 * xg + (r & 3), wcc = r >> 2;

  if (wave < 4) {
    const size_t pbase = (size_t)apair * SK * DH;
    const __amdgpu_buffer_rsrc_t ksr = __builtin_amdgcn_make_buffer_rsrc((void*)(p.k + pbase), 0, SK * DH * 2, 0x00020000);
    const __amdgpu_buffer_rsrc_t vsr = __builtin_amdgcn_make_buffer_rsrc((void*)(p.v + pbase), 0, SK * DH * 2, 0x00020000);
    const u32 vk0 = g * 256 + 16 * (c ^ g), vk1 = g * 256 + 16 * (c ^ (4 + g)), vk2 = g * 256 + 16 * (c ^ (8 + g)), vk3 = g * 256 + 16 * (c ^ (12 + g));
    const u32 vv0 = g * 256 + 16 * (c ^ (g << 1)), vv1 = g * 256 + 16 * (c ^ ((4 + g) << 1));
    const u32 ring = lbase + L_RING + wave * (RING * 4096);
    int nis = 0;
    int landed = 0;
    u32 tnow = t0, pA = t0 + PACE_T0;
    const u32 sbase = as * 16384u + wave * 4096u;
    auto issue = [&]() {
      if (nis >= PACE_FROM) {
        if (nis < PACE_FROM + 6) { const u32 an = __builtin_amdgcn_readfirstlane(lds_ld(L + L_FLAG + F_ANCH)); if (an) pA = an + PACE_T0; }
        const u32 due = pA + (((u32)nis * PER256) >> 8);
        if ((int)(tnow - due) < 0) {
          NOUNROLL
          for (u32 n = 0; n < SPIN_LDS; ++n) { tnow = rt32(); if ((int)(tnow - due) >= 0) break; __builtin_amdgcn_s_sleep(1); }
        }
      }
      const u32 soff = (u32)(nis >> 1) * 65536u + sbase, ldst = ring + (u32)(nis & (RING - 1)) * 4096u;
      if (nis & 1) dma4(ldst, vv0, vv1, vv0, vv1, vsr, soff); else dma4(ldst, vk0, vk1, vk2, vk3, ksr, soff);
      ++nis;
    };
    issue();
    bar();
    NOUNROLL
    while (nis < RING) {
      if (nis >= DPRO) { wait_units(DPRO - 1); landed = nis - (DPRO - 1); }
      if (lds_ld(L + L_FLAG + F_Q) >= 4) break;
      issue();
    }
    auto refill = [&](int cons) { while (nis - landed < DAHEAD && nis < cons + RING && nis < NUNIT) issue(); };
    lds_wait_ge(L + L_FLAG + F_Q, 4);
    u32x4 qf[4];
#pragma unroll
    for (int s = 0; s < 4; ++s) qf[s] = *(LAS u32x4*)(L + L_Q + (c & 3) * 256 + 64 * s + 16 * g);
    refill(0);
    LAS unsigned char* R = L + L_RING + wave * (RING * 4096);
    const u32 ka = c * 256 + 16 * (g ^ c);
    const u32 va = (4 * g + (c >> 2)) * 256 + 32 * ((4 * g + (c >> 2)) & 7) + 8 * (c & 3);
    float m = -INFINITY, lsum = 0.f;
    f32x4 acc[8];
#pragma unroll
    for (int i = 0; i < 8; ++i) acc[i] = f32x4{0.f, 0.f, 0.f, 0.f};
    NOUNROLL
    for (int j = 0; j < NUNIT / 2; ++j) {
      wait_units(nis - 2 * j - 1);
      landed = landed > 2 * j + 1 ? landed : 2 * j + 1;
      refill(2 * j);
      LAS unsigned char* Kt = R + ((2 * j) & (RING - 1)) * 4096;
      LAS unsigned char* Vt = R + ((2 * j + 1) & (RING - 1)) * 4096;
      f32x4 sa = {0.f, 0.f, 0.f, 0.f}, sb = {0.f, 0.f, 0.f, 0.f};
      {
        const u32x4 k0 = *(LAS u32x4*)(Kt + ka), k1 = *(LAS u32x4*)(Kt + (ka ^ 64)), k2 = *(LAS u32x4*)(Kt + (ka ^ 128)), k3 = *(LAS u32x4*)(Kt + (ka ^ 192));
        sa = mfma32(k0, qf[0], sa); sb = mfma32(k1, qf[1], sb); sa = mfma32(k2, qf[2], sa); sb = mfma32(k3, qf[3], sb);
      }
      const f32x4 sc = sa + sb;
      const float tmax = fmaxf(fmaxf(sc[0], sc[1]), fmaxf(sc[2], sc[3]));
      if (__builtin_amdgcn_ballot_w64(tmax > m + THR_RAW) != 0ull) {
        const float mn = fmaxf(m, xrow_max(tmax));
        const float al = __builtin_amdgcn_exp2f((m - mn) * SC);
        m = mn; lsum *= al;
        const float a0 = dpp<0x00>(al), a1 = dpp<0x55>(al), a2 = dpp<0xAA>(al), a3 = dpp<0xFF>(al);
#pragma unroll
        for (int i = 0; i < 8; ++i) { acc[i][0] *= a0; acc[i][1] *= a1; acc[i][2] *= a2; acc[i][3] *= a3; }
      }
      const float mc = m * SC;
      const float p0 = __builtin_amdgcn_exp2f(fmaf(sc[0], SC, -mc)), p1 = __builtin_amdgcn_exp2f(fmaf(sc[1], SC, -mc));
      const float p2 = __builtin_amdgcn_exp2f(fmaf(sc[2], SC, -mc)), p3 = __builtin_amdgcn_exp2f(fmaf(sc[3], SC, -mc));
      lsum += (p0 + p1) + (p2 + p3);
      const u32x2 pa = {pk2(p0, p1), pk2(p2, p3)};
      wait_units(nis - 2 * j - 2);
      landed = landed > 2 * j + 2 ? landed : 2 * j + 2;
      refill(2 * j + 1);
#pragma unroll
      for (int i = 0; i < 8; ++i) acc[i] = mfma16(pa, trread(Vt + (va ^ (32 * i))), acc[i]);
      refill(2 * j + 2);
    }

    lsum = xrow_sum(lsum);
    if (g == 0) {
#pragma unroll
      for (int i = 0; i < 8; ++i) *(LAS f32x4*)(R + i * 256 + c * 16) = acc[i];
      if (c < 4) *(LAS f32x2*)(R + 2048 + c * 8) = f32x2{m * SC, lsum};
    }
  } else {
    const int hw = wave - 4;
    __builtin_amdgcn_s_setprio(3);
    u32x4 xa, xb; u32x2 ev;
    if (hw == 0 && lane < 16) *(LAS u32x4*)(L + L_FLAG + lane * 16) = u32x4{0, 0, 0, 0};
    const int kq = r & 3;
    if (NCODE && r * 4 + hw < NCODE) {
      dma1p(lbase + L_SCR, (const char*)(pc0 & ~255ull) + (r * 4 + hw) * 1024 + lane * 16);
    }
    {
      const u16* xp = p.x + (2 * hw) * DM + lane * 8;
      const u16* wp = p.wq + (size_t)(kq * 128 + hw * 32 + (lane >> 3)) * 4096 + xg * 512 + (r >> 2) * 64 + (lane & 7) * 8;
      const u32 ldst = lbase + L_STAGE + hw * 4096;
      u32 keep;
      asm volatile("global_load_dwordx4 %0, %4, off\n\tglobal_load_dwordx4 %1, %4, off offset:1024\n\tglobal_load_dwordx2 %2, %5, off\n\t"
                   "s_mov_b32 %3, m0\n\ts_mov_b32 m0, %10\n\ts_nop 0\n\t"
                   "global_load_lds_dwordx4 %6, off nt\n\ts_add_u32 m0, m0, 0x400\n\ts_nop 0\n\t"
                   "global_load_lds_dwordx4 %7, off nt\n\ts_add_u32 m0, m0, 0x400\n\ts_nop 0\n\t"
                   "global_load_lds_dwordx4 %8, off nt\n\ts_add_u32 m0, m0, 0x400\n\ts_nop 0\n\t"
                   "global_load_lds_dwordx4 %9, off nt\n\ts_mov_b32 m0, %3\n\t"
                   "s_waitcnt lgkmcnt(0)\n\ts_barrier\n\ts_waitcnt vmcnt(4)"
                   : "=&v"(xa), "=&v"(xb), "=&v"(ev), "=&s"(keep)
                   : "v"(xp), "v"(p.ws + WS_EPOCH), "v"(wp), "v"(wp + (size_t)8 * 4096), "v"(wp + (size_t)16 * 4096), "v"(wp + (size_t)24 * 4096), "s"(ldst)
                   : "memory", "scc");
    }
    const u64 ep = (((u64)ev.y << 32) | ev.x) + 1ull;
    const u32 tag32 = (u32)ep, t48lo = ((u32)ep) << 16, t48hi = (u32)(ep >> 16), t40lo = ((u32)ep) << 24, t40hi = (u32)(ep >> 8);
#pragma unroll
    for (int q = 0; q < 2; ++q) {
      const u32x4 xv = q ? xb : xa;
      float v[8];
#pragma unroll
      for (int i = 0; i < 4; ++i) { v[2 * i] = bflo(xv[i]); v[2 * i + 1] = bfhi(xv[i]); }
      const float mean = wave_sum(((v[0] + v[1]) + (v[2] + v[3])) + ((v[4] + v[5]) + (v[6] + v[7]))) * (1.f / DM);
      float q2 = 0.f;
#pragma unroll
      for (int i = 0; i < 8; ++i) { const float d = v[i] - mean; q2 = fmaf(d, d, q2); }
      const float var = rbf(wave_sum(q2) * (1.f / DM));
      const float rden = __builtin_amdgcn_rcpf(rbf(__builtin_amdgcn_sqrtf(rbf(var + 1e-5f))));
      const float mb = rbf(mean);
      u32x4 o;
#pragma unroll
      for (int i = 0; i < 4; ++i) { const u32 d = pk2(v[2 * i] - mb, v[2 * i + 1] - mb); o[i] = pk2(bflo(d) * rden, bfhi(d) * rden); }
      *(LAS u32x4*)(L + L_XN + (2 * hw + q) * XN_STR + lane * 16) = o;
    }
    const int tq = c >> 2, tp = c & 3;
    auto tile_mma = [&](const u32x4 (&act)[4], int cg) {
      f32x4 a0 = {0.f, 0.f, 0.f, 0.f}, a1 = {0.f, 0.f, 0.f, 0.f};
      LAS unsigned char* ap = L + L_STAGE + (8 * g + tq) * 128 + 32 * cg + 8 * tp;
      a0 = mfma32(act[0], trread2(ap, ap + 512), a0);
      a1 = mfma32(act[1], trread2(ap + 4096, ap + 4096 + 512), a1);
      a0 = mfma32(act[2], trread2(ap + 8192, ap + 8192 + 512), a0);
      a1 = mfma32(act[3], trread2(ap + 12288, ap + 12288 + 512), a1);
      return a0 + a1;
    };
    auto xn_frags = [&](u32x4 (&act)[4], int k0) {
#pragma unroll
      for (int s = 0; s < 4; ++s) act[s] = *(LAS u32x4*)(L + L_XN + (c & 7) * XN_STR + 2 * k0 + 64 * s + 16 * g);
    };
    auto f24 = [&](float f) { const u32 u = __float_as_uint(f); return t40lo | ((u + 0x7fu + ((u >> 8) & 1u)) >> 8); };
    auto ok40 = [&](const u32x4& t) { return ((t[0] ^ t40lo) >> 24) == 0 && t[1] == t40hi && ((t[2] ^ t40lo) >> 24) == 0 && t[3] == t40hi; };
    auto ok48 = [&](const u32x4& t) { return ((t[0] ^ t48lo) >> 16) == 0 && t[1] == t48hi && ((t[2] ^ t48lo) >> 16) == 0 && t[3] == t48hi; };
    u32x4 act[4];
    asm volatile("s_waitcnt vmcnt(0) lgkmcnt(0)" ::: "memory");
    if (lane == 0) lds_add(L + L_FLAG + F_SYNC, 1u);
    lds_wait_ge(L + L_FLAG + F_SYNC, 4);
    xn_frags(act, kq * 128);
    {
      const f32x4 d = tile_mma(act, hw);
      if (g < 2) {
#pragma unroll
        for (int i = 0; i < 4; ++i) st_gran(p.ws, WS_Q + ((((xg * 8 + 4 * g + i) * 4 + kq) * 512) + (r >> 2) * 64 + 16 * hw + c) * 8, f24(d[i]), t40hi);
      }
    }
    if (hw == 1 && lane == 0) st_gran(p.ws, WS_T0 + blk * 8, t40lo | (t0 & 0xffffffu), t40hi);
    LGKM0();
    if (lane == 0) lds_add(L + L_FLAG + F_SYNC, 1u);
    if (hw < 2) {
      const u32 qb = WS_Q + ((akv * 8 + ab) * 4) * 512 * 8;
      wait_until(t0 + TQ0 - (u32)(r * TQR) / 16);
      float q0[2], q1[2];
      NOUNROLL
      for (u32 n = 0; n < SPIN_GLB; ++n) {
        bool ok = true;
#pragma unroll
        for (int h = 0; h < 2; ++h) {
          q0[h] = 0.f; q1[h] = 0.f;
#pragma unroll
          for (int i = 0; i < 4; ++i) {
            const u32x4 t = __builtin_amdgcn_raw_buffer_load_b128(wsr, qb + (i * 512 + (2 * hw + h) * 128 + 2 * lane) * 8, 0, 16);
            ok &= ok40(t); q0[h] += __uint_as_float(t[0] << 8); q1[h] += __uint_as_float(t[2] << 8);
          }
        }
        if (__all(ok)) break;
        __builtin_amdgcn_s_sleep(POLLSLP);
        asm volatile("" ::: "memory");
      }
      *(LAS u32*)(L + L_Q + (2 * hw) * 256 + lane * 4) = pk2(q0[0], q1[0]);
      *(LAS u32*)(L + L_Q + (2 * hw + 1) * 256 + lane * 4) = pk2(q0[1], q1[1]);
      LGKM0();
      if (lane == 0) lds_add(L + L_FLAG + F_Q, 2u);
      if (hw == 1) {
        const u32 ld_ = lbase + L_SCR;
        u32 keep;
        const u16* tp = (lane < 32 ? p.k : p.v) + (size_t)apair * SK * DH + (size_t)(lane & 31) * 32768 + as * 8192 + 4096;
        if ((lane & 31) >= 4)
          asm volatile("s_mov_b32 %0, m0\n\ts_mov_b32 m0, %1\n\ts_nop 0\n\tglobal_load_lds_dword %2, off nt\n\ts_mov_b32 m0, %0" : "=&s"(keep) : "s"(ld_), "v"(tp) : "memory");
        if (TOUCH2) {
          const unsigned char* tq2 = lane < 8 ? p.ws + WS_OUT + ((lane * 32 + 4 * wcc) * 48 + 47) * 128
                                   : lane == 8 ? p.ws + WS_OUT + (((r >> 2) * 32 + 4 * xg + (r & 3)) * 48 + 47) * 128
                                   : (const unsigned char*)(p.out + (r >> 2) * DM + 16 * (4 * xg + (r & 3)));
          if (lane < 10)
            asm volatile("s_mov_b32 %0, m0\n\ts_mov_b32 m0, %1\n\ts_nop 0\n\tglobal_load_lds_dword %2, off\n\ts_mov_b32 m0, %0" : "=&s"(keep) : "s"(ld_), "v"(tq2) : "memory");
        }
      }
      if (hw == 0) {
        u64 ts = 0;
        bool ok = false;
        NOUNROLL
        for (u32 n = 0; n < 64 && !ok; ++n) {
          ts = ld_gran(p.ws, WS_T0 + (xg + 8 * (lane & 31)) * 8);
          ok = __all((((u32)ts ^ t40lo) >> 24) == 0 && (u32)(ts >> 32) == t40hi);
          if (!ok) __builtin_amdgcn_s_sleep(8);
        }
        int d = ((int)(((u32)ts - t0) << 8)) >> 8;
        d = min(d, __builtin_amdgcn_mov_dpp(d, 0xB1, 0xf, 0xf, true)); d = min(d, __builtin_amdgcn_mov_dpp(d, 0x4E, 0xf, 0xf, true));
        d = min(d, __builtin_amdgcn_mov_dpp(d, 0x141, 0xf, 0xf, true)); d = min(d, __builtin_amdgcn_mov_dpp(d, 0x128, 0xf, 0xf, true));
        int dm = min(min(__builtin_amdgcn_readlane(d, 0), __builtin_amdgcn_readlane(d, 16)), 0);
        dm = dm < -400 ? -400 : dm;
        if (ok && lane == 0) *(volatile LAS u32*)(L + L_FLAG + F_ANCH) = (t0 + dm) | 1u;
      }
      if (hw == 1) {
        wait_until(t0 + TGS);
        NOUNROLL
        for (u32 n = 0; n < SPIN_GLB; ++n) {
          const u32x4 a = __builtin_amdgcn_raw_buffer_load_b128(wsr, WS_START + lane * 16, 0, 16);
          const u32x4 b = __builtin_amdgcn_raw_buffer_load_b128(wsr, WS_START + 1024 + lane * 16, 0, 16);
          const u32 e0 = (u32)ep, e1 = (u32)(ep >> 32);
          const bool ok = a[0] == e0 && a[1] == e1 && a[2] == e0 && a[3] == e1 && b[0] == e0 && b[1] == e1 && b[2] == e0 && b[3] == e1;
          if (__all(ok)) break;
          __builtin_amdgcn_s_sleep(32);
          asm volatile("" ::: "memory");
        }
      }
    } else {
      const int h2 = hw - 2;
      u32 nsync2 = 0;
      auto arrive2 = [&]() {
        LGKM0();
        if (lane == 0) lds_add(L + L_FLAG + F_SYNC2, 1u);
        nsync2 += 2;
        lds_wait_ge(L + L_FLAG + F_SYNC2, nsync2);
      };
      auto stage2 = [&](const u16* w, int ld) {
        const u16* wp = w + (size_t)(h2 * 64 + (lane >> 3)) * ld + (lane & 7) * 8;
#pragma unroll
        for (int i = 0; i < 8; ++i) dma1(lbase + L_STAGE + h2 * 8192 + i * 1024, wp + (size_t)i * 8 * ld);
      };
      if (h2 == 1) {
        const u32x4 z = {0u, ~tag32, 0u, ~tag32};
#pragma unroll
        for (int i = 0; i < 2; ++i)
          __builtin_amdgcn_raw_buffer_store_b128(z, wsr, WS_OP + ((((akv * 4 + 2 * i + (lane >> 5)) * 8 + ab) * 4 + as) * 64) * 8 + (lane & 31) * 16, 0, 16);
        if (lane < 4) __builtin_amdgcn_raw_buffer_store_b128(z, wsr, WS_ML + (((akv * 4 + lane) * 8 + ab) * 4 + as) * 16, 0, 16);
      }
      lds_wait_ge(L + L_FLAG + F_SYNC, 8);
      const bool f1 = r < 14, f15 = r >= 14 && r < 18, f2 = r >= 18;
      const int t1 = xg * 14 + r, sl1 = t1 >> 2, fq = t1 & 3, ks = r - 18;
      const u16* wo_tile = p.wo + (size_t)whead * 128 * DM + wcc * 64;
      if (f1) stage2(p.wg + (size_t)fq * 128 * FF + sl1 * 64, FF);
      else if (f2) stage2(p.wd + (size_t)ks * 128 * DM + xg * 64, DM);
      else stage2(wo_tile, DM);
      VMCNT(0);
      if (h2 == 1 && lane == 0) {
        __hip_atomic_store((GAS u64*)(p.ws + WS_START + blk * 8), ep, __ATOMIC_RELAXED, __HIP_MEMORY_SCOPE_AGENT);
        *(volatile LAS u64*)(L + L_FLAG + F_EPOCH) = ep;
      }
      arrive2();
      if (f1) {
        xn_frags(act, fq * 128);
        f32x4 d[2];
        d[0] = tile_mma(act, 2 * h2); d[1] = tile_mma(act, 2 * h2 + 1);
        asm volatile("" : "+v"(d[0]), "+v"(d[1]));
        arrive2();
        stage2(p.wu + (size_t)fq * 128 * FF + sl1 * 64, FF);
#pragma unroll
        for (int w = 0; w < 2; ++w) if (g < 2) {
#pragma unroll
          for (int i = 0; i < 4; ++i) st_gran(p.ws, WS_GU + ((((sl1 * 2 + 0) * 4 + fq) * 8 + 4 * g + i) * 64 + 16 * (2 * h2 + w) + c) * 8, f24(d[w][i]), t40hi);
        }
        VMCNT(8);
        arrive2();
        d[0] = tile_mma(act, 2 * h2); d[1] = tile_mma(act, 2 * h2 + 1);
        asm volatile("" : "+v"(d[0]), "+v"(d[1]));
        arrive2();
        stage2(wo_tile, DM);
#pragma unroll
        for (int w = 0; w < 2; ++w) if (g < 2) {
#pragma unroll
          for (int i = 0; i < 4; ++i) st_gran(p.ws, WS_GU + ((((sl1 * 2 + 1) * 4 + fq) * 8 + 4 * g + i) * 64 + 16 * (2 * h2 + w) + c) * 8, f24(d[w][i]), t40hi);
        }
        VMCNT(8);
        arrive2();
      } else if (f15) {
        const int sl = xg * 4 + (r - 14);
        if (sl < 28) {
          wait_until(t0 + TG15);
          {
            const u32 so = WS_GU + ((((sl * 2 + 1) * 4 + (lane & 3)) * 8) * 64 + 63) * 8;
            NOUNROLL
            for (u32 n = 0; n < SPIN_GLB; ++n) {
              const u64 t = ld_gran(p.ws, so);
              if (__all((((u32)t ^ t40lo) >> 24) == 0 && (u32)(t >> 32) == t40hi)) break;
              __builtin_amdgcn_s_sleep(16);
            }
          }
          float gs[2][2], us[2][2];
          const int cp = lane & 31;
          NOUNROLL
          for (u32 n = 0; n < SPIN_GLB; ++n) {
            bool ok = true;
#pragma unroll
            for (int bb = 0; bb < 2; ++bb) {
              const int b = 4 * h2 + 2 * bb + (lane >> 5);
              gs[bb][0] = gs[bb][1] = us[bb][0] = us[bb][1] = 0.f;
#pragma unroll
              for (int i = 0; i < 4; ++i) {
                const u32x4 tg = __builtin_amdgcn_raw_buffer_load_b128(wsr, WS_GU + ((((sl * 2 + 0) * 4 + i) * 8 + b) * 64 + 2 * cp) * 8, 0, 16);
                const u32x4 tu = __builtin_amdgcn_raw_buffer_load_b128(wsr, WS_GU + ((((sl * 2 + 1) * 4 + i) * 8 + b) * 64 + 2 * cp) * 8, 0, 16);
                ok &= ok40(tg) && ok40(tu);
                gs[bb][0] += __uint_as_float(tg[0] << 8); gs[bb][1] += __uint_as_float(tg[2] << 8);
                us[bb][0] += __uint_as_float(tu[0] << 8); us[bb][1] += __uint_as_float(tu[2] << 8);
              }
            }
            if (__all(ok)) break;
            __builtin_amdgcn_s_sleep(16);
            asm volatile("" ::: "memory");
          }
#pragma unroll
          for (int bb = 0; bb < 2; ++bb) {
            const int b = 4 * h2 + 2 * bb + (lane >> 5);
            const u32 hp = pk2(rbf(gs[bb][0]) * rbf(us[bb][0]), rbf(gs[bb][1]) * rbf(us[bb][1]));
            st_gran(p.ws, WS_H + (b * FF + sl * 64 + 2 * cp) * 8, t48lo | (hp & 0xffffu), t48hi);
            st_gran(p.ws, WS_H + (b * FF + sl * 64 + 2 * cp + 1) * 8, t48lo | (hp >> 16), t48hi);
          }
        }
      } else {
        wait_until(t0 + TG2);
        {
          const u32 so = WS_H + (7 * FF + ks * 128 + 64 * (lane & 1) + 63) * 8;
          NOUNROLL
          for (u32 n = 0; n < SPIN_GLB; ++n) {
            const u64 t = ld_gran(p.ws, so);
            if (__all((((u32)t ^ t48lo) >> 16) == 0 && (u32)(t >> 32) == t48hi)) break;
            __builtin_amdgcn_s_sleep(32);
          }
        }
        const int b = lane >> 3, k0 = 64 * h2 + 8 * (lane & 7);
        u32x4 hv = {0, 0, 0, 0};
        NOUNROLL
        for (u32 n = 0; n < SPIN_GLB; ++n) {
          bool ok = true;
#pragma unroll
          for (int i = 0; i < 4; ++i) {
            const u32x4 t = __builtin_amdgcn_raw_buffer_load_b128(wsr, WS_H + (b * FF + ks * 128 + k0 + 2 * i) * 8, 0, 16);
            ok &= ok48(t);
            hv[i] = (t[0] & 0xffffu) | (t[2] << 16);
          }
          if (__all(ok)) break;
          __builtin_amdgcn_s_sleep(16);
          asm volatile("" ::: "memory");
        }
        *(LAS u32x4*)(L + L_O + b * 256 + k0 * 2) = hv;
        arrive2();
#pragma unroll
        for (int s = 0; s < 4; ++s) act[s] = *(LAS u32x4*)(L + L_O + (c & 7) * 256 + 64 * s + 16 * g);
        f32x4 d[2];
        d[0] = tile_mma(act, 2 * h2); d[1] = tile_mma(act, 2 * h2 + 1);
        asm volatile("" : "+v"(d[0]), "+v"(d[1]));
        arrive2();
        stage2(wo_tile, DM);
#pragma unroll
        for (int w = 0; w < 2; ++w) if (g < 2) {
#pragma unroll
          for (int i = 0; i < 4; ++i) st_gran(p.ws, WS_OUT + ((((4 * g + i) * 32 + 4 * xg + 2 * h2 + w) * 48) + 32 + ks) * 128 + c * 8, f24(d[w][i]), t40hi);
        }
        VMCNT(8);
        arrive2();
      }
      if (h2 == 0 && lane == 0) *(volatile LAS u32*)(L + L_FLAG + F_WO) = 1;
    }
    NOUNROLL
    for (u32 n = 0; n < SPIN_LDS; ++n) { if (lds_ld(L + L_FLAG + F_WO) >= 1) break; __builtin_amdgcn_s_sleep(16); }
    asm volatile("" ::: "memory");
  }

  u32x4 wof[4];
  if (wave >= 4) {
    const int tq = c >> 2, tp = c & 3, w = wave - 4;
#pragma unroll
    for (int s = 0; s < 4; ++s) { LAS unsigned char* ap = L + L_STAGE + (32 * s + 8 * g + tq) * 128 + 32 * w + 8 * tp; wof[s] = trread2(ap, ap + 512); }
  }
  bar();
  const u64 ep = *(volatile LAS u64*)(L + L_FLAG + F_EPOCH);
  const u32 tag32 = (u32)ep, t40lo = ((u32)ep) << 24, t40hi = (u32)(ep >> 8);
  if (wave < 4) {
    const int hh = wave, ch = lane >> 3, c0 = (2 * lane) & 15;
    float mw[4], lw[4], v0[4], v1[4];
#pragma unroll
    for (int w = 0; w < 4; ++w) {
      LAS unsigned char* R = L + L_RING + w * (RING * 4096);
      const f32x2 ml = *(LAS f32x2*)(R + 2048 + hh * 8);
      mw[w] = ml[0]; lw[w] = ml[1];
      v0[w] = *(LAS float*)(R + ch * 256 + c0 * 16 + hh * 4);
      v1[w] = *(LAS float*)(R + ch * 256 + c0 * 16 + 16 + hh * 4);
    }
    const float M = fmaxf(fmaxf(mw[0], mw[1]), fmaxf(mw[2], mw[3]));
    float Ls = 0.f, o0 = 0.f, o1 = 0.f;
#pragma unroll
    for (int w = 0; w < 4; ++w) { const float f = __builtin_amdgcn_exp2f(mw[w] - M); Ls = fmaf(f, lw[w], Ls); o0 = fmaf(f, v0[w], o0); o1 = fmaf(f, v1[w], o1); }
    const float inv = __builtin_amdgcn_rcpf(Ls);
    const u32 slot = ((akv * 4 + hh) * 8 + ab) * 4 + as;
    st_gran(p.ws, WS_OP + (slot * 64 + lane) * 8, pk2(o0 * inv, o1 * inv), tag32);
    if (lane < 2) st_gran(p.ws, WS_ML + (slot * 2 + lane) * 8, __float_as_uint(lane ? Ls : M), tag32);
  } else {
    const int w = wave - 4;
    const u32 slot = (whead * 8 + 2 * w) * 4;
    u64 o[8] = {0, 0, 0, 0, 0, 0, 0, 0}, ml = 0;
    __builtin_amdgcn_s_sleep(H1DLY);
    {
      u32 have = 0;
      NOUNROLL
      for (u32 n = 0; n < SPIN_GLB; ++n) {
        u64 t[9];
#pragma unroll
        for (int s = 0; s < 8; ++s) t[s] = ld_gran(p.ws, ((have >> s) & 1) ? WS_DUMMY : WS_OP + ((slot + s) * 64 + lane) * 8);
        t[8] = ld_gran(p.ws, ((have >> 8) & 1) ? WS_DUMMY : WS_ML + (slot * 2 + (lane & 15)) * 8);
#pragma unroll
        for (int s = 0; s < 9; ++s) {
          const bool fresh = !((have >> s) & 1);
          if (s < 8) o[s] = fresh ? t[s] : o[s]; else ml = fresh ? t[8] : ml;
          if (__all((u32)(t[s] >> 32) == tag32)) have |= 1u << s;
        }
        if (have == 0x1ffu) break;
        __builtin_amdgcn_s_sleep(POLLSLP);
      }
    }
    const u32 mlv = (u32)ml;
#pragma unroll
    for (int bb = 0; bb < 2; ++bb) {
      float ms[4], ls[4];
#pragma unroll
      for (int s = 0; s < 4; ++s) { ms[s] = __uint_as_float(__builtin_amdgcn_readlane(mlv, 8 * bb + 2 * s)); ls[s] = __uint_as_float(__builtin_amdgcn_readlane(mlv, 8 * bb + 2 * s + 1)); }
      const float M = fmaxf(fmaxf(ms[0], ms[1]), fmaxf(ms[2], ms[3]));
      float W = 0.f, o0 = 0.f, o1 = 0.f;
#pragma unroll
      for (int s = 0; s < 4; ++s) { const float wt = ls[s] * __builtin_amdgcn_exp2f(ms[s] - M); W += wt; o0 = fmaf(wt, bflo((u32)o[4 * bb + s]), o0); o1 = fmaf(wt, bfhi((u32)o[4 * bb + s]), o1); }
      const float inv = __builtin_amdgcn_rcpf(W);
      *(LAS u32*)(L + L_O + (2 * w + bb) * 256 + lane * 4) = pk2(o0 * inv, o1 * inv);
    }
    LGKM0();
    if (lane == 0) lds_add(L + L_FLAG + F_SYNC3, 1u);
    NOUNROLL
    for (u32 n = 0; n < SPIN_LDS; ++n) { if (lds_ld(L + L_FLAG + F_SYNC3) >= 4) break; }
    asm volatile("" ::: "memory");
    f32x4 a = {0.f, 0.f, 0.f, 0.f}, a2 = {0.f, 0.f, 0.f, 0.f};
    a = mfma32(*(LAS u32x4*)(L + L_O + (c & 7) * 256 + 16 * g), wof[0], a);
    a2 = mfma32(*(LAS u32x4*)(L + L_O + (c & 7) * 256 + 64 + 16 * g), wof[1], a2);
    a = mfma32(*(LAS u32x4*)(L + L_O + (c & 7) * 256 + 128 + 16 * g), wof[2], a);
    a2 = mfma32(*(LAS u32x4*)(L + L_O + (c & 7) * 256 + 192 + 16 * g), wof[3], a2);
    a += a2;
    if (g < 2) {
#pragma unroll
      for (int i = 0; i < 4; ++i) {
        const u32 u = __float_as_uint(a[i]);
        st_gran(p.ws, WS_OUT + ((((4 * g + i) * 32 + 4 * wcc + w) * 48) + whead) * 128 + c * 8, t40lo | ((u + 0x7fu + ((u >> 8) & 1u)) >> 8), t40hi);
      }
    }
    if (lane == 0) lds_add(L + L_FLAG + F_OUTP, 1u);
  }
  if (wave == 0) {
    const float xres = bflo(p.x[(r >> 2) * DM + 16 * (4 * xg + (r & 3)) + c]);
    const u32 base = WS_OUT + (((r >> 2) * 32 + 4 * xg + (r & 3)) * 48) * 128 + c * 8;
    u64 t[12] = {0, 0, 0, 0, 0, 0, 0, 0, 0, 0, 0, 0};
    lds_wait_ge(L + L_FLAG + F_OUTP, 4);
    __builtin_amdgcn_s_sleep(H2DLY);
    {
      u32 have = 0;
      NOUNROLL
      for (u32 n = 0; n < SPIN_GLB; ++n) {
        u64 tt[12];
#pragma unroll
        for (int i = 0; i < 12; ++i) { const int pr = (4 * i + g < 46) ? 4 * i + g : 45; tt[i] = ld_gran(p.ws, ((have >> i) & 1) ? WS_DUMMY : base + pr * 128); }
#pragma unroll
        for (int i = 0; i < 12; ++i) {
          t[i] = ((have >> i) & 1) ? t[i] : tt[i];
          if (__all((((u32)tt[i] ^ t40lo) >> 24) == 0 && (u32)(tt[i] >> 32) == t40hi)) have |= 1u << i;
        }
        if (have == 0xfffu) break;
        __builtin_amdgcn_s_sleep(POLLSLP);
      }
    }
    float sa = 0.f, sf = 0.f;
#pragma unroll
    for (int i = 0; i < 8; ++i) sa += __uint_as_float((u32)t[i] << 8);
#pragma unroll
    for (int i = 8; i < 12; ++i) sf += (4 * i + g < 46) ? __uint_as_float((u32)t[i] << 8) : 0.f;
    sa = xrow_sum(sa); sf = xrow_sum(sf);
    const float res = rbf(xres + rbf(sa)) + rbf(sf);
    if (g == 0) p.out[(r >> 2) * DM + 16 * (4 * xg + (r & 3)) + c] = (u16)(pk2(res, 0.f) & 0xffffu);
    if (blk == 0 && lane == 0) __hip_atomic_store((GAS u64*)(p.ws + WS_EPOCH), ep, __ATOMIC_RELAXED, __HIP_MEMORY_SCOPE_AGENT);
  }
}

extern "C" void kernel_launch(void* const* d_in, const int* in_sizes, int n_in, void* d_out, int out_size, void* d_ws, size_t ws_size, hipStream_t stream) {
  Params p;
  p.x = (const u16*)d_in[0]; p.k = (const u16*)d_in[1]; p.v = (const u16*)d_in[2]; p.wq = (const u16*)d_in[3]; p.wo = (const u16*)d_in[4];
  p.wg = (const u16*)d_in[6]; p.wu = (const u16*)d_in[7]; p.wd = (const u16*)d_in[8];
  p.out = (u16*)d_out; p.ws = (unsigned char*)d_ws;
  hipLaunchKernelGGL(fused_decode_kernel, dim3(NWG), dim3(NTHR), 0, stream, p);
}
```

```cpp
#include <hip/hip_runtime.h>
#include <stdint.h>

#define GAS __attribute__((address_space(1)))
#define LAS __attribute__((address_space(3)))
typedef unsigned u32;
typedef unsigned long long u64;
typedef unsigned short u16;
typedef u32 u32x4 __attribute__((ext_vector_type(4)));
typedef u32 u32x2 __attribute__((ext_vector_type(2)));
typedef float f32x4 __attribute__((ext_vector_type(4)));
typedef float f32x2 __attribute__((ext_vector_type(2)));
typedef short s16x4 __attribute__((ext_vector_type(4)));
typedef __bf16 bf16x8 __attribute__((ext_vector_type(8)));
typedef __bf16 bf16x2 __attribute__((ext_vector_type(2)));

constexpr int NCODE = 21;
constexpr int PACE10 = 71;
constexpr int PACE_T0 = 250;
constexpr int PACE_FROM = 8;
constexpr int TOUCH2 = 1;
#define TOUCHM 2
#define TLK 4
constexpr int DAHEAD = 3;
constexpr int DPRO = 1;
constexpr int TQ0 = 380, TQR = 48;
constexpr int TGS = 1000;
constexpr int TG15 = 950;
constexpr int TG2 = 1350;
constexpr int POLLSLP = 4, H1DLY = 18, H2DLY = 14;

constexpr int DM = 512, SK = 8192, DH = 128, FF = 1792;
constexpr int NWG = 256, NTHR = 512;
constexpr int RING = 8;
constexpr int NUNIT = 64;
constexpr float SC = 0.08838834764831845f * 1.4426950408889634f;
constexpr float THR_RAW = 8.0f / SC;

constexpr u32 WS_EPOCH = 0;
constexpr u32 WS_DUMMY = 2048;
constexpr u32 WS_START = 4096;
constexpr u32 WS_T0 = 8192;
constexpr u32 WS_Q = 65536;
constexpr u32 WS_GU = WS_Q + 8 * 8 * 4 * 512 * 8;
constexpr u32 WS_H = WS_GU + 28 * 2 * 4 * 8 * 64 * 8;
constexpr u32 WS_OP = WS_H + 8 * 1792 * 8;
constexpr u32 WS_ML = WS_OP + 32 * 8 * 4 * 64 * 8;
constexpr u32 WS_OUT = WS_ML + 32 * 8 * 4 * 2 * 8;
constexpr u32 WS_END = WS_OUT + 8 * 32 * 48 * 16 * 8;
static_assert(WS_END <= (8u << 20), "ws map");

constexpr u32 L_RING = 0;
constexpr u32 L_STAGE = 4 * RING * 4096;
constexpr u32 L_XN = L_STAGE + 16384;
constexpr u32 XN_STR = 1056;
constexpr u32 L_Q = L_XN + 8 * XN_STR;
constexpr u32 L_O = L_Q + 1024;
constexpr u32 L_FLAG = L_O + 2048;
constexpr u32 L_SCR = L_FLAG + 256;
constexpr u32 L_END = L_SCR + 1024;
static_assert(L_END <= 163840, "lds");
constexpr u32 F_SYNC = 0, F_Q = 4, F_SYNC2 = 8, F_WO = 12, F_EPOCH = 16, F_SYNC3 = 24, F_ANCH = 28, F_OUTP = 32;

constexpr u32 PER256 = (u32)(4096.0 * 1024.0 / (PACE10 * 0.1e12) * 1e8 * 256.0);
constexpr u32 SPIN_LDS = 1u << 22, SPIN_GLB = 1u << 17;

struct Params {
  const u16* x; const u16* k; const u16* v; const u16* wq; const u16* wo; const u16* wg; const u16* wu; const u16* wd;
  u16* out; unsigned char* ws;
};

__device__ __forceinline__ u32 rt32() { return (u32)__builtin_amdgcn_s_memrealtime(); }
__device__ __forceinline__ float bflo(u32 w) { return __uint_as_float(w << 16); }
__device__ __forceinline__ float bfhi(u32 w) { return __uint_as_float(w & 0xffff0000u); }
__device__ __forceinline__ u32 pk2(float lo, float hi) { f32x2 t = {lo, hi}; return __builtin_bit_cast(u32, __builtin_convertvector(t, bf16x2)); }
__device__ __forceinline__ float rbf(float f) { return bflo(pk2(f, f)); }
template <int CTRL> __device__ __forceinline__ float dpp(float x) {
  return __builtin_bit_cast(float, __builtin_amdgcn_mov_dpp(__builtin_bit_cast(int, x), CTRL, 0xf, 0xf, true));
}
__device__ __forceinline__ float xrow_max(float x) {
  auto s = __builtin_amdgcn_permlane16_swap(__float_as_uint(x), __float_as_uint(x), false, false);
  x = fmaxf(__uint_as_float(s[0]), __uint_as_float(s[1]));
  auto t = __builtin_amdgcn_permlane32_swap(__float_as_uint(x), __float_as_uint(x), false, false);
  return fmaxf(__uint_as_float(t[0]), __uint_as_float(t[1]));
}
__device__ __forceinline__ float xrow_sum(float x) {
  auto s = __builtin_amdgcn_permlane16_swap(__float_as_uint(x), __float_as_uint(x), false, false);
  x = __uint_as_float(s[0]) + __uint_as_float(s[1]);
  auto t = __builtin_amdgcn_permlane32_swap(__float_as_uint(x), __float_as_uint(x), false, false);
  return __uint_as_float(t[0]) + __uint_as_float(t[1]);
}
__device__ __forceinline__ float wave_sum(float x) {
  x += dpp<0xB1>(x); x += dpp<0x4E>(x); x += dpp<0x141>(x); x += dpp<0x128>(x);
  return xrow_sum(x);
}
__device__ __forceinline__ f32x4 mfma32(u32x4 a, u32x4 b, f32x4 c) {
  return __builtin_amdgcn_mfma_f32_16x16x32_bf16(__builtin_bit_cast(bf16x8, a), __builtin_bit_cast(bf16x8, b), c, 0, 0, 0);
}
__device__ __forceinline__ f32x4 mfma16(u32x2 a, u32x2 b, f32x4 c) {
  return __builtin_amdgcn_mfma_f32_16x16x16bf16_1k(__builtin_bit_cast(s16x4, a), __builtin_bit_cast(s16x4, b), c, 0, 0, 0);
}
__device__ __forceinline__ u32x2 trread(LAS unsigned char* p) {
  return __builtin_bit_cast(u32x2, __builtin_amdgcn_ds_read_tr16_b64_v4i16((LAS s16x4*)p));
}
__device__ __forceinline__ u32x4 trread2(LAS unsigned char* p0, LAS unsigned char* p1) {
  const u32x2 a = trread(p0), b = trread(p1); return u32x4{a.x, a.y, b.x, b.y};
}
__device__ __forceinline__ void dma4(u32 ldst, u32 v0, u32 v1, u32 v2, u32 v3, __amdgpu_buffer_rsrc_t srd, u32 soff) {
  u32 keep;
  asm volatile("s_waitcnt lgkmcnt(0)\n\ts_mov_b32 %0, m0\n\ts_mov_b32 m0, %1\n\ts_nop 0\n\t"
               "buffer_load_dwordx4 %2, %6, %7 offen nt lds\n\t"
               "buffer_load_dwordx4 %3, %6, %7 offen offset:1024 nt lds\n\t"
               "buffer_load_dwordx4 %4, %6, %7 offen offset:2048 nt lds\n\t"
               "buffer_load_dwordx4 %5, %6, %7 offen offset:3072 nt lds\n\t"
               "s_mov_b32 m0, %0"
               : "=&s"(keep) : "s"(ldst), "v"(v0), "v"(v1), "v"(v2), "v"(v3), "s"(srd), "s"(soff) : "memory");
}
__device__ __forceinline__ void dma1(u32 ldst, const void* gp) {
  u32 keep;
  asm volatile("s_mov_b32 %0, m0\n\ts_mov_b32 m0, %1\n\ts_nop 0\n\tglobal_load_lds_dwordx4 %2, off nt\n\ts_mov_b32 m0, %0"
               : "=&s"(keep) : "s"(ldst), "v"(gp) : "memory");
}
__device__ __forceinline__ void dma1p(u32 ldst, const void* gp) {
  u32 keep;
  asm volatile("s_mov_b32 %0, m0\n\ts_mov_b32 m0, %1\n\ts_nop 0\n\tglobal_load_lds_dwordx4 %2, off\n\ts_mov_b32 m0, %0"
               : "=&s"(keep) : "s"(ldst), "v"(gp) : "memory");
}
#define VMCNT(N) asm volatile("s_waitcnt vmcnt(" #N ")" ::: "memory")
#define LGKM0() asm volatile("s_waitcnt lgkmcnt(0)" ::: "memory")
#define NOUNROLL _Pragma("nounroll")
__device__ __forceinline__ void wait_units(int n) {
  switch (n) {
    case 0: VMCNT(0); break;   case 1: VMCNT(4); break;   case 2: VMCNT(8); break;   case 3: VMCNT(12); break;
    case 4: VMCNT(16); break;  case 5: VMCNT(20); break;  case 6: VMCNT(24); break;  default: VMCNT(28); break;
  }
}
__device__ __forceinline__ void bar() { LGKM0(); __builtin_amdgcn_s_barrier(); asm volatile("" ::: "memory"); }
__device__ __forceinline__ u32 lds_ld(LAS unsigned char* p) { return *(volatile LAS u32*)p; }
__device__ __forceinline__ void lds_add(LAS unsigned char* p, u32 v) { __hip_atomic_fetch_add((LAS u32*)p, v, __ATOMIC_RELAXED, __HIP_MEMORY_SCOPE_WORKGROUP); }
__device__ __forceinline__ void lds_wait_ge(LAS unsigned char* p, u32 want) {
  NOUNROLL
  for (u32 n = 0; n < SPIN_LDS; ++n) { if (lds_ld(p) >= want) break; __builtin_amdgcn_s_sleep(1); }
  asm volatile("" ::: "memory");
}
__device__ __forceinline__ void wait_until(u32 due) { while ((int)(rt32() - due) < 0) __builtin_amdgcn_s_sleep(4); }
__device__ __forceinline__ void st_gran(unsigned char* ws, u32 off, u32 lo, u32 hi) {
  __hip_atomic_store((GAS u64*)(ws + off), ((u64)hi << 32) | lo, __ATOMIC_RELAXED, __HIP_MEMORY_SCOPE_AGENT);
}
__device__ __forceinline__ u64 ld_gran(unsigned char* ws, u32 off) {
  return __hip_atomic_load((GAS u64*)(ws + off), __ATOMIC_RELAXED, __HIP_MEMORY_SCOPE_AGENT);
}

__global__ void __launch_bounds__(NTHR, 2) fused_decode_kernel(Params p) {
  __shared__ __attribute__((aligned(16))) unsigned char lds_raw[L_END];
  LAS unsigned char* L = (LAS unsigned char*)lds_raw;
  const u32 lbase = (u32)(size_t)L;
  const int tid = threadIdx.x, lane = tid & 63, wave = __builtin_amdgcn_readfirstlane(tid >> 6);
  const int g = lane >> 4, c = lane & 15;
  const int blk = blockIdx.x, xg = blk & 7, r = blk >> 3;
  const u32 t0 = rt32();
  u64 pc0; asm volatile("s_getpc_b64 %0" : "=s"(pc0));
  const __amdgpu_buffer_rsrc_t wsr = __builtin_amdgcn_make_buffer_rsrc(p.ws, 0, WS_END, 0x00020000);
  const int as = r & 3, ab = r >> 2, akv = xg, apair = ab * 8 + akv;
  const int whead = 4 * xg + (r & 3), wcc = r >> 2;

  if (wave < 4) {
    const size_t pbase = (size_t)apair * SK * DH;
    const __amdgpu_buffer_rsrc_t ksr = __builtin_amdgcn_make_buffer_rsrc((void*)(p.k + pbase), 0, SK * DH * 2, 0x00020000);
    const __amdgpu_buffer_rsrc_t vsr = __builtin_amdgcn_make_buffer_rsrc((void*)(p.v + pbase), 0, SK * DH * 2, 0x00020000);
    const u32 vk0 = g * 256 + 16 * (c ^ g), vk1 = g * 256 + 16 * (c ^ (4 + g)), vk2 = g * 256 + 16 * (c ^ (8 + g)), vk3 = g * 256 + 16 * (c ^ (12 + g));
    const u32 vv0 = g * 256 + 16 * (c ^ (g << 1)), vv1 = g * 256 + 16 * (c ^ ((4 + g) << 1));
    const u32 ring = lbase + L_RING + wave * (RING * 4096);
    int nis = 0;
    int landed = 0;
    u32 tnow = t0, pA = t0 + PACE_T0;
    const u32 sbase = as * 16384u + wave * 4096u;
    auto issue = [&]() {
      if (nis >= PACE_FROM) {
        if (nis < PACE_FROM + 6) { const u32 an = __builtin_amdgcn_readfirstlane(lds_ld(L + L_FLAG + F_ANCH)); if (an) pA = an + PACE_T0; }
        const u32 due = pA + (((u32)nis * PER256) >> 8);
        if ((int)(tnow - due) < 0) {
          NOUNROLL
          for (u32 n = 0; n < SPIN_LDS; ++n) { tnow = rt32(); if ((int)(tnow - due) >= 0) break; __builtin_amdgcn_s_sleep(1); }
        }
      }
      const u32 soff = (u32)(nis >> 1) * 65536u + sbase, ldst = ring + (u32)(nis & (RING - 1)) * 4096u;
      if (nis & 1) dma4(ldst, vv0, vv1, vv0, vv1, vsr, soff); else dma4(ldst, vk0, vk1, vk2, vk3, ksr, soff);
      ++nis;
    };
    issue();
    bar();
    NOUNROLL
    while (nis < RING) {
      if (nis >= DPRO) { wait_units(DPRO - 1); landed = nis - (DPRO - 1); }
      if (lds_ld(L + L_FLAG + F_Q) >= 4) break;
      issue();
    }
    auto refill = [&](int cons) { while (nis - landed < DAHEAD && nis < cons + RING && nis < NUNIT) issue(); };
    lds_wait_ge(L + L_FLAG + F_Q, 4);
    u32x4 qf[4];
#pragma unroll
    for (int s = 0; s < 4; ++s) qf[s] = *(LAS u32x4*)(L + L_Q + (c & 3) * 256 + 64 * s + 16 * g);
    refill(0);
    LAS unsigned char* R = L + L_RING + wave * (RING * 4096);
    const u32 ka = c * 256 + 16 * (g ^ c);
    const u32 va = (4 * g + (c >> 2)) * 256 + 32 * ((4 * g + (c >> 2)) & 7) + 8 * (c & 3);
    float m = -INFINITY, lsum = 0.f;
    f32x4 acc[8];
#pragma unroll
    for (int i = 0; i < 8; ++i) acc[i] = f32x4{0.f, 0.f, 0.f, 0.f};
    NOUNROLL
    for (int j = 0; j < NUNIT / 2; ++j) {
      wait_units(nis - 2 * j - 1);
      landed = landed > 2 * j + 1 ? landed : 2 * j + 1;
      refill(2 * j);
      LAS unsigned char* Kt = R + ((2 * j) & (RING - 1)) * 4096;
      LAS unsigned char* Vt = R + ((2 * j + 1) & (RING - 1)) * 4096;
      f32x4 sa = {0.f, 0.f, 0.f, 0.f}, sb = {0.f, 0.f, 0.f, 0.f};
      {
        const u32x4 k0 = *(LAS u32x4*)(Kt + ka), k1 = *(LAS u32x4*)(Kt + (ka ^ 64)), k2 = *(LAS u32x4*)(Kt + (ka ^ 128)), k3 = *(LAS u32x4*)(Kt + (ka ^ 192));
        sa = mfma32(k0, qf[0], sa); sb = mfma32(k1, qf[1], sb); sa = mfma32(k2, qf[2], sa); sb = mfma32(k3, qf[3], sb);
      }
      const f32x4 sc = sa + sb;
      const float tmax = fmaxf(fmaxf(sc[0], sc[1]), fmaxf(sc[2], sc[3]));
      if (__builtin_amdgcn_ballot_w64(tmax > m + THR_RAW) != 0ull) {
        const float mn = fmaxf(m, xrow_max(tmax));
        const float al = __builtin_amdgcn_exp2f((m - mn) * SC);
        m = mn; lsum *= al;
        const float a0 = dpp<0x00>(al), a1 = dpp<0x55>(al), a2 = dpp<0xAA>(al), a3 = dpp<0xFF>(al);
#pragma unroll
        for (int i = 0; i < 8; ++i) { acc[i][0] *= a0; acc[i][1] *= a1; acc[i][2] *= a2; acc[i][3] *= a3; }
      }
      const float mc = m * SC;
      const float p0 = __builtin_amdgcn_exp2f(fmaf(sc[0], SC, -mc)), p1 = __builtin_amdgcn_exp2f(fmaf(sc[1], SC, -mc));
      const float p2 = __builtin_amdgcn_exp2f(fmaf(sc[2], SC, -mc)), p3 = __builtin_amdgcn_exp2f(fmaf(sc[3], SC, -mc));
      lsum += (p0 + p1) + (p2 + p3);
      const u32x2 pa = {pk2(p0, p1), pk2(p2, p3)};
      wait_units(nis - 2 * j - 2);
      landed = landed > 2 * j + 2 ? landed : 2 * j + 2;
      refill(2 * j + 1);
#pragma unroll
      for (int i = 0; i < 8; ++i) acc[i] = mfma16(pa, trread(Vt + (va ^ (32 * i))), acc[i]);
      refill(2 * j + 2);
    }

    lsum = xrow_sum(lsum);
    if (g == 0) {
#pragma unroll
      for (int i = 0; i < 8; ++i) *(LAS f32x4*)(R + i * 256 + c * 16) = acc[i];
      if (c < 4) *(LAS f32x2*)(R + 2048 + c * 8) = f32x2{m * SC, lsum};
    }
  } else {
    const int hw = wave - 4;
    __builtin_amdgcn_s_setprio(3);
    u32x4 xa, xb; u32x2 ev;
    if (hw == 0 && lane < 16) *(LAS u32x4*)(L + L_FLAG + lane * 16) = u32x4{0, 0, 0, 0};
    const int kq = r & 3;
    if (NCODE && r * 4 + hw < NCODE) {
      dma1p(lbase + L_SCR, (const char*)(pc0 & ~255ull) + (r * 4 + hw) * 1024 + lane * 16);
    }
    {
      const u16* xp = p.x + (2 * hw) * DM + lane * 8;
      const u16* wp = p.wq + (size_t)(kq * 128 + hw * 32 + (lane >> 3)) * 4096 + xg * 512 + (r >> 2) * 64 + (lane & 7) * 8;
      const u32 ldst = lbase + L_STAGE + hw * 4096;
      u32 keep;
      asm volatile("global_load_dwordx4 %0, %4, off\n\tglobal_load_dwordx4 %1, %4, off offset:1024\n\tglobal_load_dwordx2 %2, %5, off\n\t"
                   "s_mov_b32 %3, m0\n\ts_mov_b32 m0, %10\n\ts_nop 0\n\t"
                   "global_load_lds_dwordx4 %6, off nt\n\ts_add_u32 m0, m0, 0x400\n\ts_nop 0\n\t"
                   "global_load_lds_dwordx4 %7, off nt\n\ts_add_u32 m0, m0, 0x400\n\ts_nop 0\n\t"
                   "global_load_lds_dwordx4 %8, off nt\n\ts_add_u32 m0, m0, 0x400\n\ts_nop 0\n\t"
                   "global_load_lds_dwordx4 %9, off nt\n\ts_mov_b32 m0, %3\n\t"
                   "s_waitcnt lgkmcnt(0)\n\ts_barrier\n\ts_waitcnt vmcnt(4)"
                   : "=&v"(xa), "=&v"(xb), "=&v"(ev), "=&s"(keep)
                   : "v"(xp), "v"(p.ws + WS_EPOCH), "v"(wp), "v"(wp + (size_t)8 * 4096), "v"(wp + (size_t)16 * 4096), "v"(wp + (size_t)24 * 4096), "s"(ldst)
                   : "memory", "scc");
    }
    const u64 ep = (((u64)ev.y << 32) | ev.x) + 1ull;
    const u32 tag32 = (u32)ep, t48lo = ((u32)ep) << 16, t48hi = (u32)(ep >> 16), t40lo = ((u32)ep) << 24, t40hi = (u32)(ep >> 8);
#pragma unroll
    for (int q = 0; q < 2; ++q) {
      const u32x4 xv = q ? xb : xa;
      float v[8];
#pragma unroll
      for (int i = 0; i < 4; ++i) { v[2 * i] = bflo(xv[i]); v[2 * i + 1] = bfhi(xv[i]); }
      const float mean = wave_sum(((v[0] + v[1]) + (v[2] + v[3])) + ((v[4] + v[5]) + (v[6] + v[7]))) * (1.f / DM);
      float q2 = 0.f;
#pragma unroll
      for (int i = 0; i < 8; ++i) { const float d = v[i] - mean; q2 = fmaf(d, d, q2); }
      const float var = rbf(wave_sum(q2) * (1.f / DM));
      const float rden = __builtin_amdgcn_rcpf(rbf(__builtin_amdgcn_sqrtf(rbf(var + 1e-5f))));
      const float mb = rbf(mean);
      u32x4 o;
#pragma unroll
      for (int i = 0; i < 4; ++i) { const u32 d = pk2(v[2 * i] - mb, v[2 * i + 1] - mb); o[i] = pk2(bflo(d) * rden, bfhi(d) * rden); }
      *(LAS u32x4*)(L + L_XN + (2 * hw + q) * XN_STR + lane * 16) = o;
    }
    const int tq = c >> 2, tp = c & 3;
    auto tile_mma = [&](const u32x4 (&act)[4], int cg) {
      f32x4 a0 = {0.f, 0.f, 0.f, 0.f}, a1 = {0.f, 0.f, 0.f, 0.f};
      LAS unsigned char* ap = L + L_STAGE + (8 * g + tq) * 128 + 32 * cg + 8 * tp;
      a0 = mfma32(act[0], trread2(ap, ap + 512), a0);
      a1 = mfma32(act[1], trread2(ap + 4096, ap + 4096 + 512), a1);
      a0 = mfma32(act[2], trread2(ap + 8192, ap + 8192 + 512), a0);
      a1 = mfma32(act[3], trread2(ap + 12288, ap + 12288 + 512), a1);
      return a0 + a1;
    };
    auto xn_frags = [&](u32x4 (&act)[4], int k0) {
#pragma unroll
      for (int s = 0; s < 4; ++s) act[s] = *(LAS u32x4*)(L + L_XN + (c & 7) * XN_STR + 2 * k0 + 64 * s + 16 * g);
    };
    auto f24 = [&](float f) { const u32 u = __float_as_uint(f); return t40lo | ((u + 0x7fu + ((u >> 8) & 1u)) >> 8); };
    auto ok40 = [&](const u32x4& t) { return ((t[0] ^ t40lo) >> 24) == 0 && t[1] == t40hi && ((t[2] ^ t40lo) >> 24) == 0 && t[3] == t40hi; };
    auto ok48 = [&](const u32x4& t) { return ((t[0] ^ t48lo) >> 16) == 0 && t[1] == t48hi && ((t[2] ^ t48lo) >> 16) == 0 && t[3] == t48hi; };
    u32x4 act[4];
    asm volatile("s_waitcnt vmcnt(0) lgkmcnt(0)" ::: "memory");
    if (lane == 0) lds_add(L + L_FLAG + F_SYNC, 1u);
    lds_wait_ge(L + L_FLAG + F_SYNC, 4);
    xn_frags(act, kq * 128);
    {
      const f32x4 d = tile_mma(act, hw);
      if (g < 2) {
#pragma unroll
        for (int i = 0; i < 4; ++i) st_gran(p.ws, WS_Q + ((((xg * 8 + 4 * g + i) * 4 + kq) * 512) + (r >> 2) * 64 + 16 * hw + c) * 8, f24(d[i]), t40hi);
      }
    }
    if (hw == 1 && lane == 0) st_gran(p.ws, WS_T0 + blk * 8, t40lo | (t0 & 0xffffffu), t40hi);
    LGKM0();
    if (lane == 0) lds_add(L + L_FLAG + F_SYNC, 1u);
    if (hw < 2) {
      const u32 qb = WS_Q + ((akv * 8 + ab) * 4) * 512 * 8;
      wait_until(t0 + TQ0 - (u32)(r * TQR) / 16);
      float q0[2], q1[2];
      NOUNROLL
      for (u32 n = 0; n < SPIN_GLB; ++n) {
        bool ok = true;
#pragma unroll
        for (int h = 0; h < 2; ++h) {
          q0[h] = 0.f; q1[h] = 0.f;
#pragma unroll
          for (int i = 0; i < 4; ++i) {
            const u32x4 t = __builtin_amdgcn_raw_buffer_load_b128(wsr, qb + (i * 512 + (2 * hw + h) * 128 + 2 * lane) * 8, 0, 16);
            ok &= ok40(t); q0[h] += __uint_as_float(t[0] << 8); q1[h] += __uint_as_float(t[2] << 8);
          }
        }
        if (__all(ok)) break;
        __builtin_amdgcn_s_sleep(POLLSLP);
        asm volatile("" ::: "memory");
      }
      *(LAS u32*)(L + L_Q + (2 * hw) * 256 + lane * 4) = pk2(q0[0], q1[0]);
      *(LAS u32*)(L + L_Q + (2 * hw + 1) * 256 + lane * 4) = pk2(q0[1], q1[1]);
      LGKM0();
      if (lane == 0) lds_add(L + L_FLAG + F_Q, 2u);
      if (hw == 1) {
        const u32 ld_ = lbase + L_SCR;
        u32 keep;
        const u16* tp = (lane < 32 ? p.k : p.v) + (size_t)apair * SK * DH + (size_t)(lane & 31) * 32768 + as * 8192 + 4096;
        if (TOUCHM == 0 && (lane & 31) >= 4)
          asm volatile("s_mov_b32 %0, m0\n\ts_mov_b32 m0, %1\n\ts_nop 0\n\tglobal_load_lds_dword %2, off nt\n\ts_mov_b32 m0, %0" : "=&s"(keep) : "s"(ld_), "v"(tp) : "memory");
        if (TOUCH2) {
          const unsigned char* tq2 = lane < 8 ? p.ws + WS_OUT + ((lane * 32 + 4 * wcc) * 48 + 47) * 128
                                   : lane == 8 ? p.ws + WS_OUT + (((r >> 2) * 32 + 4 * xg + (r & 3)) * 48 + 47) * 128
                                   : (const unsigned char*)(p.out + (r >> 2) * DM + 16 * (4 * xg + (r & 3)));
          if (lane < 10)
            asm volatile("s_mov_b32 %0, m0\n\ts_mov_b32 m0, %1\n\ts_nop 0\n\tglobal_load_lds_dword %2, off\n\ts_mov_b32 m0, %0" : "=&s"(keep) : "s"(ld_), "v"(tq2) : "memory");
        }
      }
      if (hw == 0) {
        u64 ts = 0;
        bool ok = false;
        NOUNROLL
        for (u32 n = 0; n < 64 && !ok; ++n) {
          ts = ld_gran(p.ws, WS_T0 + (xg + 8 * (lane & 31)) * 8);
          ok = __all((((u32)ts ^ t40lo) >> 24) == 0 && (u32)(ts >> 32) == t40hi);
          if (!ok) __builtin_amdgcn_s_sleep(8);
        }
        int d = ((int)(((u32)ts - t0) << 8)) >> 8;
        d = min(d, __builtin_amdgcn_mov_dpp(d, 0xB1, 0xf, 0xf, true)); d = min(d, __builtin_amdgcn_mov_dpp(d, 0x4E, 0xf, 0xf, true));
        d = min(d, __builtin_amdgcn_mov_dpp(d, 0x141, 0xf, 0xf, true)); d = min(d, __builtin_amdgcn_mov_dpp(d, 0x128, 0xf, 0xf, true));
        int dm = min(min(__builtin_amdgcn_readlane(d, 0), __builtin_amdgcn_readlane(d, 16)), 0);
        dm = dm < -400 ? -400 : dm;
        if (ok && lane == 0) *(volatile LAS u32*)(L + L_FLAG + F_ANCH) = (t0 + dm) | 1u;
      }
      if (hw == 1) {
        if (TOUCHM) {
          const int nl = TOUCHM == 1 ? 2 : 8;
          const u16* tb = ((lane & 1) ? p.v : p.k) + (size_t)apair * SK * DH + as * 8192 + ((lane >> 1) & 3) * 2048 + 1024;
          const u32 ld_ = lbase + L_SCR;
          NOUNROLL
          for (int j = 3; j < NUNIT / 2; ++j) {
            if (j > TLK + 3) wait_until(t0 + PACE_T0 + (((u32)(2 * (j - TLK)) * PER256) >> 8));
            const u16* tp = tb + (size_t)j * 32768;
            u32 keep;
            if (lane < nl)
              asm volatile("s_mov_b32 %0, m0\n\ts_mov_b32 m0, %1\n\ts_nop 0\n\tglobal_load_lds_dword %2, off nt\n\ts_mov_b32 m0, %0" : "=&s"(keep) : "s"(ld_), "v"(tp) : "memory");
          }
        }
        wait_until(t0 + TGS);
        NOUNROLL
        for (u32 n = 0; n < SPIN_GLB; ++n) {
          const u32x4 a = __builtin_amdgcn_raw_buffer_load_b128(wsr, WS_START + lane * 16, 0, 16);
          const u32x4 b = __builtin_amdgcn_raw_buffer_load_b128(wsr, WS_START + 1024 + lane * 16, 0, 16);
          const u32 e0 = (u32)ep, e1 = (u32)(ep >> 32);
          const bool ok = a[0] == e0 && a[1] == e1 && a[2] == e0 && a[3] == e1 && b[0] == e0 && b[1] == e1 && b[2] == e0 && b[3] == e1;
          if (__all(ok)) break;
          __builtin_amdgcn_s_sleep(32);
          asm volatile("" ::: "memory");
        }
      }
    } else {
      const int h2 = hw - 2;
      u32 nsync2 = 0;
      auto arrive2 = [&]() {
        LGKM0();
        if (lane == 0) lds_add(L + L_FLAG + F_SYNC2, 1u);
        nsync2 += 2;
        lds_wait_ge(L + L_FLAG + F_SYNC2, nsync2);
      };
      auto stage2 = [&](const u16* w, int ld) {
        const u16* wp = w + (size_t)(h2 * 64 + (lane >> 3)) * ld + (lane & 7) * 8;
#pragma unroll
        for (int i = 0; i < 8; ++i) dma1(lbase + L_STAGE + h2 * 8192 + i * 1024, wp + (size_t)i * 8 * ld);
      };
      if (h2 == 1) {
        const u32x4 z = {0u, ~tag32, 0u, ~tag32};
#pragma unroll
        for (int i = 0; i < 2; ++i)
          __builtin_amdgcn_raw_buffer_store_b128(z, wsr, WS_OP + ((((akv * 4 + 2 * i + (lane >> 5)) * 8 + ab) * 4 + as) * 64) * 8 + (lane & 31) * 16, 0, 16);
        if (lane < 4) __builtin_amdgcn_raw_buffer_store_b128(z, wsr, WS_ML + (((akv * 4 + lane) * 8 + ab) * 4 + as) * 16, 0, 16);
      }
      lds_wait_ge(L + L_FLAG + F_SYNC, 8);
      const bool f1 = r < 14, f15 = r >= 14 && r < 18, f2 = r >= 18;
      const int t1 = xg * 14 + r, sl1 = t1 >> 2, fq = t1 & 3, ks = r - 18;
      const u16* wo_tile = p.wo + (size_t)whead * 128 * DM + wcc * 64;
      if (f1) stage2(p.wg + (size_t)fq * 128 * FF + sl1 * 64, FF);
      else if (f2) stage2(p.wd + (size_t)ks * 128 * DM + xg * 64, DM);
      else stage2(wo_tile, DM);
      VMCNT(0);
      if (h2 == 1 && lane == 0) {
        __hip_atomic_store((GAS u64*)(p.ws + WS_START + blk * 8), ep, __ATOMIC_RELAXED, __HIP_MEMORY_SCOPE_AGENT);
        *(volatile LAS u64*)(L + L_FLAG + F_EPOCH) = ep;
      }
      arrive2();
      if (f1) {
        xn_frags(act, fq * 128);
        f32x4 d[2];
        d[0] = tile_mma(act, 2 * h2); d[1] = tile_mma(act, 2 * h2 + 1);
        asm volatile("" : "+v"(d[0]), "+v"(d[1]));
        arrive2();
        stage2(p.wu + (size_t)fq * 128 * FF + sl1 * 64, FF);
#pragma unroll
        for (int w = 0; w < 2; ++w) if (g < 2) {
#pragma unroll
          for (int i = 0; i < 4; ++i) st_gran(p.ws, WS_GU + ((((sl1 * 2 + 0) * 4 + fq) * 8 + 4 * g + i) * 64 + 16 * (2 * h2 + w) + c) * 8, f24(d[w][i]), t40hi);
        }
        VMCNT(8);
        arrive2();
        d[0] = tile_mma(act, 2 * h2); d[1] = tile_mma(act, 2 * h2 + 1);
        asm volatile("" : "+v"(d[0]), "+v"(d[1]));
        arrive2();
        stage2(wo_tile, DM);
#pragma unroll
        for (int w = 0; w < 2; ++w) if (g < 2) {
#pragma unroll
          for (int i = 0; i < 4; ++i) st_gran(p.ws, WS_GU + ((((sl1 * 2 + 1) * 4 + fq) * 8 + 4 * g + i) * 64 + 16 * (2 * h2 + w) + c) * 8, f24(d[w][i]), t40hi);
        }
        VMCNT(8);
        arrive2();
      } else if (f15) {
        const int sl = xg * 4 + (r - 14);
        if (sl < 28) {
          wait_until(t0 + TG15);
          {
            const u32 so = WS_GU + ((((sl * 2 + 1) * 4 + (lane & 3)) * 8) * 64 + 63) * 8;
            NOUNROLL
            for (u32 n = 0; n < SPIN_GLB; ++n) {
              const u64 t = ld_gran(p.ws, so);
              if (__all((((u32)t ^ t40lo) >> 24) == 0 && (u32)(t >> 32) == t40hi)) break;
              __builtin_amdgcn_s_sleep(16);
            }
          }
          float gs[2][2], us[2][2];
          const int cp = lane & 31;
          NOUNROLL
          for (u32 n = 0; n < SPIN_GLB; ++n) {
            bool ok = true;
#pragma unroll
            for (int bb = 0; bb < 2; ++bb) {
              const int b = 4 * h2 + 2 * bb + (lane >> 5);
              gs[bb][0] = gs[bb][1] = us[bb][0] = us[bb][1] = 0.f;
#pragma unroll
              for (int i = 0; i < 4; ++i) {
                const u32x4 tg = __builtin_amdgcn_raw_buffer_load_b128(wsr, WS_GU + ((((sl * 2 + 0) * 4 + i) * 8 + b) * 64 + 2 * cp) * 8, 0, 16);
                const u32x4 tu = __builtin_amdgcn_raw_buffer_load_b128(wsr, WS_GU + ((((sl * 2 + 1) * 4 + i) * 8 + b) * 64 + 2 * cp) * 8, 0, 16);
                ok &= ok40(tg) && ok40(tu);
                gs[bb][0] += __uint_as_float(tg[0] << 8); gs[bb][1] += __uint_as_float(tg[2] << 8);
                us[bb][0] += __uint_as_float(tu[0] << 8); us[bb][1] += __uint_as_float(tu[2] << 8);
              }
            }
            if (__all(ok)) break;
            __builtin_amdgcn_s_sleep(16);
            asm volatile("" ::: "memory");
          }
#pragma unroll
          for (int bb = 0; bb < 2; ++bb) {
            const int b = 4 * h2 + 2 * bb + (lane >> 5);
            const u32 hp = pk2(rbf(gs[bb][0]) * rbf(us[bb][0]), rbf(gs[bb][1]) * rbf(us[bb][1]));
            st_gran(p.ws, WS_H + (b * FF + sl * 64 + 2 * cp) * 8, t48lo | (hp & 0xffffu), t48hi);
            st_gran(p.ws, WS_H + (b * FF + sl * 64 + 2 * cp + 1) * 8, t48lo | (hp >> 16), t48hi);
          }
        }
      } else {
        wait_until(t0 + TG2);
        {
          const u32 so = WS_H + (7 * FF + ks * 128 + 64 * (lane & 1) + 63) * 8;
          NOUNROLL
          for (u32 n = 0; n < SPIN_GLB; ++n) {
            const u64 t = ld_gran(p.ws, so);
            if (__all((((u32)t ^ t48lo) >> 16) == 0 && (u32)(t >> 32) == t48hi)) break;
            __builtin_amdgcn_s_sleep(32);
          }
        }
        const int b = lane >> 3, k0 = 64 * h2 + 8 * (lane & 7);
        u32x4 hv = {0, 0, 0, 0};
        NOUNROLL
        for (u32 n = 0; n < SPIN_GLB; ++n) {
          bool ok = true;
#pragma unroll
          for (int i = 0; i < 4; ++i) {
            const u32x4 t = __builtin_amdgcn_raw_buffer_load_b128(wsr, WS_H + (b * FF + ks * 128 + k0 + 2 * i) * 8, 0, 16);
            ok &= ok48(t);
            hv[i] = (t[0] & 0xffffu) | (t[2] << 16);
          }
          if (__all(ok)) break;
          __builtin_amdgcn_s_sleep(16);
          asm volatile("" ::: "memory");
        }
        *(LAS u32x4*)(L + L_O + b * 256 + k0 * 2) = hv;
        arrive2();
#pragma unroll
        for (int s = 0; s < 4; ++s) act[s] = *(LAS u32x4*)(L + L_O + (c & 7) * 256 + 64 * s + 16 * g);
        f32x4 d[2];
        d[0] = tile_mma(act, 2 * h2); d[1] = tile_mma(act, 2 * h2 + 1);
        asm volatile("" : "+v"(d[0]), "+v"(d[1]));
        arrive2();
        stage2(wo_tile, DM);
#pragma unroll
        for (int w = 0; w < 2; ++w) if (g < 2) {
#pragma unroll
          for (int i = 0; i < 4; ++i) st_gran(p.ws, WS_OUT + ((((4 * g + i) * 32 + 4 * xg + 2 * h2 + w) * 48) + 32 + ks) * 128 + c * 8, f24(d[w][i]), t40hi);
        }
        VMCNT(8);
        arrive2();
      }
      if (h2 == 0 && lane == 0) *(volatile LAS u32*)(L + L_FLAG + F_WO) = 1;
    }
    NOUNROLL
    for (u32 n = 0; n < SPIN_LDS; ++n) { if (lds_ld(L + L_FLAG + F_WO) >= 1) break; __builtin_amdgcn_s_sleep(16); }
    asm volatile("" ::: "memory");
  }

  u32x4 wof[4];
  if (wave >= 4) {
    const int tq = c >> 2, tp = c & 3, w = wave - 4;
#pragma unroll
    for (int s = 0; s < 4; ++s) { LAS unsigned char* ap = L + L_STAGE + (32 * s + 8 * g + tq) * 128 + 32 * w + 8 * tp; wof[s] = trread2(ap, ap + 512); }
  }
  bar();
  const u64 ep = *(volatile LAS u64*)(L + L_FLAG + F_EPOCH);
  const u32 tag32 = (u32)ep, t40lo = ((u32)ep) << 24, t40hi = (u32)(ep >> 8);
  if (wave < 4) {
    const int hh = wave, ch = lane >> 3, c0 = (2 * lane) & 15;
    float mw[4], lw[4], v0[4], v1[4];
#pragma unroll
    for (int w = 0; w < 4; ++w) {
      LAS unsigned char* R = L + L_RING + w * (RING * 4096);
      const f32x2 ml = *(LAS f32x2*)(R + 2048 + hh * 8);
      mw[w] = ml[0]; lw[w] = ml[1];
      v0[w] = *(LAS float*)(R + ch * 256 + c0 * 16 + hh * 4);
      v1[w] = *(LAS float*)(R + ch * 256 + c0 * 16 + 16 + hh * 4);
    }
    const float M = fmaxf(fmaxf(mw[0], mw[1]), fmaxf(mw[2], mw[3]));
    float Ls = 0.f, o0 = 0.f, o1 = 0.f;
#pragma unroll
    for (int w = 0; w < 4; ++w) { const float f = __builtin_amdgcn_exp2f(mw[w] - M); Ls = fmaf(f, lw[w], Ls); o0 = fmaf(f, v0[w], o0); o1 = fmaf(f, v1[w], o1); }
    const float inv = __builtin_amdgcn_rcpf(Ls);
    const u32 slot = ((akv * 4 + hh) * 8 + ab) * 4 + as;
    st_gran(p.ws, WS_OP + (slot * 64 + lane) * 8, pk2(o0 * inv, o1 * inv), tag32);
    if (lane < 2) st_gran(p.ws, WS_ML + (slot * 2 + lane) * 8, __float_as_uint(lane ? Ls : M), tag32);
  } else {
    const int w = wave - 4;
    const u32 slot = (whead * 8 + 2 * w) * 4;
    u64 o[8] = {0, 0, 0, 0, 0, 0, 0, 0}, ml = 0;
    __builtin_amdgcn_s_sleep(H1DLY);
    {
      u32 have = 0;
      NOUNROLL
      for (u32 n = 0; n < SPIN_GLB; ++n) {
        u64 t[9];
#pragma unroll
        for (int s = 0; s < 8; ++s) t[s] = ld_gran(p.ws, ((have >> s) & 1) ? WS_DUMMY : WS_OP + ((slot + s) * 64 + lane) * 8);
        t[8] = ld_gran(p.ws, ((have >> 8) & 1) ? WS_DUMMY : WS_ML + (slot * 2 + (lane & 15)) * 8);
#pragma unroll
        for (int s = 0; s < 9; ++s) {
          const bool fresh = !((have >> s) & 1);
          if (s < 8) o[s] = fresh ? t[s] : o[s]; else ml = fresh ? t[8] : ml;
          if (__all((u32)(t[s] >> 32) == tag32)) have |= 1u << s;
        }
        if (have == 0x1ffu) break;
        __builtin_amdgcn_s_sleep(POLLSLP);
      }
    }
    const u32 mlv = (u32)ml;
#pragma unroll
    for (int bb = 0; bb < 2; ++bb) {
      float ms[4], ls[4];
#pragma unroll
      for (int s = 0; s < 4; ++s) { ms[s] = __uint_as_float(__builtin_amdgcn_readlane(mlv, 8 * bb + 2 * s)); ls[s] = __uint_as_float(__builtin_amdgcn_readlane(mlv, 8 * bb + 2 * s + 1)); }
      const float M = fmaxf(fmaxf(ms[0], ms[1]), fmaxf(ms[2], ms[3]));
      float W = 0.f, o0 = 0.f, o1 = 0.f;
#pragma unroll
      for (int s = 0; s < 4; ++s) { const float wt = ls[s] * __builtin_amdgcn_exp2f(ms[s] - M); W += wt; o0 = fmaf(wt, bflo((u32)o[4 * bb + s]), o0); o1 = fmaf(wt, bfhi((u32)o[4 * bb + s]), o1); }
      const float inv = __builtin_amdgcn_rcpf(W);
      *(LAS u32*)(L + L_O + (2 * w + bb) * 256 + lane * 4) = pk2(o0 * inv, o1 * inv);
    }
    LGKM0();
    if (lane == 0) lds_add(L + L_FLAG + F_SYNC3, 1u);
    NOUNROLL
    for (u32 n = 0; n < SPIN_LDS; ++n) { if (lds_ld(L + L_FLAG + F_SYNC3) >= 4) break; }
    asm volatile("" ::: "memory");
    f32x4 a = {0.f, 0.f, 0.f, 0.f}, a2 = {0.f, 0.f, 0.f, 0.f};
    a = mfma32(*(LAS u32x4*)(L + L_O + (c & 7) * 256 + 16 * g), wof[0], a);
    a2 = mfma32(*(LAS u32x4*)(L + L_O + (c & 7) * 256 + 64 + 16 * g), wof[1], a2);
    a = mfma32(*(LAS u32x4*)(L + L_O + (c & 7) * 256 + 128 + 16 * g), wof[2], a);
    a2 = mfma32(*(LAS u32x4*)(L + L_O + (c & 7) * 256 + 192 + 16 * g), wof[3], a2);
    a += a2;
    if (g < 2) {
#pragma unroll
      for (int i = 0; i < 4; ++i) {
        const u32 u = __float_as_uint(a[i]);
        st_gran(p.ws, WS_OUT + ((((4 * g + i) * 32 + 4 * wcc + w) * 48) + whead) * 128 + c * 8, t40lo | ((u + 0x7fu + ((u >> 8) & 1u)) >> 8), t40hi);
      }
    }
    if (lane == 0) lds_add(L + L_FLAG + F_OUTP, 1u);
  }
  if (wave == 0) {
    const float xres = bflo(p.x[(r >> 2) * DM + 16 * (4 * xg + (r & 3)) + c]);
    const u32 base = WS_OUT + (((r >> 2) * 32 + 4 * xg + (r & 3)) * 48) * 128 + c * 8;
    u64 t[12] = {0, 0, 0, 0, 0, 0, 0, 0, 0, 0, 0, 0};
    lds_wait_ge(L + L_FLAG + F_OUTP, 4);
    __builtin_amdgcn_s_sleep(H2DLY);
    {
      u32 have = 0;
      NOUNROLL
      for (u32 n = 0; n < SPIN_GLB; ++n) {
        u64 tt[12];
#pragma unroll
        for (int i = 0; i < 12; ++i) { const int pr = (4 * i + g < 46) ? 4 * i + g : 45; tt[i] = ld_gran(p.ws, ((have >> i) & 1) ? WS_DUMMY : base + pr * 128); }
#pragma unroll
        for (int i = 0; i < 12; ++i) {
          t[i] = ((have >> i) & 1) ? t[i] : tt[i];
          if (__all((((u32)tt[i] ^ t40lo) >> 24) == 0 && (u32)(tt[i] >> 32) == t40hi)) have |= 1u << i;
        }
        if (have == 0xfffu) break;
        __builtin_amdgcn_s_sleep(POLLSLP);
      }
    }
    float sa = 0.f, sf = 0.f;
#pragma unroll
    for (int i = 0; i < 8; ++i) sa += __uint_as_float((u32)t[i] << 8);
#pragma unroll
    for (int i = 8; i < 12; ++i) sf += (4 * i + g < 46) ? __uint_as_float((u32)t[i] << 8) : 0.f;
    sa = xrow_sum(sa); sf = xrow_sum(sf);
    const float res = rbf(xres + rbf(sa)) + rbf(sf);
    if (g == 0) p.out[(r >> 2) * DM + 16 * (4 * xg + (r & 3)) + c] = (u16)(pk2(res, 0.f) & 0xffffu);
    if (blk == 0 && lane == 0) __hip_atomic_store((GAS u64*)(p.ws + WS_EPOCH), ep, __ATOMIC_RELAXED, __HIP_MEMORY_SCOPE_AGENT);
  }
}

extern "C" void kernel_launch(void* const* d_in, const int* in_sizes, int n_in, void* d_out, int out_size, void* d_ws, size_t ws_size, hipStream_t stream) {
  Params p;
  p.x = (const u16*)d_in[0]; p.k = (const u16*)d_in[1]; p.v = (const u16*)d_in[2]; p.wq = (const u16*)d_in[3]; p.wo = (const u16*)d_in[4];
  p.wg = (const u16*)d_in[6]; p.wu = (const u16*)d_in[7]; p.wd = (const u16*)d_in[8];
  p.out = (u16*)d_out; p.ws = (unsigned char*)d_ws;
  hipLaunchKernelGGL(fused_decode_kernel, dim3(NWG), dim3(NTHR), 0, stream, p);
}
```

```cpp
#define T3 0
#define T4 0
#include <hip/hip_runtime.h>
#include <stdint.h>

#define GAS __attribute__((address_space(1)))
#define LAS __attribute__((address_space(3)))
typedef unsigned u32;
typedef unsigned long long u64;
typedef unsigned short u16;
typedef u32 u32x4 __attribute__((ext_vector_type(4)));
typedef u32 u32x2 __attribute__((ext_vector_type(2)));
typedef float f32x4 __attribute__((ext_vector_type(4)));
typedef float f32x2 __attribute__((ext_vector_type(2)));
typedef short s16x4 __attribute__((ext_vector_type(4)));
typedef __bf16 bf16x8 __attribute__((ext_vector_type(8)));
typedef __bf16 bf16x2 __attribute__((ext_vector_type(2)));

constexpr int NCODE = 21;
constexpr int PACE10 = 71;
constexpr int PACE_T0 = 250;
constexpr int PACE_FROM = 8;
constexpr int DAHEAD = 3;
constexpr int DPRO = 1;
constexpr int TQ0 = 380, TQR = 48;
constexpr int TGS = 1000;
constexpr int TG15 = 950;
constexpr int TG2 = 1350;
constexpr int POLLSLP = 4, H1DLY = 21, H2DLY = 13;

constexpr int DM = 512, SK = 8192, DH = 128, FF = 1792;
constexpr int NWG = 256, NTHR = 512;
constexpr int RING = 8;
constexpr int NUNIT = 64;
constexpr float SC = 0.08838834764831845f * 1.4426950408889634f;
constexpr float THR_RAW = 8.0f / SC;

constexpr u32 WS_EPOCH = 0;
constexpr u32 WS_DUMMY = 2048;
constexpr u32 WS_START = 4096;
constexpr u32 WS_T0 = 8192;
constexpr u32 WS_Q = 65536;
constexpr u32 WS_GU = WS_Q + 8 * 8 * 4 * 512 * 8;
constexpr u32 WS_H = WS_GU + 28 * 2 * 4 * 8 * 64 * 8;
constexpr u32 WS_OP = WS_H + 8 * 1792 * 8;
constexpr u32 WS_ML = WS_OP + 32 * 8 * 4 * 64 * 8;
constexpr u32 WS_OUT = WS_ML + 32 * 8 * 4 * 2 * 8;
constexpr u32 WS_END = WS_OUT + 8 * 32 * 48 * 16 * 8;
static_assert(WS_END <= (8u << 20), "ws map");

constexpr u32 L_RING = 0;
constexpr u32 L_STAGE = 4 * RING * 4096;
constexpr u32 L_XN = L_STAGE + 16384;
constexpr u32 XN_STR = 1056;
constexpr u32 L_Q = L_XN + 8 * XN_STR;
constexpr u32 L_O = L_Q + 1024;
constexpr u32 L_FLAG = L_O + 2048;
constexpr u32 L_SCR = L_FLAG + 256;
constexpr u32 L_END = L_SCR + 1024;
static_assert(L_END <= 163840, "lds");
constexpr u32 F_SYNC = 0, F_Q = 4, F_SYNC2 = 8, F_WO = 12, F_EPOCH = 16, F_SYNC3 = 24, F_ANCH = 28, F_OUTP = 32;

constexpr u32 PER256 = (u32)(4096.0 * 1024.0 / (PACE10 * 0.1e12) * 1e8 * 256.0);
constexpr u32 SPIN_LDS = 1u << 22, SPIN_GLB = 1u << 17;

struct Params {
  const u16* x; const u16* k; const u16* v; const u16* wq; const u16* wo; const u16* wg; const u16* wu; const u16* wd;
  u16* out; unsigned char* ws;
};

__device__ __forceinline__ u32 rt32() { return (u32)__builtin_amdgcn_s_memrealtime(); }
__device__ __forceinline__ float bflo(u32 w) { return __uint_as_float(w << 16); }
__device__ __forceinline__ float bfhi(u32 w) { return __uint_as_float(w & 0xffff0000u); }
__device__ __forceinline__ u32 pk2(float lo, float hi) { f32x2 t = {lo, hi}; return __builtin_bit_cast(u32, __builtin_convertvector(t, bf16x2)); }
__device__ __forceinline__ float rbf(float f) { return bflo(pk2(f, f)); }
template <int CTRL> __device__ __forceinline__ float dpp(float x) {
  return __builtin_bit_cast(float, __builtin_amdgcn_mov_dpp(__builtin_bit_cast(int, x), CTRL, 0xf, 0xf, true));
}
__device__ __forceinline__ float xrow_max(float x) {
  auto s = __builtin_amdgcn_permlane16_swap(__float_as_uint(x), __float_as_uint(x), false, false);
  x = fmaxf(__uint_as_float(s[0]), __uint_as_float(s[1]));
  auto t = __builtin_amdgcn_permlane32_swap(__float_as_uint(x), __float_as_uint(x), false, false);
  return fmaxf(__uint_as_float(t[0]), __uint_as_float(t[1]));
}
__device__ __forceinline__ float xrow_sum(float x) {
  auto s = __builtin_amdgcn_permlane16_swap(__float_as_uint(x), __float_as_uint(x), false, false);
  x = __uint_as_float(s[0]) + __uint_as_float(s[1]);
  auto t = __builtin_amdgcn_permlane32_swap(__float_as_uint(x), __float_as_uint(x), false, false);
  return __uint_as_float(t[0]) + __uint_as_float(t[1]);
}
__device__ __forceinline__ float wave_sum(float x) {
  x += dpp<0xB1>(x); x += dpp<0x4E>(x); x += dpp<0x141>(x); x += dpp<0x128>(x);
  return xrow_sum(x);
}
__device__ __forceinline__ f32x4 mfma32(u32x4 a, u32x4 b, f32x4 c) {
  return __builtin_amdgcn_mfma_f32_16x16x32_bf16(__builtin_bit_cast(bf16x8, a), __builtin_bit_cast(bf16x8, b), c, 0, 0, 0);
}
__device__ __forceinline__ f32x4 mfma16(u32x2 a, u32x2 b, f32x4 c) {
  return __builtin_amdgcn_mfma_f32_16x16x16bf16_1k(__builtin_bit_cast(s16x4, a), __builtin_bit_cast(s16x4, b), c, 0, 0, 0);
}
__device__ __forceinline__ u32x2 trread(LAS unsigned char* p) {
  return __builtin_bit_cast(u32x2, __builtin_amdgcn_ds_read_tr16_b64_v4i16((LAS s16x4*)p));
}
__device__ __forceinline__ u32x4 trread2(LAS unsigned char* p0, LAS unsigned char* p1) {
  const u32x2 a = trread(p0), b = trread(p1); return u32x4{a.x, a.y, b.x, b.y};
}
__device__ __forceinline__ void dma4(u32 ldst, u32 v0, u32 v1, u32 v2, u32 v3, __amdgpu_buffer_rsrc_t srd, u32 soff) {
  u32 keep;
  asm volatile("s_waitcnt lgkmcnt(0)\n\ts_mov_b32 %0, m0\n\ts_mov_b32 m0, %1\n\ts_nop 0\n\t"
               "buffer_load_dwordx4 %2, %6, %7 offen nt lds\n\t"
               "buffer_load_dwordx4 %3, %6, %7 offen offset:1024 nt lds\n\t"
               "buffer_load_dwordx4 %4, %6, %7 offen offset:2048 nt lds\n\t"
               "buffer_load_dwordx4 %5, %6, %7 offen offset:3072 nt lds\n\t"
               "s_mov_b32 m0, %0"
               : "=&s"(keep) : "s"(ldst), "v"(v0), "v"(v1), "v"(v2), "v"(v3), "s"(srd), "s"(soff) : "memory");
}
__device__ __forceinline__ void dma1(u32 ldst, const void* gp) {
  u32 keep;
  asm volatile("s_mov_b32 %0, m0\n\ts_mov_b32 m0, %1\n\ts_nop 0\n\tglobal_load_lds_dwordx4 %2, off nt\n\ts_mov_b32 m0, %0"
               : "=&s"(keep) : "s"(ldst), "v"(gp) : "memory");
}
__device__ __forceinline__ void dma1p(u32 ldst, const void* gp) {
  u32 keep;
  asm volatile("s_mov_b32 %0, m0\n\ts_mov_b32 m0, %1\n\ts_nop 0\n\tglobal_load_lds_dwordx4 %2, off\n\ts_mov_b32 m0, %0"
               : "=&s"(keep) : "s"(ldst), "v"(gp) : "memory");
}
#define VMCNT(N) asm volatile("s_waitcnt vmcnt(" #N ")" ::: "memory")
#define LGKM0() asm volatile("s_waitcnt lgkmcnt(0)" ::: "memory")
#define NOUNROLL _Pragma("nounroll")
__device__ __forceinline__ void wait_units(int n) {
  switch (n) {
    case 0: VMCNT(0); break;   case 1: VMCNT(4); break;   case 2: VMCNT(8); break;   case 3: VMCNT(12); break;
    case 4: VMCNT(16); break;  case 5: VMCNT(20); break;  case 6: VMCNT(24); break;  default: VMCNT(28); break;
  }
}
__device__ __forceinline__ void bar() { LGKM0(); __builtin_amdgcn_s_barrier(); asm volatile("" ::: "memory"); }
__device__ __forceinline__ u32 lds_ld(LAS unsigned char* p) { return *(volatile LAS u32*)p; }
__device__ __forceinline__ void lds_add(LAS unsigned char* p, u32 v) { __hip_atomic_fetch_add((LAS u32*)p, v, __ATOMIC_RELAXED, __HIP_MEMORY_SCOPE_WORKGROUP); }
__device__ __forceinline__ void lds_wait_ge(LAS unsigned char* p, u32 want) {
  NOUNROLL
  for (u32 n = 0; n < SPIN_LDS; ++n) { if (lds_ld(p) >= want) break; __builtin_amdgcn_s_sleep(1); }
  asm volatile("" ::: "memory");
}
__device__ __forceinline__ void wait_until(u32 due) { while ((int)(rt32() - due) < 0) __builtin_amdgcn_s_sleep(4); }
__device__ __forceinline__ void st_gran(unsigned char* ws, u32 off, u32 lo, u32 hi) {
  __hip_atomic_store((GAS u64*)(ws + off), ((u64)hi << 32) | lo, __ATOMIC_RELAXED, __HIP_MEMORY_SCOPE_AGENT);
}
__device__ __forceinline__ u64 ld_gran(unsigned char* ws, u32 off) {
  return __hip_atomic_load((GAS u64*)(ws + off), __ATOMIC_RELAXED, __HIP_MEMORY_SCOPE_AGENT);
}

__global__ void __launch_bounds__(NTHR, 2) fused_decode_kernel(Params p) {
  __shared__ __attribute__((aligned(16))) unsigned char lds_raw[L_END];
  LAS unsigned char* L = (LAS unsigned char*)lds_raw;
  const u32 lbase = (u32)(size_t)L;
  const int tid = threadIdx.x, lane = tid & 63, wave = __builtin_amdgcn_readfirstlane(tid >> 6);
  const int g = lane >> 4, c = lane & 15;
  const int blk = blockIdx.x, xg = blk & 7, r = blk >> 3;
  const u32 t0 = rt32();
  u64 pc0; asm volatile("s_getpc_b64 %0" : "=s"(pc0));
  const __amdgpu_buffer_rsrc_t wsr = __builtin_amdgcn_make_buffer_rsrc(p.ws, 0, WS_END, 0x00020000);
  const int as = r & 3, ab = r >> 2, akv = xg, apair = ab * 8 + akv;
  const int whead = 4 * xg + (r & 3), wcc = r >> 2;

  if (wave < 4) {
    const size_t pbase = (size_t)apair * SK * DH;
    const __amdgpu_buffer_rsrc_t ksr = __builtin_amdgcn_make_buffer_rsrc((void*)(p.k + pbase), 0, SK * DH * 2, 0x00020000);
    const __amdgpu_buffer_rsrc_t vsr = __builtin_amdgcn_make_buffer_rsrc((void*)(p.v + pbase), 0, SK * DH * 2, 0x00020000);
    const u32 vk0 = g * 256 + 16 * (c ^ g), vk1 = g * 256 + 16 * (c ^ (4 + g)), vk2 = g * 256 + 16 * (c ^ (8 + g)), vk3 = g * 256 + 16 * (c ^ (12 + g));
    const u32 vv0 = g * 256 + 16 * (c ^ (g << 1)), vv1 = g * 256 + 16 * (c ^ ((4 + g) << 1));
    const u32 ring = lbase + L_RING + wave * (RING * 4096);
    int nis = 0;
    int landed = 0;
    u32 tnow = t0, pA = t0 + PACE_T0;
    const u32 sbase = as * 16384u + wave * 4096u;
    auto issue = [&]() {
      if (nis >= PACE_FROM) {
        if (nis < PACE_FROM + 6) { const u32 an = __builtin_amdgcn_readfirstlane(lds_ld(L + L_FLAG + F_ANCH)); if (an) pA = an + PACE_T0; }
        const u32 due = pA + (((u32)nis * PER256) >> 8);
        if ((int)(tnow - due) < 0) {
          NOUNROLL
          for (u32 n = 0; n < SPIN_LDS; ++n) { tnow = rt32(); if ((int)(tnow - due) >= 0) break; __builtin_amdgcn_s_sleep(1); }
        }
      }
      const u32 soff = (u32)(nis >> 1) * 65536u + sbase, ldst = ring + (u32)(nis & (RING - 1)) * 4096u;
      if (nis & 1) dma4(ldst, vv0, vv1, vv0, vv1, vsr, soff); else dma4(ldst, vk0, vk1, vk2, vk3, ksr, soff);
      ++nis;
    };
    if (T4 && wave == 3) {
      u32 keep; const u32 ld_ = lbase + L_SCR;
      const unsigned char* tz = p.ws + WS_Q + ((((xg * 8 + lane) * 4 + (r & 3)) * 512) + (r >> 2) * 64) * 8;
      if (lane < 8)
        asm volatile("s_mov_b32 %0, m0\n\ts_mov_b32 m0, %1\n\ts_nop 0\n\tglobal_load_lds_dword %2, off\n\ts_mov_b32 m0, %0" : "=&s"(keep) : "s"(ld_), "v"(tz) : "memory");
    }
    issue();
    bar();
    NOUNROLL
    while (nis < RING) {
      if (nis >= DPRO) { wait_units(DPRO - 1); landed = nis - (DPRO - 1); }
      if (lds_ld(L + L_FLAG + F_Q) >= 4) break;
      issue();
    }
    auto refill = [&](int cons) { while (nis - landed < DAHEAD && nis < cons + RING && nis < NUNIT) issue(); };
    lds_wait_ge(L + L_FLAG + F_Q, 4);
    u32x4 qf[4];
#pragma unroll
    for (int s = 0; s < 4; ++s) qf[s] = *(LAS u32x4*)(L + L_Q + (c & 3) * 256 + 64 * s + 16 * g);
    refill(0);
    LAS unsigned char* R = L + L_RING + wave * (RING * 4096);
    const u32 ka = c * 256 + 16 * (g ^ c);
    const u32 va = (4 * g + (c >> 2)) * 256 + 32 * ((4 * g + (c >> 2)) & 7) + 8 * (c & 3);
    float m = -INFINITY, lsum = 0.f;
    f32x4 acc[8];
#pragma unroll
    for (int i = 0; i < 8; ++i) acc[i] = f32x4{0.f, 0.f, 0.f, 0.f};
    NOUNROLL
    for (int j = 0; j < NUNIT / 2; ++j) {
      wait_units(nis - 2 * j - 1);
      landed = landed > 2 * j + 1 ? landed : 2 * j + 1;
      refill(2 * j);
      LAS unsigned char* Kt = R + ((2 * j) & (RING - 1)) * 4096;
      LAS unsigned char* Vt = R + ((2 * j + 1) & (RING - 1)) * 4096;
      f32x4 sa = {0.f, 0.f, 0.f, 0.f}, sb = {0.f, 0.f, 0.f, 0.f};
      {
        const u32x4 k0 = *(LAS u32x4*)(Kt + ka), k1 = *(LAS u32x4*)(Kt + (ka ^ 64)), k2 = *(LAS u32x4*)(Kt + (ka ^ 128)), k3 = *(LAS u32x4*)(Kt + (ka ^ 192));
        sa = mfma32(k0, qf[0], sa); sb = mfma32(k1, qf[1], sb); sa = mfma32(k2, qf[2], sa); sb = mfma32(k3, qf[3], sb);
      }
      const f32x4 sc = sa + sb;
      const float tmax = fmaxf(fmaxf(sc[0], sc[1]), fmaxf(sc[2], sc[3]));
      if (__builtin_amdgcn_ballot_w64(tmax > m + THR_RAW) != 0ull) {
        const float mn = fmaxf(m, xrow_max(tmax));
        const float al = __builtin_amdgcn_exp2f((m - mn) * SC);
        m = mn; lsum *= al;
        const float a0 = dpp<0x00>(al), a1 = dpp<0x55>(al), a2 = dpp<0xAA>(al), a3 = dpp<0xFF>(al);
#pragma unroll
        for (int i = 0; i < 8; ++i) { acc[i][0] *= a0; acc[i][1] *= a1; acc[i][2] *= a2; acc[i][3] *= a3; }
      }
      const float mc = m * SC;
      const float p0 = __builtin_amdgcn_exp2f(fmaf(sc[0], SC, -mc)), p1 = __builtin_amdgcn_exp2f(fmaf(sc[1], SC, -mc));
      const float p2 = __builtin_amdgcn_exp2f(fmaf(sc[2], SC, -mc)), p3 = __builtin_amdgcn_exp2f(fmaf(sc[3], SC, -mc));
      lsum += (p0 + p1) + (p2 + p3);
      const u32x2 pa = {pk2(p0, p1), pk2(p2, p3)};
      wait_units(nis - 2 * j - 2);
      landed = landed > 2 * j + 2 ? landed : 2 * j + 2;
      refill(2 * j + 1);
#pragma unroll
      for (int i = 0; i < 8; ++i) acc[i] = mfma16(pa, trread(Vt + (va ^ (32 * i))), acc[i]);
      refill(2 * j + 2);
    }

    lsum = xrow_sum(lsum);
    if (g == 0) {
#pragma unroll
      for (int i = 0; i < 8; ++i) *(LAS f32x4*)(R + i * 256 + c * 16) = acc[i];
      if (c < 4) *(LAS f32x2*)(R + 2048 + c * 8) = f32x2{m * SC, lsum};
    }
  } else {
    const int hw = wave - 4;
    __builtin_amdgcn_s_setprio(3);
    u32x4 xa, xb; u32x2 ev;
    if (hw == 0 && lane < 16) *(LAS u32x4*)(L + L_FLAG + lane * 16) = u32x4{0, 0, 0, 0};
    const int kq = r & 3;
    if (NCODE && r * 4 + hw < NCODE) {
      dma1p(lbase + L_SCR, (const char*)(pc0 & ~255ull) + (r * 4 + hw) * 1024 + lane * 16);
    }
    {
      const u16* xp = p.x + (2 * hw) * DM + lane * 8;
      const u16* wp = p.wq + (size_t)(kq * 128 + hw * 32 + (lane >> 3)) * 4096 + xg * 512 + (r >> 2) * 64 + (lane & 7) * 8;
      const u32 ldst = lbase + L_STAGE + hw * 4096;
      u32 keep;
      asm volatile("global_load_dwordx4 %0, %4, off\n\tglobal_load_dwordx4 %1, %4, off offset:1024\n\tglobal_load_dwordx2 %2, %5, off\n\t"
                   "s_mov_b32 %3, m0\n\ts_mov_b32 m0, %10\n\ts_nop 0\n\t"
                   "global_load_lds_dwordx4 %6, off nt\n\ts_add_u32 m0, m0, 0x400\n\ts_nop 0\n\t"
                   "global_load_lds_dwordx4 %7, off nt\n\ts_add_u32 m0, m0, 0x400\n\ts_nop 0\n\t"
                   "global_load_lds_dwordx4 %8, off nt\n\ts_add_u32 m0, m0, 0x400\n\ts_nop 0\n\t"
                   "global_load_lds_dwordx4 %9, off nt\n\ts_mov_b32 m0, %3\n\t"
                   "s_waitcnt lgkmcnt(0)\n\ts_barrier\n\ts_waitcnt vmcnt(4)"
                   : "=&v"(xa), "=&v"(xb), "=&v"(ev), "=&s"(keep)
                   : "v"(xp), "v"(p.ws + WS_EPOCH), "v"(wp), "v"(wp + (size_t)8 * 4096), "v"(wp + (size_t)16 * 4096), "v"(wp + (size_t)24 * 4096), "s"(ldst)
                   : "memory", "scc");
    }
    const u64 ep = (((u64)ev.y << 32) | ev.x) + 1ull;
    const u32 tag32 = (u32)ep, t48lo = ((u32)ep) << 16, t48hi = (u32)(ep >> 16), t40lo = ((u32)ep) << 24, t40hi = (u32)(ep >> 8);
#pragma unroll
    for (int q = 0; q < 2; ++q) {
      const u32x4 xv = q ? xb : xa;
      float v[8];
#pragma unroll
      for (int i = 0; i < 4; ++i) { v[2 * i] = bflo(xv[i]); v[2 * i + 1] = bfhi(xv[i]); }
      const float mean = wave_sum(((v[0] + v[1]) + (v[2] + v[3])) + ((v[4] + v[5]) + (v[6] + v[7]))) * (1.f / DM);
      float q2 = 0.f;
#pragma unroll
      for (int i = 0; i < 8; ++i) { const float d = v[i] - mean; q2 = fmaf(d, d, q2); }
      const float var = rbf(wave_sum(q2) * (1.f / DM));
      const float rden = __builtin_amdgcn_rcpf(rbf(__builtin_amdgcn_sqrtf(rbf(var + 1e-5f))));
      const float mb = rbf(mean);
      u32x4 o;
#pragma unroll
      for (int i = 0; i < 4; ++i) { const u32 d = pk2(v[2 * i] - mb, v[2 * i + 1] - mb); o[i] = pk2(bflo(d) * rden, bfhi(d) * rden); }
      *(LAS u32x4*)(L + L_XN + (2 * hw + q) * XN_STR + lane * 16) = o;
    }
    const int tq = c >> 2, tp = c & 3;
    auto tile_mma = [&](const u32x4 (&act)[4], int cg) {
      f32x4 a0 = {0.f, 0.f, 0.f, 0.f}, a1 = {0.f, 0.f, 0.f, 0.f};
      LAS unsigned char* ap = L + L_STAGE + (8 * g + tq) * 128 + 32 * cg + 8 * tp;
      a0 = mfma32(act[0], trread2(ap, ap + 512), a0);
      a1 = mfma32(act[1], trread2(ap + 4096, ap + 4096 + 512), a1);
      a0 = mfma32(act[2], trread2(ap + 8192, ap + 8192 + 512), a0);
      a1 = mfma32(act[3], trread2(ap + 12288, ap + 12288 + 512), a1);
      return a0 + a1;
    };
    auto xn_frags = [&](u32x4 (&act)[4], int k0) {
#pragma unroll
      for (int s = 0; s < 4; ++s) act[s] = *(LAS u32x4*)(L + L_XN + (c & 7) * XN_STR + 2 * k0 + 64 * s + 16 * g);
    };
    auto f24 = [&](float f) { const u32 u = __float_as_uint(f); return t40lo | ((u + 0x7fu + ((u >> 8) & 1u)) >> 8); };
    auto ok40 = [&](const u32x4& t) { return ((t[0] ^ t40lo) >> 24) == 0 && t[1] == t40hi && ((t[2] ^ t40lo) >> 24) == 0 && t[3] == t40hi; };
    auto ok48 = [&](const u32x4& t) { return ((t[0] ^ t48lo) >> 16) == 0 && t[1] == t48hi && ((t[2] ^ t48lo) >> 16) == 0 && t[3] == t48hi; };
    u32x4 act[4];
    asm volatile("s_waitcnt vmcnt(0) lgkmcnt(0)" ::: "memory");
    if (lane == 0) lds_add(L + L_FLAG + F_SYNC, 1u);
    lds_wait_ge(L + L_FLAG + F_SYNC, 4);
    xn_frags(act, kq * 128);
    {
      const f32x4 d = tile_mma(act, hw);
      if (g < 2) {
#pragma unroll
        for (int i = 0; i < 4; ++i) st_gran(p.ws, WS_Q + ((((xg * 8 + 4 * g + i) * 4 + kq) * 512) + (r >> 2) * 64 + 16 * hw + c) * 8, f24(d[i]), t40hi);
      }
    }
    if (hw == 1 && lane == 0) st_gran(p.ws, WS_T0 + blk * 8, t40lo | (t0 & 0xffffffu), t40hi);
    LGKM0();
    if (lane == 0) lds_add(L + L_FLAG + F_SYNC, 1u);
    if (hw < 2) {
      const u32 qb = WS_Q + ((akv * 8 + ab) * 4) * 512 * 8;
      wait_until(t0 + TQ0 - (u32)(r * TQR) / 16);
      float q0[2], q1[2];
      NOUNROLL
      for (u32 n = 0; n < SPIN_GLB; ++n) {
        bool ok = true;
#pragma unroll
        for (int h = 0; h < 2; ++h) {
          q0[h] = 0.f; q1[h] = 0.f;
#pragma unroll
          for (int i = 0; i < 4; ++i) {
            const u32x4 t = __builtin_amdgcn_raw_buffer_load_b128(wsr, qb + (i * 512 + (2 * hw + h) * 128 + 2 * lane) * 8, 0, 16);
            ok &= ok40(t); q0[h] += __uint_as_float(t[0] << 8); q1[h] += __uint_as_float(t[2] << 8);
          }
        }
        if (__all(ok)) break;
        __builtin_amdgcn_s_sleep(POLLSLP);
        asm volatile("" ::: "memory");
      }
      *(LAS u32*)(L + L_Q + (2 * hw) * 256 + lane * 4) = pk2(q0[0], q1[0]);
      *(LAS u32*)(L + L_Q + (2 * hw + 1) * 256 + lane * 4) = pk2(q0[1], q1[1]);
      LGKM0();
      if (lane == 0) lds_add(L + L_FLAG + F_Q, 2u);
      if (hw == 1) {
        const u32 ld_ = lbase + L_SCR;
        u32 keep;
        const u16* tp = (lane < 32 ? p.k : p.v) + (size_t)apair * SK * DH + (size_t)(lane & 31) * 32768 + as * 8192 + 4096;
        if ((lane & 31) >= 4)
          asm volatile("s_mov_b32 %0, m0\n\ts_mov_b32 m0, %1\n\ts_nop 0\n\tglobal_load_lds_dword %2, off nt\n\ts_mov_b32 m0, %0" : "=&s"(keep) : "s"(ld_), "v"(tp) : "memory");
        const unsigned char* tw = lane < 8 ? p.ws + WS_OUT + ((lane * 32 + 4 * wcc) * 48 + 47) * 128
                                : lane == 8 ? p.ws + WS_OUT + (((r >> 2) * 32 + 4 * xg + (r & 3)) * 48 + 47) * 128
                                : (const unsigned char*)(p.out + (r >> 2) * DM + 16 * (4 * xg + (r & 3)));
        if (lane < 10)
          asm volatile("s_mov_b32 %0, m0\n\ts_mov_b32 m0, %1\n\ts_nop 0\n\tglobal_load_lds_dword %2, off\n\ts_mov_b32 m0, %0" : "=&s"(keep) : "s"(ld_), "v"(tw) : "memory");
        if (T3) {
          const int t1 = xg * 14 + (r < 14 ? r : 0);
          const u16* tz = lane < 4 ? p.wo + (size_t)(whead * 128 + lane * 42) * DM + wcc * 64 : p.wu + (size_t)((t1 & 3) * 128 + (lane - 4) * 14) * FF + (t1 >> 2) * 64;
          if (lane < (r < 14 ? 14 : 4))
            asm volatile("s_mov_b32 %0, m0\n\ts_mov_b32 m0, %1\n\ts_nop 0\n\tglobal_load_lds_dword %2, off nt\n\ts_mov_b32 m0, %0" : "=&s"(keep) : "s"(ld_), "v"(tz) : "memory");
        }
      }
      if (hw == 0) {
        u64 ts = 0;
        bool ok = false;
        NOUNROLL
        for (u32 n = 0; n < 64 && !ok; ++n) {
          ts = ld_gran(p.ws, WS_T0 + (xg + 8 * (lane & 31)) * 8);
          ok = __all((((u32)ts ^ t40lo) >> 24) == 0 && (u32)(ts >> 32) == t40hi);
          if (!ok) __builtin_amdgcn_s_sleep(8);
        }
        int d = ((int)(((u32)ts - t0) << 8)) >> 8;
        d = min(d, __builtin_amdgcn_mov_dpp(d, 0xB1, 0xf, 0xf, true)); d = min(d, __builtin_amdgcn_mov_dpp(d, 0x4E, 0xf, 0xf, true));
        d = min(d, __builtin_amdgcn_mov_dpp(d, 0x141, 0xf, 0xf, true)); d = min(d, __builtin_amdgcn_mov_dpp(d, 0x128, 0xf, 0xf, true));
        int dm = min(min(__builtin_amdgcn_readlane(d, 0), __builtin_amdgcn_readlane(d, 16)), 0);
        dm = dm < -400 ? -400 : dm;
        if (ok && lane == 0) *(volatile LAS u32*)(L + L_FLAG + F_ANCH) = (t0 + dm) | 1u;
      }
      if (hw == 1) {
        wait_until(t0 + TGS);
        NOUNROLL
        for (u32 n = 0; n < SPIN_GLB; ++n) {
          const u32x4 a = __builtin_amdgcn_raw_buffer_load_b128(wsr, WS_START + lane * 16, 0, 16);
          const u32x4 b = __builtin_amdgcn_raw_buffer_load_b128(wsr, WS_START + 1024 + lane * 16, 0, 16);
          const u32 e0 = (u32)ep, e1 = (u32)(ep >> 32);
          const bool ok = a[0] == e0 && a[1] == e1 && a[2] == e0 && a[3] == e1 && b[0] == e0 && b[1] == e1 && b[2] == e0 && b[3] == e1;
          if (__all(ok)) break;
          __builtin_amdgcn_s_sleep(32);
          asm volatile("" ::: "memory");
        }
      }
    } else {
      const int h2 = hw - 2;
      u32 nsync2 = 0;
      auto arrive2 = [&]() {
        LGKM0();
        if (lane == 0) lds_add(L + L_FLAG + F_SYNC2, 1u);
        nsync2 += 2;
        lds_wait_ge(L + L_FLAG + F_SYNC2, nsync2);
      };
      auto stage2 = [&](const u16* w, int ld) {
        const u16* wp = w + (size_t)(h2 * 64 + (lane >> 3)) * ld + (lane & 7) * 8;
#pragma unroll
        for (int i = 0; i < 8; ++i) dma1(lbase + L_STAGE + h2 * 8192 + i * 1024, wp + (size_t)i * 8 * ld);
      };
      if (h2 == 1) {
        const u32x4 z = {0u, ~tag32, 0u, ~tag32};
#pragma unroll
        for (int i = 0; i < 2; ++i)
          __builtin_amdgcn_raw_buffer_store_b128(z, wsr, WS_OP + ((((akv * 4 + 2 * i + (lane >> 5)) * 8 + ab) * 4 + as) * 64) * 8 + (lane & 31) * 16, 0, 16);
        if (lane < 4) __builtin_amdgcn_raw_buffer_store_b128(z, wsr, WS_ML + (((akv * 4 + lane) * 8 + ab) * 4 + as) * 16, 0, 16);
      }
      lds_wait_ge(L + L_FLAG + F_SYNC, 8);
      const bool f1 = r < 14, f15 = r >= 14 && r < 18, f2 = r >= 18;
      const int t1 = xg * 14 + r, sl1 = t1 >> 2, fq = t1 & 3, ks = r - 18;
      const u16* wo_tile = p.wo + (size_t)whead * 128 * DM + wcc * 64;
      if (f1) stage2(p.wg + (size_t)fq * 128 * FF + sl1 * 64, FF);
      else if (f2) stage2(p.wd + (size_t)ks * 128 * DM + xg * 64, DM);
      else stage2(wo_tile, DM);
      VMCNT(0);
      if (h2 == 1 && lane == 0) {
        __hip_atomic_store((GAS u64*)(p.ws + WS_START + blk * 8), ep, __ATOMIC_RELAXED, __HIP_MEMORY_SCOPE_AGENT);
        *(volatile LAS u64*)(L + L_FLAG + F_EPOCH) = ep;
      }
      arrive2();
      if (f1) {
        xn_frags(act, fq * 128);
        f32x4 d[2];
        d[0] = tile_mma(act, 2 * h2); d[1] = tile_mma(act, 2 * h2 + 1);
        asm volatile("" : "+v"(d[0]), "+v"(d[1]));
        arrive2();
        stage2(p.wu + (size_t)fq * 128 * FF + sl1 * 64, FF);
#pragma unroll
        for (int w = 0; w < 2; ++w) if (g < 2) {
#pragma unroll
          for (int i = 0; i < 4; ++i) st_gran(p.ws, WS_GU + ((((sl1 * 2 + 0) * 4 + fq) * 8 + 4 * g + i) * 64 + 16 * (2 * h2 + w) + c) * 8, f24(d[w][i]), t40hi);
        }
        VMCNT(8);
        arrive2();
        d[0] = tile_mma(act, 2 * h2); d[1] = tile_mma(act, 2 * h2 + 1);
        asm volatile("" : "+v"(d[0]), "+v"(d[1]));
        arrive2();
        stage2(wo_tile, DM);
#pragma unroll
        for (int w = 0; w < 2; ++w) if (g < 2) {
#pragma unroll
          for (int i = 0; i < 4; ++i) st_gran(p.ws, WS_GU + ((((sl1 * 2 + 1) * 4 + fq) * 8 + 4 * g + i) * 64 + 16 * (2 * h2 + w) + c) * 8, f24(d[w][i]), t40hi);
        }
        VMCNT(8);
        arrive2();
      } else if (f15) {
        const int sl = xg * 4 + (r - 14);
        if (sl < 28) {
          wait_until(t0 + TG15);
          {
            const u32 so = WS_GU + ((((sl * 2 + 1) * 4 + (lane & 3)) * 8) * 64 + 63) * 8;
            NOUNROLL
            for (u32 n = 0; n < SPIN_GLB; ++n) {
              const u64 t = ld_gran(p.ws, so);
              if (__all((((u32)t ^ t40lo) >> 24) == 0 && (u32)(t >> 32) == t40hi)) break;
              __builtin_amdgcn_s_sleep(16);
            }
          }
          float gs[2][2], us[2][2];
          const int cp = lane & 31;
          NOUNROLL
          for (u32 n = 0; n < SPIN_GLB; ++n) {
            bool ok = true;
#pragma unroll
            for (int bb = 0; bb < 2; ++bb) {
              const int b = 4 * h2 + 2 * bb + (lane >> 5);
              gs[bb][0] = gs[bb][1] = us[bb][0] = us[bb][1] = 0.f;
#pragma unroll
              for (int i = 0; i < 4; ++i) {
                const u32x4 tg = __builtin_amdgcn_raw_buffer_load_b128(wsr, WS_GU + ((((sl * 2 + 0) * 4 + i) * 8 + b) * 64 + 2 * cp) * 8, 0, 16);
                const u32x4 tu = __builtin_amdgcn_raw_buffer_load_b128(wsr, WS_GU + ((((sl * 2 + 1) * 4 + i) * 8 + b) * 64 + 2 * cp) * 8, 0, 16);
                ok &= ok40(tg) && ok40(tu);
                gs[bb][0] += __uint_as_float(tg[0] << 8); gs[bb][1] += __uint_as_float(tg[2] << 8);
                us[bb][0] += __uint_as_float(tu[0] << 8); us[bb][1] += __uint_as_float(tu[2] << 8);
              }
            }
            if (__all(ok)) break;
            __builtin_amdgcn_s_sleep(16);
            asm volatile("" ::: "memory");
          }
#pragma unroll
          for (int bb = 0; bb < 2; ++bb) {
            const int b = 4 * h2 + 2 * bb + (lane >> 5);
            const u32 hp = pk2(rbf(gs[bb][0]) * rbf(us[bb][0]), rbf(gs[bb][1]) * rbf(us[bb][1]));
            st_gran(p.ws, WS_H + (b * FF + sl * 64 + 2 * cp) * 8, t48lo | (hp & 0xffffu), t48hi);
            st_gran(p.ws, WS_H + (b * FF + sl * 64 + 2 * cp + 1) * 8, t48lo | (hp >> 16), t48hi);
          }
        }
      } else {
        wait_until(t0 + TG2);
        {
          const u32 so = WS_H + (7 * FF + ks * 128 + 64 * (lane & 1) + 63) * 8;
          NOUNROLL
          for (u32 n = 0; n < SPIN_GLB; ++n) {
            const u64 t = ld_gran(p.ws, so);
            if (__all((((u32)t ^ t48lo) >> 16) == 0 && (u32)(t >> 32) == t48hi)) break;
            __builtin_amdgcn_s_sleep(32);
          }
        }
        const int b = lane >> 3, k0 = 64 * h2 + 8 * (lane & 7);
        u32x4 hv = {0, 0, 0, 0};
        NOUNROLL
        for (u32 n = 0; n < SPIN_GLB; ++n) {
          bool ok = true;
#pragma unroll
          for (int i = 0; i < 4; ++i) {
            const u32x4 t = __builtin_amdgcn_raw_buffer_load_b128(wsr, WS_H + (b * FF + ks * 128 + k0 + 2 * i) * 8, 0, 16);
            ok &= ok48(t);
            hv[i] = (t[0] & 0xffffu) | (t[2] << 16);
          }
          if (__all(ok)) break;
          __builtin_amdgcn_s_sleep(16);
          asm volatile("" ::: "memory");
        }
        *(LAS u32x4*)(L + L_O + b * 256 + k0 * 2) = hv;
        arrive2();
#pragma unroll
        for (int s = 0; s < 4; ++s) act[s] = *(LAS u32x4*)(L + L_O + (c & 7) * 256 + 64 * s + 16 * g);
        f32x4 d[2];
        d[0] = tile_mma(act, 2 * h2); d[1] = tile_mma(act, 2 * h2 + 1);
        asm volatile("" : "+v"(d[0]), "+v"(d[1]));
        arrive2();
        stage2(wo_tile, DM);
#pragma unroll
        for (int w = 0; w < 2; ++w) if (g < 2) {
#pragma unroll
          for (int i = 0; i < 4; ++i) st_gran(p.ws, WS_OUT + ((((4 * g + i) * 32 + 4 * xg + 2 * h2 + w) * 48) + 32 + ks) * 128 + c * 8, f24(d[w][i]), t40hi);
        }
        VMCNT(8);
        arrive2();
      }
      if (h2 == 0 && lane == 0) *(volatile LAS u32*)(L + L_FLAG + F_WO) = 1;
    }
    NOUNROLL
    for (u32 n = 0; n < SPIN_LDS; ++n) { if (lds_ld(L + L_FLAG + F_WO) >= 1) break; __builtin_amdgcn_s_sleep(16); }
    asm volatile("" ::: "memory");
  }

  u32x4 wof[4];
  if (wave >= 4) {
    const int tq = c >> 2, tp = c & 3, w = wave - 4;
#pragma unroll
    for (int s = 0; s < 4; ++s) { LAS unsigned char* ap = L + L_STAGE + (32 * s + 8 * g + tq) * 128 + 32 * w + 8 * tp; wof[s] = trread2(ap, ap + 512); }
  }
  bar();
  const u64 ep = *(volatile LAS u64*)(L + L_FLAG + F_EPOCH);
  const u32 tag32 = (u32)ep, t40lo = ((u32)ep) << 24, t40hi = (u32)(ep >> 8);
  if (wave < 4) {
    const int hh = wave, ch = lane >> 3, c0 = (2 * lane) & 15;
    float mw[4], lw[4], v0[4], v1[4];
#pragma unroll
    for (int w = 0; w < 4; ++w) {
      LAS unsigned char* R = L + L_RING + w * (RING * 4096);
      const f32x2 ml = *(LAS f32x2*)(R + 2048 + hh * 8);
      mw[w] = ml[0]; lw[w] = ml[1];
      v0[w] = *(LAS float*)(R + ch * 256 + c0 * 16 + hh * 4);
      v1[w] = *(LAS float*)(R + ch * 256 + c0 * 16 + 16 + hh * 4);
    }
    const float M = fmaxf(fmaxf(mw[0], mw[1]), fmaxf(mw[2], mw[3]));
    float Ls = 0.f, o0 = 0.f, o1 = 0.f;
#pragma unroll
    for (int w = 0; w < 4; ++w) { const float f = __builtin_amdgcn_exp2f(mw[w] - M); Ls = fmaf(f, lw[w], Ls); o0 = fmaf(f, v0[w], o0); o1 = fmaf(f, v1[w], o1); }
    const float inv = __builtin_amdgcn_rcpf(Ls);
    const u32 slot = ((akv * 4 + hh) * 8 + ab) * 4 + as;
    st_gran(p.ws, WS_OP + (slot * 64 + lane) * 8, pk2(o0 * inv, o1 * inv), tag32);
    if (lane < 2) st_gran(p.ws, WS_ML + (slot * 2 + lane) * 8, __float_as_uint(lane ? Ls : M), tag32);
  } else {
    const int w = wave - 4;
    const u32 slot = (whead * 8 + 2 * w) * 4;
    u64 o[8] = {0, 0, 0, 0, 0, 0, 0, 0}, ml = 0;
    __builtin_amdgcn_s_sleep(H1DLY);
    {
      u32 have = 0;
      NOUNROLL
      for (u32 n = 0; n < SPIN_GLB; ++n) {
        u64 t[9];
#pragma unroll
        for (int s = 0; s < 8; ++s) t[s] = ld_gran(p.ws, ((have >> s) & 1) ? WS_DUMMY : WS_OP + ((slot + s) * 64 + lane) * 8);
        t[8] = ld_gran(p.ws, ((have >> 8) & 1) ? WS_DUMMY : WS_ML + (slot * 2 + (lane & 15)) * 8);
#pragma unroll
        for (int s = 0; s < 9; ++s) {
          const bool fresh = !((have >> s) & 1);
          if (s < 8) o[s] = fresh ? t[s] : o[s]; else ml = fresh ? t[8] : ml;
          if (__all((u32)(t[s] >> 32) == tag32)) have |= 1u << s;
        }
        if (have == 0x1ffu) break;
        __builtin_amdgcn_s_sleep(POLLSLP);
      }
    }
    const u32 mlv = (u32)ml;
#pragma unroll
    for (int bb = 0; bb < 2; ++bb) {
      float ms[4], ls[4];
#pragma unroll
      for (int s = 0; s < 4; ++s) { ms[s] = __uint_as_float(__builtin_amdgcn_readlane(mlv, 8 * bb + 2 * s)); ls[s] = __uint_as_float(__builtin_amdgcn_readlane(mlv, 8 * bb + 2 * s + 1)); }
      const float M = fmaxf(fmaxf(ms[0], ms[1]), fmaxf(ms[2], ms[3]));
      float W = 0.f, o0 = 0.f, o1 = 0.f;
#pragma unroll
      for (int s = 0; s < 4; ++s) { const float wt = ls[s] * __builtin_amdgcn_exp2f(ms[s] - M); W += wt; o0 = fmaf(wt, bflo((u32)o[4 * bb + s]), o0); o1 = fmaf(wt, bfhi((u32)o[4 * bb + s]), o1); }
      const float inv = __builtin_amdgcn_rcpf(W);
      *(LAS u32*)(L + L_O + (2 * w + bb) * 256 + lane * 4) = pk2(o0 * inv, o1 * inv);
    }
    LGKM0();
    if (lane == 0) lds_add(L + L_FLAG + F_SYNC3, 1u);
    NOUNROLL
    for (u32 n = 0; n < SPIN_LDS; ++n) { if (lds_ld(L + L_FLAG + F_SYNC3) >= 4) break; }
    asm volatile("" ::: "memory");
    f32x4 a = {0.f, 0.f, 0.f, 0.f}, a2 = {0.f, 0.f, 0.f, 0.f};
    a = mfma32(*(LAS u32x4*)(L + L_O + (c & 7) * 256 + 16 * g), wof[0], a);
    a2 = mfma32(*(LAS u32x4*)(L + L_O + (c & 7) * 256 + 64 + 16 * g), wof[1], a2);
    a = mfma32(*(LAS u32x4*)(L + L_O + (c & 7) * 256 + 128 + 16 * g), wof[2], a);
    a2 = mfma32(*(LAS u32x4*)(L + L_O + (c & 7) * 256 + 192 + 16 * g), wof[3], a2);
    a += a2;
    if (g < 2) {
#pragma unroll
      for (int i = 0; i < 4; ++i) {
        const u32 u = __float_as_uint(a[i]);
        st_gran(p.ws, WS_OUT + ((((4 * g + i) * 32 + 4 * wcc + w) * 48) + whead) * 128 + c * 8, t40lo | ((u + 0x7fu + ((u >> 8) & 1u)) >> 8), t40hi);
      }
    }
    if (lane == 0) lds_add(L + L_FLAG + F_OUTP, 1u);
  }
  if (wave == 0) {
    const float xres = bflo(p.x[(r >> 2) * DM + 16 * (4 * xg + (r & 3)) + c]);
    const u32 base = WS_OUT + (((r >> 2) * 32 + 4 * xg + (r & 3)) * 48) * 128 + c * 8;
    u64 t[12] = {0, 0, 0, 0, 0, 0, 0, 0, 0, 0, 0, 0};
    lds_wait_ge(L + L_FLAG + F_OUTP, 4);
    __builtin_amdgcn_s_sleep(H2DLY);
    {
      u32 have = 0;
      NOUNROLL
      for (u32 n = 0; n < SPIN_GLB; ++n) {
        u64 tt[12];
#pragma unroll
        for (int i = 0; i < 12; ++i) { const int pr = (4 * i + g < 46) ? 4 * i + g : 45; tt[i] = ld_gran(p.ws, ((have >> i) & 1) ? WS_DUMMY : base + pr * 128); }
#pragma unroll
        for (int i = 0; i < 12; ++i) {
          t[i] = ((have >> i) & 1) ? t[i] : tt[i];
          if (__all((((u32)tt[i] ^ t40lo) >> 24) == 0 && (u32)(tt[i] >> 32) == t40hi)) have |= 1u << i;
        }
        if (have == 0xfffu) break;
        __builtin_amdgcn_s_sleep(POLLSLP);
      }
    }
    float sa = 0.f, sf = 0.f;
#pragma unroll
    for (int i = 0; i < 8; ++i) sa += __uint_as_float((u32)t[i] << 8);
#pragma unroll
    for (int i = 8; i < 12; ++i) sf += (4 * i + g < 46) ? __uint_as_float((u32)t[i] << 8) : 0.f;
    sa = xrow_sum(sa); sf = xrow_sum(sf);
    const float res = rbf(xres + rbf(sa)) + rbf(sf);
    if (g == 0) p.out[(r >> 2) * DM + 16 * (4 * xg + (r & 3)) + c] = (u16)(pk2(res, 0.f) & 0xffffu);
    if (blk == 0 && lane == 0) __hip_atomic_store((GAS u64*)(p.ws + WS_EPOCH), ep, __ATOMIC_RELAXED, __HIP_MEMORY_SCOPE_AGENT);
  }
}

extern "C" void kernel_launch(void* const* d_in, const int* in_sizes, int n_in, void* d_out, int out_size, void* d_ws, size_t ws_size, hipStream_t stream) {
  Params p;
  p.x = (const u16*)d_in[0]; p.k = (const u16*)d_in[1]; p.v = (const u16*)d_in[2]; p.wq = (const u16*)d_in[3]; p.wo = (const u16*)d_in[4];
  p.wg = (const u16*)d_in[6]; p.wu = (const u16*)d_in[7]; p.wd = (const u16*)d_in[8];
  p.out = (u16*)d_out; p.ws = (unsigned char*)d_ws;
  hipLaunchKernelGGL(fused_decode_kernel, dim3(NWG), dim3(NTHR), 0, stream, p);
}
```

```cpp
#define ASHIFT 0
#include <hip/hip_runtime.h>
#include <stdint.h>

#define GAS __attribute__((address_space(1)))
#define LAS __attribute__((address_space(3)))
typedef unsigned u32;
typedef unsigned long long u64;
typedef unsigned short u16;
typedef u32 u32x4 __attribute__((ext_vector_type(4)));
typedef u32 u32x2 __attribute__((ext_vector_type(2)));
typedef float f32x4 __attribute__((ext_vector_type(4)));
typedef float f32x2 __attribute__((ext_vector_type(2)));
typedef short s16x4 __attribute__((ext_vector_type(4)));
typedef __bf16 bf16x8 __attribute__((ext_vector_type(8)));
typedef __bf16 bf16x2 __attribute__((ext_vector_type(2)));

constexpr int NCODE = 21;
constexpr int PACE10 = 71;
constexpr int PACE_T0 = 250;
constexpr int PACE_FROM = 8;
constexpr int NROUND = 4, TA0 = 900, TAP = 450, TAW = 200, ALATE = 55;
constexpr int DAHEAD = 3;
constexpr int DPRO = 1;
constexpr int TQ0 = 380, TQR = 48;
constexpr int TGS = 1000;
constexpr int TG15 = 950;
constexpr int TG2 = 1350;
constexpr int POLLSLP = 4, H1DLY = 21, H2DLY = 13;

constexpr int DM = 512, SK = 8192, DH = 128, FF = 1792;
constexpr int NWG = 256, NTHR = 512;
constexpr int RING = 8;
constexpr int NUNIT = 64;
constexpr float SC = 0.08838834764831845f * 1.4426950408889634f;
constexpr float THR_RAW = 8.0f / SC;

constexpr u32 WS_EPOCH = 0;
constexpr u32 WS_DUMMY = 2048;
constexpr u32 WS_START = 4096;
constexpr u32 WS_T0 = 8192;
constexpr u32 WS_PROG = 12288;
constexpr u32 WS_Q = 65536;
constexpr u32 WS_GU = WS_Q + 8 * 8 * 4 * 512 * 8;
constexpr u32 WS_H = WS_GU + 28 * 2 * 4 * 8 * 64 * 8;
constexpr u32 WS_OP = WS_H + 8 * 1792 * 8;
constexpr u32 WS_ML = WS_OP + 32 * 8 * 4 * 64 * 8;
constexpr u32 WS_OUT = WS_ML + 32 * 8 * 4 * 2 * 8;
constexpr u32 WS_END = WS_OUT + 8 * 32 * 48 * 16 * 8;
static_assert(WS_END <= (8u << 20), "ws map");

constexpr u32 L_RING = 0;
constexpr u32 L_STAGE = 4 * RING * 4096;
constexpr u32 L_XN = L_STAGE + 16384;
constexpr u32 XN_STR = 1056;
constexpr u32 L_Q = L_XN + 8 * XN_STR;
constexpr u32 L_O = L_Q + 1024;
constexpr u32 L_FLAG = L_O + 2048;
constexpr u32 L_SCR = L_FLAG + 256;
constexpr u32 L_END = L_SCR + 1024;
static_assert(L_END <= 163840, "lds");
constexpr u32 F_SYNC = 0, F_Q = 4, F_SYNC2 = 8, F_WO = 12, F_EPOCH = 16, F_SYNC3 = 24, F_ANCH = 28, F_OUTP = 32, F_PROG = 36;

constexpr u32 PER256 = (u32)(4096.0 * 1024.0 / (PACE10 * 0.1e12) * 1e8 * 256.0);
constexpr u32 SPIN_LDS = 1u << 22, SPIN_GLB = 1u << 17;

struct Params {
  const u16* x; const u16* k; const u16* v; const u16* wq; const u16* wo; const u16* wg; const u16* wu; const u16* wd;
  u16* out; unsigned char* ws;
};

__device__ __forceinline__ u32 rt32() { return (u32)__builtin_amdgcn_s_memrealtime(); }
__device__ __forceinline__ float bflo(u32 w) { return __uint_as_float(w << 16); }
__device__ __forceinline__ float bfhi(u32 w) { return __uint_as_float(w & 0xffff0000u); }
__device__ __forceinline__ u32 pk2(float lo, float hi) { f32x2 t = {lo, hi}; return __builtin_bit_cast(u32, __builtin_convertvector(t, bf16x2)); }
__device__ __forceinline__ float rbf(float f) { return bflo(pk2(f, f)); }
template <int CTRL> __device__ __forceinline__ float dpp(float x) {
  return __builtin_bit_cast(float, __builtin_amdgcn_mov_dpp(__builtin_bit_cast(int, x), CTRL, 0xf, 0xf, true));
}
__device__ __forceinline__ float xrow_max(float x) {
  auto s = __builtin_amdgcn_permlane16_swap(__float_as_uint(x), __float_as_uint(x), false, false);
  x = fmaxf(__uint_as_float(s[0]), __uint_as_float(s[1]));
  auto t = __builtin_amdgcn_permlane32_swap(__float_as_uint(x), __float_as_uint(x), false, false);
  return fmaxf(__uint_as_float(t[0]), __uint_as_float(t[1]));
}
__device__ __forceinline__ float xrow_sum(float x) {
  auto s = __builtin_amdgcn_permlane16_swap(__float_as_uint(x), __float_as_uint(x), false, false);
  x = __uint_as_float(s[0]) + __uint_as_float(s[1]);
  auto t = __builtin_amdgcn_permlane32_swap(__float_as_uint(x), __float_as_uint(x), false, false);
  return __uint_as_float(t[0]) + __uint_as_float(t[1]);
}
__device__ __forceinline__ float wave_sum(float x) {
  x += dpp<0xB1>(x); x += dpp<0x4E>(x); x += dpp<0x141>(x); x += dpp<0x128>(x);
  return xrow_sum(x);
}
__device__ __forceinline__ f32x4 mfma32(u32x4 a, u32x4 b, f32x4 c) {
  return __builtin_amdgcn_mfma_f32_16x16x32_bf16(__builtin_bit_cast(bf16x8, a), __builtin_bit_cast(bf16x8, b), c, 0, 0, 0);
}
__device__ __forceinline__ f32x4 mfma16(u32x2 a, u32x2 b, f32x4 c) {
  return __builtin_amdgcn_mfma_f32_16x16x16bf16_1k(__builtin_bit_cast(s16x4, a), __builtin_bit_cast(s16x4, b), c, 0, 0, 0);
}
__device__ __forceinline__ u32x2 trread(LAS unsigned char* p) {
  return __builtin_bit_cast(u32x2, __builtin_amdgcn_ds_read_tr16_b64_v4i16((LAS s16x4*)p));
}
__device__ __forceinline__ u32x4 trread2(LAS unsigned char* p0, LAS unsigned char* p1) {
  const u32x2 a = trread(p0), b = trread(p1); return u32x4{a.x, a.y, b.x, b.y};
}
__device__ __forceinline__ void dma4(u32 ldst, u32 v0, u32 v1, u32 v2, u32 v3, __amdgpu_buffer_rsrc_t srd, u32 soff) {
  u32 keep;
  asm volatile("s_waitcnt lgkmcnt(0)\n\ts_mov_b32 %0, m0\n\ts_mov_b32 m0, %1\n\ts_nop 0\n\t"
               "buffer_load_dwordx4 %2, %6, %7 offen nt lds\n\t"
               "buffer_load_dwordx4 %3, %6, %7 offen offset:1024 nt lds\n\t"
               "buffer_load_dwordx4 %4, %6, %7 offen offset:2048 nt lds\n\t"
               "buffer_load_dwordx4 %5, %6, %7 offen offset:3072 nt lds\n\t"
               "s_mov_b32 m0, %0"
               : "=&s"(keep) : "s"(ldst), "v"(v0), "v"(v1), "v"(v2), "v"(v3), "s"(srd), "s"(soff) : "memory");
}
__device__ __forceinline__ void dma1(u32 ldst, const void* gp) {
  u32 keep;
  asm volatile("s_mov_b32 %0, m0\n\ts_mov_b32 m0, %1\n\ts_nop 0\n\tglobal_load_lds_dwordx4 %2, off nt\n\ts_mov_b32 m0, %0"
               : "=&s"(keep) : "s"(ldst), "v"(gp) : "memory");
}
__device__ __forceinline__ void dma1p(u32 ldst, const void* gp) {
  u32 keep;
  asm volatile("s_mov_b32 %0, m0\n\ts_mov_b32 m0, %1\n\ts_nop 0\n\tglobal_load_lds_dwordx4 %2, off\n\ts_mov_b32 m0, %0"
               : "=&s"(keep) : "s"(ldst), "v"(gp) : "memory");
}
#define VMCNT(N) asm volatile("s_waitcnt vmcnt(" #N ")" ::: "memory")
#define LGKM0() asm volatile("s_waitcnt lgkmcnt(0)" ::: "memory")
#define NOUNROLL _Pragma("nounroll")
__device__ __forceinline__ void wait_units(int n) {
  switch (n) {
    case 0: VMCNT(0); break;   case 1: VMCNT(4); break;   case 2: VMCNT(8); break;   case 3: VMCNT(12); break;
    case 4: VMCNT(16); break;  case 5: VMCNT(20); break;  case 6: VMCNT(24); break;  default: VMCNT(28); break;
  }
}
__device__ __forceinline__ void bar() { LGKM0(); __builtin_amdgcn_s_barrier(); asm volatile("" ::: "memory"); }
__device__ __forceinline__ u32 lds_ld(LAS unsigned char* p) { return *(volatile LAS u32*)p; }
__device__ __forceinline__ void lds_add(LAS unsigned char* p, u32 v) { __hip_atomic_fetch_add((LAS u32*)p, v, __ATOMIC_RELAXED, __HIP_MEMORY_SCOPE_WORKGROUP); }
__device__ __forceinline__ void lds_wait_ge(LAS unsigned char* p, u32 want) {
  NOUNROLL
  for (u32 n = 0; n < SPIN_LDS; ++n) { if (lds_ld(p) >= want) break; __builtin_amdgcn_s_sleep(1); }
  asm volatile("" ::: "memory");
}
__device__ __forceinline__ void wait_until(u32 due) { while ((int)(rt32() - due) < 0) __builtin_amdgcn_s_sleep(4); }
__device__ __forceinline__ void st_gran(unsigned char* ws, u32 off, u32 lo, u32 hi) {
  __hip_atomic_store((GAS u64*)(ws + off), ((u64)hi << 32) | lo, __ATOMIC_RELAXED, __HIP_MEMORY_SCOPE_AGENT);
}
__device__ __forceinline__ u64 ld_gran(unsigned char* ws, u32 off) {
  return __hip_atomic_load((GAS u64*)(ws + off), __ATOMIC_RELAXED, __HIP_MEMORY_SCOPE_AGENT);
}

__global__ void __launch_bounds__(NTHR, 2) fused_decode_kernel(Params p) {
  __shared__ __attribute__((aligned(16))) unsigned char lds_raw[L_END];
  LAS unsigned char* L = (LAS unsigned char*)lds_raw;
  const u32 lbase = (u32)(size_t)L;
  const int tid = threadIdx.x, lane = tid & 63, wave = __builtin_amdgcn_readfirstlane(tid >> 6);
  const int g = lane >> 4, c = lane & 15;
  const int blk = blockIdx.x, xg = blk & 7, r = blk >> 3;
  const u32 t0 = rt32();
  u64 pc0; asm volatile("s_getpc_b64 %0" : "=s"(pc0));
  const __amdgpu_buffer_rsrc_t wsr = __builtin_amdgcn_make_buffer_rsrc(p.ws, 0, WS_END, 0x00020000);
  const int as = r & 3, ab = r >> 2, akv = xg, apair = ab * 8 + akv;
  const int whead = 4 * xg + (r & 3), wcc = r >> 2;

  if (wave < 4) {
    const size_t pbase = (size_t)apair * SK * DH;
    const __amdgpu_buffer_rsrc_t ksr = __builtin_amdgcn_make_buffer_rsrc((void*)(p.k + pbase), 0, SK * DH * 2, 0x00020000);
    const __amdgpu_buffer_rsrc_t vsr = __builtin_amdgcn_make_buffer_rsrc((void*)(p.v + pbase), 0, SK * DH * 2, 0x00020000);
    const u32 vk0 = g * 256 + 16 * (c ^ g), vk1 = g * 256 + 16 * (c ^ (4 + g)), vk2 = g * 256 + 16 * (c ^ (8 + g)), vk3 = g * 256 + 16 * (c ^ (12 + g));
    const u32 vv0 = g * 256 + 16 * (c ^ (g << 1)), vv1 = g * 256 + 16 * (c ^ ((4 + g) << 1));
    const u32 ring = lbase + L_RING + wave * (RING * 4096);
    int nis = 0;
    int landed = 0;
    u32 tnow = t0, pA = t0 + PACE_T0;
    const u32 sbase = as * 16384u + wave * 4096u;
    auto issue = [&]() {
      if (nis >= PACE_FROM) {
        if (nis < PACE_FROM + 6) { const u32 an = __builtin_amdgcn_readfirstlane(lds_ld(L + L_FLAG + F_ANCH)); if (an) pA = an + PACE_T0; }
        const u32 due = pA + (((u32)nis * PER256) >> 8);
        if ((int)(tnow - due) < 0) {
          NOUNROLL
          for (u32 n = 0; n < SPIN_LDS; ++n) { tnow = rt32(); if ((int)(tnow - due) >= 0) break; __builtin_amdgcn_s_sleep(1); }
        }
      }
      const u32 soff = (u32)(nis >> 1) * 65536u + sbase, ldst = ring + (u32)(nis & (RING - 1)) * 4096u;
      if (nis & 1) dma4(ldst, vv0, vv1, vv0, vv1, vsr, soff); else dma4(ldst, vk0, vk1, vk2, vk3, ksr, soff);
      ++nis;
    };
    issue();
    bar();
    NOUNROLL
    while (nis < RING) {
      if (nis >= DPRO) { wait_units(DPRO - 1); landed = nis - (DPRO - 1); }
      if (lds_ld(L + L_FLAG + F_Q) >= 4) break;
      issue();
    }
    auto refill = [&](int cons) { while (nis - landed < DAHEAD && nis < cons + RING && nis < NUNIT) issue(); };
    lds_wait_ge(L + L_FLAG + F_Q, 4);
    u32x4 qf[4];
#pragma unroll
    for (int s = 0; s < 4; ++s) qf[s] = *(LAS u32x4*)(L + L_Q + (c & 3) * 256 + 64 * s + 16 * g);
    refill(0);
    LAS unsigned char* R = L + L_RING + wave * (RING * 4096);
    const u32 ka = c * 256 + 16 * (g ^ c);
    const u32 va = (4 * g + (c >> 2)) * 256 + 32 * ((4 * g + (c >> 2)) & 7) + 8 * (c & 3);
    float m = -INFINITY, lsum = 0.f;
    f32x4 acc[8];
#pragma unroll
    for (int i = 0; i < 8; ++i) acc[i] = f32x4{0.f, 0.f, 0.f, 0.f};
    NOUNROLL
    for (int j = 0; j < NUNIT / 2; ++j) {
      wait_units(nis - 2 * j - 1);
      landed = landed > 2 * j + 1 ? landed : 2 * j + 1;
      refill(2 * j);
      if (ASHIFT) {
        const u32 an = __builtin_amdgcn_readfirstlane(lds_ld(L + L_FLAG + F_ANCH));
        if (an) pA = an + PACE_T0;
        if (wave == 0) { tnow = rt32(); *(volatile LAS u32*)(L + L_FLAG + F_PROG) = (tnow << 8) | (u32)(2 * j + 1); }
      }
      LAS unsigned char* Kt = R + ((2 * j) & (RING - 1)) * 4096;
      LAS unsigned char* Vt = R + ((2 * j + 1) & (RING - 1)) * 4096;
      f32x4 sa = {0.f, 0.f, 0.f, 0.f}, sb = {0.f, 0.f, 0.f, 0.f};
      {
        const u32x4 k0 = *(LAS u32x4*)(Kt + ka), k1 = *(LAS u32x4*)(Kt + (ka ^ 64)), k2 = *(LAS u32x4*)(Kt + (ka ^ 128)), k3 = *(LAS u32x4*)(Kt + (ka ^ 192));
        sa = mfma32(k0, qf[0], sa); sb = mfma32(k1, qf[1], sb); sa = mfma32(k2, qf[2], sa); sb = mfma32(k3, qf[3], sb);
      }
      const f32x4 sc = sa + sb;
      const float tmax = fmaxf(fmaxf(sc[0], sc[1]), fmaxf(sc[2], sc[3]));
      if (__builtin_amdgcn_ballot_w64(tmax > m + THR_RAW) != 0ull) {
        const float mn = fmaxf(m, xrow_max(tmax));
        const float al = __builtin_amdgcn_exp2f((m - mn) * SC);
        m = mn; lsum *= al;
        const float a0 = dpp<0x00>(al), a1 = dpp<0x55>(al), a2 = dpp<0xAA>(al), a3 = dpp<0xFF>(al);
#pragma unroll
        for (int i = 0; i < 8; ++i) { acc[i][0] *= a0; acc[i][1] *= a1; acc[i][2] *= a2; acc[i][3] *= a3; }
      }
      const float mc = m * SC;
      const float p0 = __builtin_amdgcn_exp2f(fmaf(sc[0], SC, -mc)), p1 = __builtin_amdgcn_exp2f(fmaf(sc[1], SC, -mc));
      const float p2 = __builtin_amdgcn_exp2f(fmaf(sc[2], SC, -mc)), p3 = __builtin_amdgcn_exp2f(fmaf(sc[3], SC, -mc));
      lsum += (p0 + p1) + (p2 + p3);
      const u32x2 pa = {pk2(p0, p1), pk2(p2, p3)};
      wait_units(nis - 2 * j - 2);
      landed = landed > 2 * j + 2 ? landed : 2 * j + 2;
      refill(2 * j + 1);
#pragma unroll
      for (int i = 0; i < 8; ++i) acc[i] = mfma16(pa, trread(Vt + (va ^ (32 * i))), acc[i]);
      refill(2 * j + 2);
    }

    lsum = xrow_sum(lsum);
    if (g == 0) {
#pragma unroll
      for (int i = 0; i < 8; ++i) *(LAS f32x4*)(R + i * 256 + c * 16) = acc[i];
      if (c < 4) *(LAS f32x2*)(R + 2048 + c * 8) = f32x2{m * SC, lsum};
    }
  } else {
    const int hw = wave - 4;
    __builtin_amdgcn_s_setprio(3);
    u32x4 xa, xb; u32x2 ev;
    if (hw == 0 && lane < 16) *(LAS u32x4*)(L + L_FLAG + lane * 16) = u32x4{0, 0, 0, 0};
    const int kq = r & 3;
    if (NCODE && r * 4 + hw < NCODE) {
      dma1p(lbase + L_SCR, (const char*)(pc0 & ~255ull) + (r * 4 + hw) * 1024 + lane * 16);
    }
    {
      const u16* xp = p.x + (2 * hw) * DM + lane * 8;
      const u16* wp = p.wq + (size_t)(kq * 128 + hw * 32 + (lane >> 3)) * 4096 + xg * 512 + (r >> 2) * 64 + (lane & 7) * 8;
      const u32 ldst = lbase + L_STAGE + hw * 4096;
      u32 keep;
      asm volatile("global_load_dwordx4 %0, %4, off\n\tglobal_load_dwordx4 %1, %4, off offset:1024\n\tglobal_load_dwordx2 %2, %5, off\n\t"
                   "s_mov_b32 %3, m0\n\ts_mov_b32 m0, %10\n\ts_nop 0\n\t"
                   "global_load_lds_dwordx4 %6, off nt\n\ts_add_u32 m0, m0, 0x400\n\ts_nop 0\n\t"
                   "global_load_lds_dwordx4 %7, off nt\n\ts_add_u32 m0, m0, 0x400\n\ts_nop 0\n\t"
                   "global_load_lds_dwordx4 %8, off nt\n\ts_add_u32 m0, m0, 0x400\n\ts_nop 0\n\t"
                   "global_load_lds_dwordx4 %9, off nt\n\ts_mov_b32 m0, %3\n\t"
                   "s_waitcnt lgkmcnt(0)\n\ts_barrier\n\ts_waitcnt vmcnt(4)"
                   : "=&v"(xa), "=&v"(xb), "=&v"(ev), "=&s"(keep)
                   : "v"(xp), "v"(p.ws + WS_EPOCH), "v"(wp), "v"(wp + (size_t)8 * 4096), "v"(wp + (size_t)16 * 4096), "v"(wp + (size_t)24 * 4096), "s"(ldst)
                   : "memory", "scc");
    }
    const u64 ep = (((u64)ev.y << 32) | ev.x) + 1ull;
    const u32 tag32 = (u32)ep, t48lo = ((u32)ep) << 16, t48hi = (u32)(ep >> 16), t40lo = ((u32)ep) << 24, t40hi = (u32)(ep >> 8);
#pragma unroll
    for (int q = 0; q < 2; ++q) {
      const u32x4 xv = q ? xb : xa;
      float v[8];
#pragma unroll
      for (int i = 0; i < 4; ++i) { v[2 * i] = bflo(xv[i]); v[2 * i + 1] = bfhi(xv[i]); }
      const float mean = wave_sum(((v[0] + v[1]) + (v[2] + v[3])) + ((v[4] + v[5]) + (v[6] + v[7]))) * (1.f / DM);
      float q2 = 0.f;
#pragma unroll
      for (int i = 0; i < 8; ++i) { const float d = v[i] - mean; q2 = fmaf(d, d, q2); }
      const float var = rbf(wave_sum(q2) * (1.f / DM));
      const float rden = __builtin_amdgcn_rcpf(rbf(__builtin_amdgcn_sqrtf(rbf(var + 1e-5f))));
      const float mb = rbf(mean);
      u32x4 o;
#pragma unroll
      for (int i = 0; i < 4; ++i) { const u32 d = pk2(v[2 * i] - mb, v[2 * i + 1] - mb); o[i] = pk2(bflo(d) * rden, bfhi(d) * rden); }
      *(LAS u32x4*)(L + L_XN + (2 * hw + q) * XN_STR + lane * 16) = o;
    }
    const int tq = c >> 2, tp = c & 3;
    auto tile_mma = [&](const u32x4 (&act)[4], int cg) {
      f32x4 a0 = {0.f, 0.f, 0.f, 0.f}, a1 = {0.f, 0.f, 0.f, 0.f};
      LAS unsigned char* ap = L + L_STAGE + (8 * g + tq) * 128 + 32 * cg + 8 * tp;
      a0 = mfma32(act[0], trread2(ap, ap + 512), a0);
      a1 = mfma32(act[1], trread2(ap + 4096, ap + 4096 + 512), a1);
      a0 = mfma32(act[2], trread2(ap + 8192, ap + 8192 + 512), a0);
      a1 = mfma32(act[3], trread2(ap + 12288, ap + 12288 + 512), a1);
      return a0 + a1;
    };
    auto xn_frags = [&](u32x4 (&act)[4], int k0) {
#pragma unroll
      for (int s = 0; s < 4; ++s) act[s] = *(LAS u32x4*)(L + L_XN + (c & 7) * XN_STR + 2 * k0 + 64 * s + 16 * g);
    };
    auto f24 = [&](float f) { const u32 u = __float_as_uint(f); return t40lo | ((u + 0x7fu + ((u >> 8) & 1u)) >> 8); };
    auto ok40 = [&](const u32x4& t) { return ((t[0] ^ t40lo) >> 24) == 0 && t[1] == t40hi && ((t[2] ^ t40lo) >> 24) == 0 && t[3] == t40hi; };
    auto ok48 = [&](const u32x4& t) { return ((t[0] ^ t48lo) >> 16) == 0 && t[1] == t48hi && ((t[2] ^ t48lo) >> 16) == 0 && t[3] == t48hi; };
    u32x4 act[4];
    asm volatile("s_waitcnt vmcnt(0) lgkmcnt(0)" ::: "memory");
    if (lane == 0) lds_add(L + L_FLAG + F_SYNC, 1u);
    lds_wait_ge(L + L_FLAG + F_SYNC, 4);
    xn_frags(act, kq * 128);
    {
      const f32x4 d = tile_mma(act, hw);
      if (g < 2) {
#pragma unroll
        for (int i = 0; i < 4; ++i) st_gran(p.ws, WS_Q + ((((xg * 8 + 4 * g + i) * 4 + kq) * 512) + (r >> 2) * 64 + 16 * hw + c) * 8, f24(d[i]), t40hi);
      }
    }
    if (hw == 1 && lane == 0) st_gran(p.ws, WS_T0 + blk * 8, t40lo | (t0 & 0xffffffu), t40hi);
    LGKM0();
    if (lane == 0) lds_add(L + L_FLAG + F_SYNC, 1u);
    if (hw < 2) {
      const u32 qb = WS_Q + ((akv * 8 + ab) * 4) * 512 * 8;
      wait_until(t0 + TQ0 - (u32)(r * TQR) / 16);
      float q0[2], q1[2];
      NOUNROLL
      for (u32 n = 0; n < SPIN_GLB; ++n) {
        bool ok = true;
#pragma unroll
        for (int h = 0; h < 2; ++h) {
          q0[h] = 0.f; q1[h] = 0.f;
#pragma unroll
          for (int i = 0; i < 4; ++i) {
            const u32x4 t = __builtin_amdgcn_raw_buffer_load_b128(wsr, qb + (i * 512 + (2 * hw + h) * 128 + 2 * lane) * 8, 0, 16);
            ok &= ok40(t); q0[h] += __uint_as_float(t[0] << 8); q1[h] += __uint_as_float(t[2] << 8);
          }
        }
        if (__all(ok)) break;
        __builtin_amdgcn_s_sleep(POLLSLP);
        asm volatile("" ::: "memory");
      }
      *(LAS u32*)(L + L_Q + (2 * hw) * 256 + lane * 4) = pk2(q0[0], q1[0]);
      *(LAS u32*)(L + L_Q + (2 * hw + 1) * 256 + lane * 4) = pk2(q0[1], q1[1]);
      LGKM0();
      if (lane == 0) lds_add(L + L_FLAG + F_Q, 2u);
      if (hw == 1) {
        const u32 ld_ = lbase + L_SCR;
        u32 keep;
        const u16* tp = (lane < 32 ? p.k : p.v) + (size_t)apair * SK * DH + (size_t)(lane & 31) * 32768 + as * 8192 + 4096;
        if ((lane & 31) >= 4)
          asm volatile("s_mov_b32 %0, m0\n\ts_mov_b32 m0, %1\n\ts_nop 0\n\tglobal_load_lds_dword %2, off nt\n\ts_mov_b32 m0, %0" : "=&s"(keep) : "s"(ld_), "v"(tp) : "memory");
        const unsigned char* tw = lane < 8 ? p.ws + WS_OUT + ((lane * 32 + 4 * wcc) * 48 + 47) * 128
                                : lane == 8 ? p.ws + WS_OUT + (((r >> 2) * 32 + 4 * xg + (r & 3)) * 48 + 47) * 128
                                : (const unsigned char*)(p.out + (r >> 2) * DM + 16 * (4 * xg + (r & 3)));
        if (lane < 10)
          asm volatile("s_mov_b32 %0, m0\n\ts_mov_b32 m0, %1\n\ts_nop 0\n\tglobal_load_lds_dword %2, off\n\ts_mov_b32 m0, %0" : "=&s"(keep) : "s"(ld_), "v"(tw) : "memory");
      }
      if (hw == 0) {
        u64 ts = 0;
        bool ok = false;
        NOUNROLL
        for (u32 n = 0; n < 64 && !ok; ++n) {
          ts = ld_gran(p.ws, WS_T0 + (xg + 8 * (lane & 31)) * 8);
          ok = __all((((u32)ts ^ t40lo) >> 24) == 0 && (u32)(ts >> 32) == t40hi);
          if (!ok) __builtin_amdgcn_s_sleep(8);
        }
        int d = ((int)(((u32)ts - t0) << 8)) >> 8;
        d = min(d, __builtin_amdgcn_mov_dpp(d, 0xB1, 0xf, 0xf, true)); d = min(d, __builtin_amdgcn_mov_dpp(d, 0x4E, 0xf, 0xf, true));
        d = min(d, __builtin_amdgcn_mov_dpp(d, 0x141, 0xf, 0xf, true)); d = min(d, __builtin_amdgcn_mov_dpp(d, 0x128, 0xf, 0xf, true));
        int dm = min(min(__builtin_amdgcn_readlane(d, 0), __builtin_amdgcn_readlane(d, 16)), 0);
        dm = dm < -400 ? -400 : dm;
        if (ok && lane == 0) *(volatile LAS u32*)(L + L_FLAG + F_ANCH) = (t0 + dm) | 1u;
      }
      if (hw == 1) {
        if (ASHIFT) {
          float snp = 0.f, stp = 0.f;
          const float lP = (float)PER256 * (1.f / 256.f);
          NOUNROLL
          for (int round = 0; round < NROUND; ++round) {
            wait_until(t0 + TA0 + round * TAP);
            const u32 pv = lds_ld(L + L_FLAG + F_PROG);
            if (lane == 0) st_gran(p.ws, WS_PROG + (round * 256 + blk) * 8, t40lo | ((pv >> 8 & 0x1ffffu) << 7) | (pv & 127u), t40hi);
            wait_until(t0 + TA0 + TAW + round * TAP);
            u32x4 a, b;
            bool okk = false;
            NOUNROLL
            for (int n = 0; n < 3; ++n) {
              a = __builtin_amdgcn_raw_buffer_load_b128(wsr, WS_PROG + round * 2048 + lane * 32, 0, 16);
              b = __builtin_amdgcn_raw_buffer_load_b128(wsr, WS_PROG + round * 2048 + lane * 32 + 16, 0, 16);
              okk = __all(ok40(a) && ok40(b));
              if (okk) break;
              __builtin_amdgcn_s_sleep(32);
              asm volatile("" ::: "memory");
            }
            if (!okk) continue;
            const u32 now = rt32();
            const u32 w4[4] = {a[0], a[2], b[0], b[2]};
            float sn = 0.f, st = 0.f;
#pragma unroll
            for (int i = 0; i < 4; ++i) { sn += (float)(w4[i] & 127u); st += (float)(((int)(((w4[i] >> 7) - now) << 15)) >> 15); }
            sn = wave_sum(sn) * (1.f / 256.f); st = wave_sum(st) * (1.f / 256.f);
            const u32 an0 = lds_ld(L + L_FLAG + F_ANCH), an = an0 ? an0 : t0;
            if (snp != 0.f && sn - snp > 3.f) {
              const float stA = st + (float)(int)(now - t0), P = (stA - stp) * __builtin_amdgcn_rcpf(sn - snp);
              const float late = st + (float)(int)(now - an) - ((float)PACE_T0 + (sn + (float)(DAHEAD - 1)) * lP);
              if (late > (float)ALATE && late > 0.4f * (lP - P) * ((float)NUNIT - sn) && lane == 0)
                *(volatile LAS u32*)(L + L_FLAG + F_ANCH) = (an + (u32)(int)late) | 1u;
            }
            snp = sn; stp = st + (float)(int)(now - t0);
          }
        }
        wait_until(t0 + TGS);
        NOUNROLL
        for (u32 n = 0; n < SPIN_GLB; ++n) {
          const u32x4 a = __builtin_amdgcn_raw_buffer_load_b128(wsr, WS_START + lane * 16, 0, 16);
          const u32x4 b = __builtin_amdgcn_raw_buffer_load_b128(wsr, WS_START + 1024 + lane * 16, 0, 16);
          const u32 e0 = (u32)ep, e1 = (u32)(ep >> 32);
          const bool ok = a[0] == e0 && a[1] == e1 && a[2] == e0 && a[3] == e1 && b[0] == e0 && b[1] == e1 && b[2] == e0 && b[3] == e1;
          if (__all(ok)) break;
          __builtin_amdgcn_s_sleep(32);
          asm volatile("" ::: "memory");
        }
      }
    } else {
      const int h2 = hw - 2;
      u32 nsync2 = 0;
      auto arrive2 = [&]() {
        LGKM0();
        if (lane == 0) lds_add(L + L_FLAG + F_SYNC2, 1u);
        nsync2 += 2;
        lds_wait_ge(L + L_FLAG + F_SYNC2, nsync2);
      };
      auto stage2 = [&](const u16* w, int ld) {
        const u16* wp = w + (size_t)(h2 * 64 + (lane >> 3)) * ld + (lane & 7) * 8;
#pragma unroll
        for (int i = 0; i < 8; ++i) dma1(lbase + L_STAGE + h2 * 8192 + i * 1024, wp + (size_t)i * 8 * ld);
      };
      if (h2 == 1) {
        const u32x4 z = {0u, ~tag32, 0u, ~tag32};
#pragma unroll
        for (int i = 0; i < 2; ++i)
          __builtin_amdgcn_raw_buffer_store_b128(z, wsr, WS_OP + ((((akv * 4 + 2 * i + (lane >> 5)) * 8 + ab) * 4 + as) * 64) * 8 + (lane & 31) * 16, 0, 16);
        if (lane < 4) __builtin_amdgcn_raw_buffer_store_b128(z, wsr, WS_ML + (((akv * 4 + lane) * 8 + ab) * 4 + as) * 16, 0, 16);
      }
      lds_wait_ge(L + L_FLAG + F_SYNC, 8);
      const bool f1 = r < 14, f15 = r >= 14 && r < 18, f2 = r >= 18;
      const int t1 = xg * 14 + r, sl1 = t1 >> 2, fq = t1 & 3, ks = r - 18;
      const u16* wo_tile = p.wo + (size_t)whead * 128 * DM + wcc * 64;
      if (f1) stage2(p.wg + (size_t)fq * 128 * FF + sl1 * 64, FF);
      else if (f2) stage2(p.wd + (size_t)ks * 128 * DM + xg * 64, DM);
      else stage2(wo_tile, DM);
      VMCNT(0);
      if (h2 == 1 && lane == 0) {
        __hip_atomic_store((GAS u64*)(p.ws + WS_START + blk * 8), ep, __ATOMIC_RELAXED, __HIP_MEMORY_SCOPE_AGENT);
        *(volatile LAS u64*)(L + L_FLAG + F_EPOCH) = ep;
      }
      arrive2();
      if (f1) {
        xn_frags(act, fq * 128);
        f32x4 d[2];
        d[0] = tile_mma(act, 2 * h2); d[1] = tile_mma(act, 2 * h2 + 1);
        asm volatile("" : "+v"(d[0]), "+v"(d[1]));
        arrive2();
        stage2(p.wu + (size_t)fq * 128 * FF + sl1 * 64, FF);
#pragma unroll
        for (int w = 0; w < 2; ++w) if (g < 2) {
#pragma unroll
          for (int i = 0; i < 4; ++i) st_gran(p.ws, WS_GU + ((((sl1 * 2 + 0) * 4 + fq) * 8 + 4 * g + i) * 64 + 16 * (2 * h2 + w) + c) * 8, f24(d[w][i]), t40hi);
        }
        VMCNT(8);
        arrive2();
        d[0] = tile_mma(act, 2 * h2); d[1] = tile_mma(act, 2 * h2 + 1);
        asm volatile("" : "+v"(d[0]), "+v"(d[1]));
        arrive2();
        stage2(wo_tile, DM);
#pragma unroll
        for (int w = 0; w < 2; ++w) if (g < 2) {
#pragma unroll
          for (int i = 0; i < 4; ++i) st_gran(p.ws, WS_GU + ((((sl1 * 2 + 1) * 4 + fq) * 8 + 4 * g + i) * 64 + 16 * (2 * h2 + w) + c) * 8, f24(d[w][i]), t40hi);
        }
        VMCNT(8);
        arrive2();
      } else if (f15) {
        const int sl = xg * 4 + (r - 14);
        if (sl < 28) {
          wait_until(t0 + TG15);
          {
            const u32 so = WS_GU + ((((sl * 2 + 1) * 4 + (lane & 3)) * 8) * 64 + 63) * 8;
            NOUNROLL
            for (u32 n = 0; n < SPIN_GLB; ++n) {
              const u64 t = ld_gran(p.ws, so);
              if (__all((((u32)t ^ t40lo) >> 24) == 0 && (u32)(t >> 32) == t40hi)) break;
              __builtin_amdgcn_s_sleep(16);
            }
          }
          float gs[2][2], us[2][2];
          const int cp = lane & 31;
          NOUNROLL
          for (u32 n = 0; n < SPIN_GLB; ++n) {
            bool ok = true;
#pragma unroll
            for (int bb = 0; bb < 2; ++bb) {
              const int b = 4 * h2 + 2 * bb + (lane >> 5);
              gs[bb][0] = gs[bb][1] = us[bb][0] = us[bb][1] = 0.f;
#pragma unroll
              for (int i = 0; i < 4; ++i) {
                const u32x4 tg = __builtin_amdgcn_raw_buffer_load_b128(wsr, WS_GU + ((((sl * 2 + 0) * 4 + i) * 8 + b) * 64 + 2 * cp) * 8, 0, 16);
                const u32x4 tu = __builtin_amdgcn_raw_buffer_load_b128(wsr, WS_GU + ((((sl * 2 + 1) * 4 + i) * 8 + b) * 64 + 2 * cp) * 8, 0, 16);
                ok &= ok40(tg) && ok40(tu);
                gs[bb][0] += __uint_as_float(tg[0] << 8); gs[bb][1] += __uint_as_float(tg[2] << 8);
                us[bb][0] += __uint_as_float(tu[0] << 8); us[bb][1] += __uint_as_float(tu[2] << 8);
              }
            }
            if (__all(ok)) break;
            __builtin_amdgcn_s_sleep(16);
            asm volatile("" ::: "memory");
          }
#pragma unroll
          for (int bb = 0; bb < 2; ++bb) {
            const int b = 4 * h2 + 2 * bb + (lane >> 5);
            const u32 hp = pk2(rbf(gs[bb][0]) * rbf(us[bb][0]), rbf(gs[bb][1]) * rbf(us[bb][1]));
            st_gran(p.ws, WS_H + (b * FF + sl * 64 + 2 * cp) * 8, t48lo | (hp & 0xffffu), t48hi);
            st_gran(p.ws, WS_H + (b * FF + sl * 64 + 2 * cp + 1) * 8, t48lo | (hp >> 16), t48hi);
          }
        }
      } else {
        wait_until(t0 + TG2);
        {
          const u32 so = WS_H + (7 * FF + ks * 128 + 64 * (lane & 1) + 63) * 8;
          NOUNROLL
          for (u32 n = 0; n < SPIN_GLB; ++n) {
            const u64 t = ld_gran(p.ws, so);
            if (__all((((u32)t ^ t48lo) >> 16) == 0 && (u32)(t >> 32) == t48hi)) break;
            __builtin_amdgcn_s_sleep(32);
          }
        }
        const int b = lane >> 3, k0 = 64 * h2 + 8 * (lane & 7);
        u32x4 hv = {0, 0, 0, 0};
        NOUNROLL
        for (u32 n = 0; n < SPIN_GLB; ++n) {
          bool ok = true;
#pragma unroll
          for (int i = 0; i < 4; ++i) {
            const u32x4 t = __builtin_amdgcn_raw_buffer_load_b128(wsr, WS_H + (b * FF + ks * 128 + k0 + 2 * i) * 8, 0, 16);
            ok &= ok48(t);
            hv[i] = (t[0] & 0xffffu) | (t[2] << 16);
          }
          if (__all(ok)) break;
          __builtin_amdgcn_s_sleep(16);
          asm volatile("" ::: "memory");
        }
        *(LAS u32x4*)(L + L_O + b * 256 + k0 * 2) = hv;
        arrive2();
#pragma unroll
        for (int s = 0; s < 4; ++s) act[s] = *(LAS u32x4*)(L + L_O + (c & 7) * 256 + 64 * s + 16 * g);
        f32x4 d[2];
        d[0] = tile_mma(act, 2 * h2); d[1] = tile_mma(act, 2 * h2 + 1);
        asm volatile("" : "+v"(d[0]), "+v"(d[1]));
        arrive2();
        stage2(wo_tile, DM);
#pragma unroll
        for (int w = 0; w < 2; ++w) if (g < 2) {
#pragma unroll
          for (int i = 0; i < 4; ++i) st_gran(p.ws, WS_OUT + ((((4 * g + i) * 32 + 4 * xg + 2 * h2 + w) * 48) + 32 + ks) * 128 + c * 8, f24(d[w][i]), t40hi);
        }
        VMCNT(8);
        arrive2();
      }
      if (h2 == 0 && lane == 0) *(volatile LAS u32*)(L + L_FLAG + F_WO) = 1;
    }
    NOUNROLL
    for (u32 n = 0; n < SPIN_LDS; ++n) { if (lds_ld(L + L_FLAG + F_WO) >= 1) break; __builtin_amdgcn_s_sleep(16); }
    asm volatile("" ::: "memory");
  }

  u32x4 wof[4];
  if (wave >= 4) {
    const int tq = c >> 2, tp = c & 3, w = wave - 4;
#pragma unroll
    for (int s = 0; s < 4; ++s) { LAS unsigned char* ap = L + L_STAGE + (32 * s + 8 * g + tq) * 128 + 32 * w + 8 * tp; wof[s] = trread2(ap, ap + 512); }
  }
  bar();
  const u64 ep = *(volatile LAS u64*)(L + L_FLAG + F_EPOCH);
  const u32 tag32 = (u32)ep, t40lo = ((u32)ep) << 24, t40hi = (u32)(ep >> 8);
  if (wave < 4) {
    const int hh = wave, ch = lane >> 3, c0 = (2 * lane) & 15;
    float mw[4], lw[4], v0[4], v1[4];
#pragma unroll
    for (int w = 0; w < 4; ++w) {
      LAS unsigned char* R = L + L_RING + w * (RING * 4096);
      const f32x2 ml = *(LAS f32x2*)(R + 2048 + hh * 8);
      mw[w] = ml[0]; lw[w] = ml[1];
      v0[w] = *(LAS float*)(R + ch * 256 + c0 * 16 + hh * 4);
      v1[w] = *(LAS float*)(R + ch * 256 + c0 * 16 + 16 + hh * 4);
    }
    const float M = fmaxf(fmaxf(mw[0], mw[1]), fmaxf(mw[2], mw[3]));
    float Ls = 0.f, o0 = 0.f, o1 = 0.f;
#pragma unroll
    for (int w = 0; w < 4; ++w) { const float f = __builtin_amdgcn_exp2f(mw[w] - M); Ls = fmaf(f, lw[w], Ls); o0 = fmaf(f, v0[w], o0); o1 = fmaf(f, v1[w], o1); }
    const float inv = __builtin_amdgcn_rcpf(Ls);
    const u32 slot = ((akv * 4 + hh) * 8 + ab) * 4 + as;
    st_gran(p.ws, WS_OP + (slot * 64 + lane) * 8, pk2(o0 * inv, o1 * inv), tag32);
    if (lane < 2) st_gran(p.ws, WS_ML + (slot * 2 + lane) * 8, __float_as_uint(lane ? Ls : M), tag32);
  } else {
    const int w = wave - 4;
    const u32 slot = (whead * 8 + 2 * w) * 4;
    u64 o[8] = {0, 0, 0, 0, 0, 0, 0, 0}, ml = 0;
    __builtin_amdgcn_s_sleep(H1DLY);
    {
      u32 have = 0;
      NOUNROLL
      for (u32 n = 0; n < SPIN_GLB; ++n) {
        u64 t[9];
#pragma unroll
        for (int s = 0; s < 8; ++s) t[s] = ld_gran(p.ws, ((have >> s) & 1) ? WS_DUMMY : WS_OP + ((slot + s) * 64 + lane) * 8);
        t[8] = ld_gran(p.ws, ((have >> 8) & 1) ? WS_DUMMY : WS_ML + (slot * 2 + (lane & 15)) * 8);
#pragma unroll
        for (int s = 0; s < 9; ++s) {
          const bool fresh = !((have >> s) & 1);
          if (s < 8) o[s] = fresh ? t[s] : o[s]; else ml = fresh ? t[8] : ml;
          if (__all((u32)(t[s] >> 32) == tag32)) have |= 1u << s;
        }
        if (have == 0x1ffu) break;
        __builtin_amdgcn_s_sleep(POLLSLP);
      }
    }
    const u32 mlv = (u32)ml;
#pragma unroll
    for (int bb = 0; bb < 2; ++bb) {
      float ms[4], ls[4];
#pragma unroll
      for (int s = 0; s < 4; ++s) { ms[s] = __uint_as_float(__builtin_amdgcn_readlane(mlv, 8 * bb + 2 * s)); ls[s] = __uint_as_float(__builtin_amdgcn_readlane(mlv, 8 * bb + 2 * s + 1)); }
      const float M = fmaxf(fmaxf(ms[0], ms[1]), fmaxf(ms[2], ms[3]));
      float W = 0.f, o0 = 0.f, o1 = 0.f;
#pragma unroll
      for (int s = 0; s < 4; ++s) { const float wt = ls[s] * __builtin_amdgcn_exp2f(ms[s] - M); W += wt; o0 = fmaf(wt, bflo((u32)o[4 * bb + s]), o0); o1 = fmaf(wt, bfhi((u32)o[4 * bb + s]), o1); }
      const float inv = __builtin_amdgcn_rcpf(W);
      *(LAS u32*)(L + L_O + (2 * w + bb) * 256 + lane * 4) = pk2(o0 * inv, o1 * inv);
    }
    LGKM0();
    if (lane == 0) lds_add(L + L_FLAG + F_SYNC3, 1u);
    NOUNROLL
    for (u32 n = 0; n < SPIN_LDS; ++n) { if (lds_ld(L + L_FLAG + F_SYNC3) >= 4) break; }
    asm volatile("" ::: "memory");
    f32x4 a = {0.f, 0.f, 0.f, 0.f}, a2 = {0.f, 0.f, 0.f, 0.f};
    a = mfma32(*(LAS u32x4*)(L + L_O + (c & 7) * 256 + 16 * g), wof[0], a);
    a2 = mfma32(*(LAS u32x4*)(L + L_O + (c & 7) * 256 + 64 + 16 * g), wof[1], a2);
    a = mfma32(*(LAS u32x4*)(L + L_O + (c & 7) * 256 + 128 + 16 * g), wof[2], a);
    a2 = mfma32(*(LAS u32x4*)(L + L_O + (c & 7) * 256 + 192 + 16 * g), wof[3], a2);
    a += a2;
    if (g < 2) {
#pragma unroll
      for (int i = 0; i < 4; ++i) {
        const u32 u = __float_as_uint(a[i]);
        st_gran(p.ws, WS_OUT + ((((4 * g + i) * 32 + 4 * wcc + w) * 48) + whead) * 128 + c * 8, t40lo | ((u + 0x7fu + ((u >> 8) & 1u)) >> 8), t40hi);
      }
    }
    if (lane == 0) lds_add(L + L_FLAG + F_OUTP, 1u);
  }
  if (wave == 0) {
    const float xres = bflo(p.x[(r >> 2) * DM + 16 * (4 * xg + (r & 3)) + c]);
    const u32 base = WS_OUT + (((r >> 2) * 32 + 4 * xg + (r & 3)) * 48) * 128 + c * 8;
    u64 t[12] = {0, 0, 0, 0, 0, 0, 0, 0, 0, 0, 0, 0};
    lds_wait_ge(L + L_FLAG + F_OUTP, 4);
    __builtin_amdgcn_s_sleep(H2DLY);
    {
      u32 have = 0;
      NOUNROLL
      for (u32 n = 0; n < SPIN_GLB; ++n) {
        u64 tt[12];
#pragma unroll
        for (int i = 0; i < 12; ++i) { const int pr = (4 * i + g < 46) ? 4 * i + g : 45; tt[i] = ld_gran(p.ws, ((have >> i) & 1) ? WS_DUMMY : base + pr * 128); }
#pragma unroll
        for (int i = 0; i < 12; ++i) {
          t[i] = ((have >> i) & 1) ? t[i] : tt[i];
          if (__all((((u32)tt[i] ^ t40lo) >> 24) == 0 && (u32)(tt[i] >> 32) == t40hi)) have |= 1u << i;
        }
        if (have == 0xfffu) break;
        __builtin_amdgcn_s_sleep(POLLSLP);
      }
    }
    float sa = 0.f, sf = 0.f;
#pragma unroll
    for (int i = 0; i < 8; ++i) sa += __uint_as_float((u32)t[i] << 8);
#pragma unroll
    for (int i = 8; i < 12; ++i) sf += (4 * i + g < 46) ? __uint_as_float((u32)t[i] << 8) : 0.f;
    sa = xrow_sum(sa); sf = xrow_sum(sf);
    const float res = rbf(xres + rbf(sa)) + rbf(sf);
    if (g == 0) p.out[(r >> 2) * DM + 16 * (4 * xg + (r & 3)) + c] = (u16)(pk2(res, 0.f) & 0xffffu);
    if (blk == 0 && lane == 0) __hip_atomic_store((GAS u64*)(p.ws + WS_EPOCH), ep, __ATOMIC_RELAXED, __HIP_MEMORY_SCOPE_AGENT);
  }
}

extern "C" void kernel_launch(void* const* d_in, const int* in_sizes, int n_in, void* d_out, int out_size, void* d_ws, size_t ws_size, hipStream_t stream) {
  Params p;
  p.x = (const u16*)d_in[0]; p.k = (const u16*)d_in[1]; p.v = (const u16*)d_in[2]; p.wq = (const u16*)d_in[3]; p.wo = (const u16*)d_in[4];
  p.wg = (const u16*)d_in[6]; p.wu = (const u16*)d_in[7]; p.wd = (const u16*)d_in[8];
  p.out = (u16*)d_out; p.ws = (unsigned char*)d_ws;
  hipLaunchKernelGGL(fused_decode_kernel, dim3(NWG), dim3(NTHR), 0, stream, p);
}
```

```cpp
#define KPOL "nt"
#define VPOL "nt"
#include <hip/hip_runtime.h>
#include <stdint.h>

#define GAS __attribute__((address_space(1)))
#define LAS __attribute__((address_space(3)))
typedef unsigned u32;
typedef unsigned long long u64;
typedef unsigned short u16;
typedef u32 u32x4 __attribute__((ext_vector_type(4)));
typedef u32 u32x2 __attribute__((ext_vector_type(2)));
typedef float f32x4 __attribute__((ext_vector_type(4)));
typedef float f32x2 __attribute__((ext_vector_type(2)));
typedef short s16x4 __attribute__((ext_vector_type(4)));
typedef __bf16 bf16x8 __attribute__((ext_vector_type(8)));
typedef __bf16 bf16x2 __attribute__((ext_vector_type(2)));

constexpr int NCODE = 21;
constexpr int PACE10 = 71;
constexpr int PACE_T0 = 250;
constexpr int PACE_FROM = 8;
constexpr int DAHEAD = 3;
constexpr int DPRO = 1;
constexpr int TQ0 = 380, TQR = 48;
constexpr int TGS = 1000;
constexpr int TG15 = 950;
constexpr int TG2 = 1350;
constexpr int POLLSLP = 4, H1DLY = 21, H2DLY = 13;

constexpr int DM = 512, SK = 8192, DH = 128, FF = 1792;
constexpr int NWG = 256, NTHR = 512;
constexpr int RING = 8;
constexpr int NUNIT = 64;
constexpr float SC = 0.08838834764831845f * 1.4426950408889634f;
constexpr float THR_RAW = 8.0f / SC;

constexpr u32 WS_EPOCH = 0;
constexpr u32 WS_DUMMY = 2048;
constexpr u32 WS_START = 4096;
constexpr u32 WS_T0 = 8192;
constexpr u32 WS_Q = 65536;
constexpr u32 WS_GU = WS_Q + 8 * 8 * 4 * 512 * 8;
constexpr u32 WS_H = WS_GU + 28 * 2 * 4 * 8 * 64 * 8;
constexpr u32 WS_OP = WS_H + 8 * 1792 * 8;
constexpr u32 WS_ML = WS_OP + 32 * 8 * 4 * 64 * 8;
constexpr u32 WS_OUT = WS_ML + 32 * 8 * 4 * 2 * 8;
constexpr u32 WS_END = WS_OUT + 8 * 32 * 48 * 16 * 8;
static_assert(WS_END <= (8u << 20), "ws map");

constexpr u32 L_RING = 0;
constexpr u32 L_STAGE = 4 * RING * 4096;
constexpr u32 L_XN = L_STAGE + 16384;
constexpr u32 XN_STR = 1056;
constexpr u32 L_Q = L_XN + 8 * XN_STR;
constexpr u32 L_O = L_Q + 1024;
constexpr u32 L_FLAG = L_O + 2048;
constexpr u32 L_SCR = L_FLAG + 256;
constexpr u32 L_END = L_SCR + 1024;
static_assert(L_END <= 163840, "lds");
constexpr u32 F_SYNC = 0, F_Q = 4, F_SYNC2 = 8, F_WO = 12, F_EPOCH = 16, F_SYNC3 = 24, F_ANCH = 28, F_OUTP = 32;

constexpr u32 PER256 = (u32)(4096.0 * 1024.0 / (PACE10 * 0.1e12) * 1e8 * 256.0);
constexpr u32 SPIN_LDS = 1u << 22, SPIN_GLB = 1u << 17;

struct Params {
  const u16* x; const u16* k; const u16* v; const u16* wq; const u16* wo; const u16* wg; const u16* wu; const u16* wd;
  u16* out; unsigned char* ws;
};

__device__ __forceinline__ u32 rt32() { return (u32)__builtin_amdgcn_s_memrealtime(); }
__device__ __forceinline__ float bflo(u32 w) { return __uint_as_float(w << 16); }
__device__ __forceinline__ float bfhi(u32 w) { return __uint_as_float(w & 0xffff0000u); }
__device__ __forceinline__ u32 pk2(float lo, float hi) { f32x2 t = {lo, hi}; return __builtin_bit_cast(u32, __builtin_convertvector(t, bf16x2)); }
__device__ __forceinline__ float rbf(float f) { return bflo(pk2(f, f)); }
template <int CTRL> __device__ __forceinline__ float dpp(float x) {
  return __builtin_bit_cast(float, __builtin_amdgcn_mov_dpp(__builtin_bit_cast(int, x), CTRL, 0xf, 0xf, true));
}
__device__ __forceinline__ float xrow_max(float x) {
  auto s = __builtin_amdgcn_permlane16_swap(__float_as_uint(x), __float_as_uint(x), false, false);
  x = fmaxf(__uint_as_float(s[0]), __uint_as_float(s[1]));
  auto t = __builtin_amdgcn_permlane32_swap(__float_as_uint(x), __float_as_uint(x), false, false);
  return fmaxf(__uint_as_float(t[0]), __uint_as_float(t[1]));
}
__device__ __forceinline__ float xrow_sum(float x) {
  auto s = __builtin_amdgcn_permlane16_swap(__float_as_uint(x), __float_as_uint(x), false, false);
  x = __uint_as_float(s[0]) + __uint_as_float(s[1]);
  auto t = __builtin_amdgcn_permlane32_swap(__float_as_uint(x), __float_as_uint(x), false, false);
  return __uint_as_float(t[0]) + __uint_as_float(t[1]);
}
__device__ __forceinline__ float wave_sum(float x) {
  x += dpp<0xB1>(x); x += dpp<0x4E>(x); x += dpp<0x141>(x); x += dpp<0x128>(x);
  return xrow_sum(x);
}
__device__ __forceinline__ f32x4 mfma32(u32x4 a, u32x4 b, f32x4 c) {
  return __builtin_amdgcn_mfma_f32_16x16x32_bf16(__builtin_bit_cast(bf16x8, a), __builtin_bit_cast(bf16x8, b), c, 0, 0, 0);
}
__device__ __forceinline__ f32x4 mfma16(u32x2 a, u32x2 b, f32x4 c) {
  return __builtin_amdgcn_mfma_f32_16x16x16bf16_1k(__builtin_bit_cast(s16x4, a), __builtin_bit_cast(s16x4, b), c, 0, 0, 0);
}
__device__ __forceinline__ u32x2 trread(LAS unsigned char* p) {
  return __builtin_bit_cast(u32x2, __builtin_amdgcn_ds_read_tr16_b64_v4i16((LAS s16x4*)p));
}
__device__ __forceinline__ u32x4 trread2(LAS unsigned char* p0, LAS unsigned char* p1) {
  const u32x2 a = trread(p0), b = trread(p1); return u32x4{a.x, a.y, b.x, b.y};
}
#define DMA4DEF(NAME, POL) \
__device__ __forceinline__ void NAME(u32 ldst, u32 v0, u32 v1, u32 v2, u32 v3, __amdgpu_buffer_rsrc_t srd, u32 soff) { \
  u32 keep; \
  asm volatile("s_waitcnt lgkmcnt(0)\n\ts_mov_b32 %0, m0\n\ts_mov_b32 m0, %1\n\ts_nop 0\n\t" \
               "buffer_load_dwordx4 %2, %6, %7 offen " POL " lds\n\t" \
               "buffer_load_dwordx4 %3, %6, %7 offen offset:1024 " POL " lds\n\t" \
               "buffer_load_dwordx4 %4, %6, %7 offen offset:2048 " POL " lds\n\t" \
               "buffer_load_dwordx4 %5, %6, %7 offen offset:3072 " POL " lds\n\t" \
               "s_mov_b32 m0, %0" \
               : "=&s"(keep) : "s"(ldst), "v"(v0), "v"(v1), "v"(v2), "v"(v3), "s"(srd), "s"(soff) : "memory"); \
}
DMA4DEF(dma4k, KPOL)
DMA4DEF(dma4v, VPOL)
__device__ __forceinline__ void dma1(u32 ldst, const void* gp) {
  u32 keep;
  asm volatile("s_mov_b32 %0, m0\n\ts_mov_b32 m0, %1\n\ts_nop 0\n\tglobal_load_lds_dwordx4 %2, off nt\n\ts_mov_b32 m0, %0"
               : "=&s"(keep) : "s"(ldst), "v"(gp) : "memory");
}
__device__ __forceinline__ void dma1p(u32 ldst, const void* gp) {
  u32 keep;
  asm volatile("s_mov_b32 %0, m0\n\ts_mov_b32 m0, %1\n\ts_nop 0\n\tglobal_load_lds_dwordx4 %2, off\n\ts_mov_b32 m0, %0"
               : "=&s"(keep) : "s"(ldst), "v"(gp) : "memory");
}
#define VMCNT(N) asm volatile("s_waitcnt vmcnt(" #N ")" ::: "memory")
#define LGKM0() asm volatile("s_waitcnt lgkmcnt(0)" ::: "memory")
#define NOUNROLL _Pragma("nounroll")
__device__ __forceinline__ void wait_units(int n) {
  switch (n) {
    case 0: VMCNT(0); break;   case 1: VMCNT(4); break;   case 2: VMCNT(8); break;   case 3: VMCNT(12); break;
    case 4: VMCNT(16); break;  case 5: VMCNT(20); break;  case 6: VMCNT(24); break;  default: VMCNT(28); break;
  }
}
__device__ __forceinline__ void bar() { LGKM0(); __builtin_amdgcn_s_barrier(); asm volatile("" ::: "memory"); }
__device__ __forceinline__ u32 lds_ld(LAS unsigned char* p) { return *(volatile LAS u32*)p; }
__device__ __forceinline__ void lds_add(LAS unsigned char* p, u32 v) { __hip_atomic_fetch_add((LAS u32*)p, v, __ATOMIC_RELAXED, __HIP_MEMORY_SCOPE_WORKGROUP); }
__device__ __forceinline__ void lds_wait_ge(LAS unsigned char* p, u32 want) {
  NOUNROLL
  for (u32 n = 0; n < SPIN_LDS; ++n) { if (lds_ld(p) >= want) break; __builtin_amdgcn_s_sleep(1); }
  asm volatile("" ::: "memory");
}
__device__ __forceinline__ void wait_until(u32 due) { while ((int)(rt32() - due) < 0) __builtin_amdgcn_s_sleep(4); }
__device__ __forceinline__ void st_gran(unsigned char* ws, u32 off, u32 lo, u32 hi) {
  __hip_atomic_store((GAS u64*)(ws + off), ((u64)hi << 32) | lo, __ATOMIC_RELAXED, __HIP_MEMORY_SCOPE_AGENT);
}
__device__ __forceinline__ u64 ld_gran(unsigned char* ws, u32 off) {
  return __hip_atomic_load((GAS u64*)(ws + off), __ATOMIC_RELAXED, __HIP_MEMORY_SCOPE_AGENT);
}

__global__ void __launch_bounds__(NTHR, 2) fused_decode_kernel(Params p) {
  __shared__ __attribute__((aligned(16))) unsigned char lds_raw[L_END];
  LAS unsigned char* L = (LAS unsigned char*)lds_raw;
  const u32 lbase = (u32)(size_t)L;
  const int tid = threadIdx.x, lane = tid & 63, wave = __builtin_amdgcn_readfirstlane(tid >> 6);
  const int g = lane >> 4, c = lane & 15;
  const int blk = blockIdx.x, xg = blk & 7, r = blk >> 3;
  const u32 t0 = rt32();
  u64 pc0; asm volatile("s_getpc_b64 %0" : "=s"(pc0));
  const __amdgpu_buffer_rsrc_t wsr = __builtin_amdgcn_make_buffer_rsrc(p.ws, 0, WS_END, 0x00020000);
  const int as = r & 3, ab = r >> 2, akv = xg, apair = ab * 8 + akv;
  const int whead = 4 * xg + (r & 3), wcc = r >> 2;

  if (wave < 4) {
    const size_t pbase = (size_t)apair * SK * DH;
    const __amdgpu_buffer_rsrc_t ksr = __builtin_amdgcn_make_buffer_rsrc((void*)(p.k + pbase), 0, SK * DH * 2, 0x00020000);
    const __amdgpu_buffer_rsrc_t vsr = __builtin_amdgcn_make_buffer_rsrc((void*)(p.v + pbase), 0, SK * DH * 2, 0x00020000);
    const u32 vk0 = g * 256 + 16 * (c ^ g), vk1 = g * 256 + 16 * (c ^ (4 + g)), vk2 = g * 256 + 16 * (c ^ (8 + g)), vk3 = g * 256 + 16 * (c ^ (12 + g));
    const u32 vv0 = g * 256 + 16 * (c ^ (g << 1)), vv1 = g * 256 + 16 * (c ^ ((4 + g) << 1));
    const u32 ring = lbase + L_RING + wave * (RING * 4096);
    int nis = 0;
    int landed = 0;
    u32 tnow = t0, pA = t0 + PACE_T0;
    const u32 sbase = as * 16384u + wave * 4096u;
    auto issue = [&]() {
      if (nis >= PACE_FROM) {
        if (nis < PACE_FROM + 6) { const u32 an = __builtin_amdgcn_readfirstlane(lds_ld(L + L_FLAG + F_ANCH)); if (an) pA = an + PACE_T0; }
        const u32 due = pA + (((u32)nis * PER256) >> 8);
        if ((int)(tnow - due) < 0) {
          NOUNROLL
          for (u32 n = 0; n < SPIN_LDS; ++n) { tnow = rt32(); if ((int)(tnow - due) >= 0) break; __builtin_amdgcn_s_sleep(1); }
        }
      }
      const u32 soff = (u32)(nis >> 1) * 65536u + sbase, ldst = ring + (u32)(nis & (RING - 1)) * 4096u;
      if (nis & 1) dma4v(ldst, vv0, vv1, vv0, vv1, vsr, soff); else dma4k(ldst, vk0, vk1, vk2, vk3, ksr, soff);
      ++nis;
    };
    issue();
    bar();
    NOUNROLL
    while (nis < RING) {
      if (nis >= DPRO) { wait_units(DPRO - 1); landed = nis - (DPRO - 1); }
      if (lds_ld(L + L_FLAG + F_Q) >= 4) break;
      issue();
    }
    auto refill = [&](int cons) { while (nis - landed < DAHEAD && nis < cons + RING && nis < NUNIT) issue(); };
    lds_wait_ge(L + L_FLAG + F_Q, 4);
    u32x4 qf[4];
#pragma unroll
    for (int s = 0; s < 4; ++s) qf[s] = *(LAS u32x4*)(L + L_Q + (c & 3) * 256 + 64 * s + 16 * g);
    refill(0);
    LAS unsigned char* R = L + L_RING + wave * (RING * 4096);
    const u32 ka = c * 256 + 16 * (g ^ c);
    const u32 va = (4 * g + (c >> 2)) * 256 + 32 * ((4 * g + (c >> 2)) & 7) + 8 * (c & 3);
    float m = -INFINITY, lsum = 0.f;
    f32x4 acc[8];
#pragma unroll
    for (int i = 0; i < 8; ++i) acc[i] = f32x4{0.f, 0.f, 0.f, 0.f};
    NOUNROLL
    for (int j = 0; j < NUNIT / 2; ++j) {
      wait_units(nis - 2 * j - 1);
      landed = landed > 2 * j + 1 ? landed : 2 * j + 1;
      refill(2 * j);
      LAS unsigned char* Kt = R + ((2 * j) & (RING - 1)) * 4096;
      LAS unsigned char* Vt = R + ((2 * j + 1) & (RING - 1)) * 4096;
      f32x4 sa = {0.f, 0.f, 0.f, 0.f}, sb = {0.f, 0.f, 0.f, 0.f};
      {
        const u32x4 k0 = *(LAS u32x4*)(Kt + ka), k1 = *(LAS u32x4*)(Kt + (ka ^ 64)), k2 = *(LAS u32x4*)(Kt + (ka ^ 128)), k3 = *(LAS u32x4*)(Kt + (ka ^ 192));
        sa = mfma32(k0, qf[0], sa); sb = mfma32(k1, qf[1], sb); sa = mfma32(k2, qf[2], sa); sb = mfma32(k3, qf[3], sb);
      }
      const f32x4 sc = sa + sb;
      const float tmax = fmaxf(fmaxf(sc[0], sc[1]), fmaxf(sc[2], sc[3]));
      if (__builtin_amdgcn_ballot_w64(tmax > m + THR_RAW) != 0ull) {
        const float mn = fmaxf(m, xrow_max(tmax));
        const float al = __builtin_amdgcn_exp2f((m - mn) * SC);
        m = mn; lsum *= al;
        const float a0 = dpp<0x00>(al), a1 = dpp<0x55>(al), a2 = dpp<0xAA>(al), a3 = dpp<0xFF>(al);
#pragma unroll
        for (int i = 0; i < 8; ++i) { acc[i][0] *= a0; acc[i][1] *= a1; acc[i][2] *= a2; acc[i][3] *= a3; }
      }
      const float mc = m * SC;
      const float p0 = __builtin_amdgcn_exp2f(fmaf(sc[0], SC, -mc)), p1 = __builtin_amdgcn_exp2f(fmaf(sc[1], SC, -mc));
      const float p2 = __builtin_amdgcn_exp2f(fmaf(sc[2], SC, -mc)), p3 = __builtin_amdgcn_exp2f(fmaf(sc[3], SC, -mc));
      lsum += (p0 + p1) + (p2 + p3);
      const u32x2 pa = {pk2(p0, p1), pk2(p2, p3)};
      wait_units(nis - 2 * j - 2);
      landed = landed > 2 * j + 2 ? landed : 2 * j + 2;
      refill(2 * j + 1);
#pragma unroll
      for (int i = 0; i < 8; ++i) acc[i] = mfma16(pa, trread(Vt + (va ^ (32 * i))), acc[i]);
      refill(2 * j + 2);
    }

    lsum = xrow_sum(lsum);
    if (g == 0) {
#pragma unroll
      for (int i = 0; i < 8; ++i) *(LAS f32x4*)(R + i * 256 + c * 16) = acc[i];
      if (c < 4) *(LAS f32x2*)(R + 2048 + c * 8) = f32x2{m * SC, lsum};
    }
  } else {
    const int hw = wave - 4;
    __builtin_amdgcn_s_setprio(3);
    u32x4 xa, xb; u32x2 ev;
    if (hw == 0 && lane < 16) *(LAS u32x4*)(L + L_FLAG + lane * 16) = u32x4{0, 0, 0, 0};
    const int kq = r & 3;
    if (NCODE && r * 4 + hw < NCODE) {
      dma1p(lbase + L_SCR, (const char*)(pc0 & ~255ull) + (r * 4 + hw) * 1024 + lane * 16);
    }
    {
      const u16* xp = p.x + (2 * hw) * DM + lane * 8;
      const u16* wp = p.wq + (size_t)(kq * 128 + hw * 32 + (lane >> 3)) * 4096 + xg * 512 + (r >> 2) * 64 + (lane & 7) * 8;
      const u32 ldst = lbase + L_STAGE + hw * 4096;
      u32 keep;
      asm volatile("global_load_dwordx4 %0, %4, off\n\tglobal_load_dwordx4 %1, %4, off offset:1024\n\tglobal_load_dwordx2 %2, %5, off\n\t"
                   "s_mov_b32 %3, m0\n\ts_mov_b32 m0, %10\n\ts_nop 0\n\t"
                   "global_load_lds_dwordx4 %6, off nt\n\ts_add_u32 m0, m0, 0x400\n\ts_nop 0\n\t"
                   "global_load_lds_dwordx4 %7, off nt\n\ts_add_u32 m0, m0, 0x400\n\ts_nop 0\n\t"
                   "global_load_lds_dwordx4 %8, off nt\n\ts_add_u32 m0, m0, 0x400\n\ts_nop 0\n\t"
                   "global_load_lds_dwordx4 %9, off nt\n\ts_mov_b32 m0, %3\n\t"
                   "s_waitcnt lgkmcnt(0)\n\ts_barrier\n\ts_waitcnt vmcnt(4)"
                   : "=&v"(xa), "=&v"(xb), "=&v"(ev), "=&s"(keep)
                   : "v"(xp), "v"(p.ws + WS_EPOCH), "v"(wp), "v"(wp + (size_t)8 * 4096), "v"(wp + (size_t)16 * 4096), "v"(wp + (size_t)24 * 4096), "s"(ldst)
                   : "memory", "scc");
    }
    const u64 ep = (((u64)ev.y << 32) | ev.x) + 1ull;
    const u32 tag32 = (u32)ep, t48lo = ((u32)ep) << 16, t48hi = (u32)(ep >> 16), t40lo = ((u32)ep) << 24, t40hi = (u32)(ep >> 8);
#pragma unroll
    for (int q = 0; q < 2; ++q) {
      const u32x4 xv = q ? xb : xa;
      float v[8];
#pragma unroll
      for (int i = 0; i < 4; ++i) { v[2 * i] = bflo(xv[i]); v[2 * i + 1] = bfhi(xv[i]); }
      const float mean = wave_sum(((v[0] + v[1]) + (v[2] + v[3])) + ((v[4] + v[5]) + (v[6] + v[7]))) * (1.f / DM);
      float q2 = 0.f;
#pragma unroll
      for (int i = 0; i < 8; ++i) { const float d = v[i] - mean; q2 = fmaf(d, d, q2); }
      const float var = rbf(wave_sum(q2) * (1.f / DM));
      const float rden = __builtin_amdgcn_rcpf(rbf(__builtin_amdgcn_sqrtf(rbf(var + 1e-5f))));
      const float mb = rbf(mean);
      u32x4 o;
#pragma unroll
      for (int i = 0; i < 4; ++i) { const u32 d = pk2(v[2 * i] - mb, v[2 * i + 1] - mb); o[i] = pk2(bflo(d) * rden, bfhi(d) * rden); }
      *(LAS u32x4*)(L + L_XN + (2 * hw + q) * XN_STR + lane * 16) = o;
    }
    const int tq = c >> 2, tp = c & 3;
    auto tile_mma = [&](const u32x4 (&act)[4], int cg) {
      f32x4 a0 = {0.f, 0.f, 0.f, 0.f}, a1 = {0.f, 0.f, 0.f, 0.f};
      LAS unsigned char* ap = L + L_STAGE + (8 * g + tq) * 128 + 32 * cg + 8 * tp;
      a0 = mfma32(act[0], trread2(ap, ap + 512), a0);
      a1 = mfma32(act[1], trread2(ap + 4096, ap + 4096 + 512), a1);
      a0 = mfma32(act[2], trread2(ap + 8192, ap + 8192 + 512), a0);
      a1 = mfma32(act[3], trread2(ap + 12288, ap + 12288 + 512), a1);
      return a0 + a1;
    };
    auto xn_frags = [&](u32x4 (&act)[4], int k0) {
#pragma unroll
      for (int s = 0; s < 4; ++s) act[s] = *(LAS u32x4*)(L + L_XN + (c & 7) * XN_STR + 2 * k0 + 64 * s + 16 * g);
    };
    auto f24 = [&](float f) { const u32 u = __float_as_uint(f); return t40lo | ((u + 0x7fu + ((u >> 8) & 1u)) >> 8); };
    auto ok40 = [&](const u32x4& t) { return ((t[0] ^ t40lo) >> 24) == 0 && t[1] == t40hi && ((t[2] ^ t40lo) >> 24) == 0 && t[3] == t40hi; };
    auto ok48 = [&](const u32x4& t) { return ((t[0] ^ t48lo) >> 16) == 0 && t[1] == t48hi && ((t[2] ^ t48lo) >> 16) == 0 && t[3] == t48hi; };
    u32x4 act[4];
    asm volatile("s_waitcnt vmcnt(0) lgkmcnt(0)" ::: "memory");
    if (lane == 0) lds_add(L + L_FLAG + F_SYNC, 1u);
    lds_wait_ge(L + L_FLAG + F_SYNC, 4);
    xn_frags(act, kq * 128);
    {
      const f32x4 d = tile_mma(act, hw);
      if (g < 2) {
#pragma unroll
        for (int i = 0; i < 4; ++i) st_gran(p.ws, WS_Q + ((((xg * 8 + 4 * g + i) * 4 + kq) * 512) + (r >> 2) * 64 + 16 * hw + c) * 8, f24(d[i]), t40hi);
      }
    }
    if (hw == 1 && lane == 0) st_gran(p.ws, WS_T0 + blk * 8, t40lo | (t0 & 0xffffffu), t40hi);
    LGKM0();
    if (lane == 0) lds_add(L + L_FLAG + F_SYNC, 1u);
    if (hw < 2) {
      const u32 qb = WS_Q + ((akv * 8 + ab) * 4) * 512 * 8;
      wait_until(t0 + TQ0 - (u32)(r * TQR) / 16);
      float q0[2], q1[2];
      NOUNROLL
      for (u32 n = 0; n < SPIN_GLB; ++n) {
        bool ok = true;
#pragma unroll
        for (int h = 0; h < 2; ++h) {
          q0[h] = 0.f; q1[h] = 0.f;
#pragma unroll
          for (int i = 0; i < 4; ++i) {
            const u32x4 t = __builtin_amdgcn_raw_buffer_load_b128(wsr, qb + (i * 512 + (2 * hw + h) * 128 + 2 * lane) * 8, 0, 16);
            ok &= ok40(t); q0[h] += __uint_as_float(t[0] << 8); q1[h] += __uint_as_float(t[2] << 8);
          }
        }
        if (__all(ok)) break;
        __builtin_amdgcn_s_sleep(POLLSLP);
        asm volatile("" ::: "memory");
      }
      *(LAS u32*)(L + L_Q + (2 * hw) * 256 + lane * 4) = pk2(q0[0], q1[0]);
      *(LAS u32*)(L + L_Q + (2 * hw + 1) * 256 + lane * 4) = pk2(q0[1], q1[1]);
      LGKM0();
      if (lane == 0) lds_add(L + L_FLAG + F_Q, 2u);
      if (hw == 1) {
        const u32 ld_ = lbase + L_SCR;
        u32 keep;
        const u16* tp = (lane < 32 ? p.k : p.v) + (size_t)apair * SK * DH + (size_t)(lane & 31) * 32768 + as * 8192 + 4096;
        if ((lane & 31) >= 4)
          asm volatile("s_mov_b32 %0, m0\n\ts_mov_b32 m0, %1\n\ts_nop 0\n\tglobal_load_lds_dword %2, off nt\n\ts_mov_b32 m0, %0" : "=&s"(keep) : "s"(ld_), "v"(tp) : "memory");
        const unsigned char* tw = lane < 8 ? p.ws + WS_OUT + ((lane * 32 + 4 * wcc) * 48 + 47) * 128
                                : lane == 8 ? p.ws + WS_OUT + (((r >> 2) * 32 + 4 * xg + (r & 3)) * 48 + 47) * 128
                                : (const unsigned char*)(p.out + (r >> 2) * DM + 16 * (4 * xg + (r & 3)));
        if (lane < 10)
          asm volatile("s_mov_b32 %0, m0\n\ts_mov_b32 m0, %1\n\ts_nop 0\n\tglobal_load_lds_dword %2, off\n\ts_mov_b32 m0, %0" : "=&s"(keep) : "s"(ld_), "v"(tw) : "memory");
      }
      if (hw == 0) {
        u64 ts = 0;
        bool ok = false;
        NOUNROLL
        for (u32 n = 0; n < 64 && !ok; ++n) {
          ts = ld_gran(p.ws, WS_T0 + (xg + 8 * (lane & 31)) * 8);
          ok = __all((((u32)ts ^ t40lo) >> 24) == 0 && (u32)(ts >> 32) == t40hi);
          if (!ok) __builtin_amdgcn_s_sleep(8);
        }
        int d = ((int)(((u32)ts - t0) << 8)) >> 8;
        d = min(d, __builtin_amdgcn_mov_dpp(d, 0xB1, 0xf, 0xf, true)); d = min(d, __builtin_amdgcn_mov_dpp(d, 0x4E, 0xf, 0xf, true));
        d = min(d, __builtin_amdgcn_mov_dpp(d, 0x141, 0xf, 0xf, true)); d = min(d, __builtin_amdgcn_mov_dpp(d, 0x128, 0xf, 0xf, true));
        int dm = min(min(__builtin_amdgcn_readlane(d, 0), __builtin_amdgcn_readlane(d, 16)), 0);
        dm = dm < -400 ? -400 : dm;
        if (ok && lane == 0) *(volatile LAS u32*)(L + L_FLAG + F_ANCH) = (t0 + dm) | 1u;
      }
      if (hw == 1) {
        wait_until(t0 + TGS);
        NOUNROLL
        for (u32 n = 0; n < SPIN_GLB; ++n) {
          const u32x4 a = __builtin_amdgcn_raw_buffer_load_b128(wsr, WS_START + lane * 16, 0, 16);
          const u32x4 b = __builtin_amdgcn_raw_buffer_load_b128(wsr, WS_START + 1024 + lane * 16, 0, 16);
          const u32 e0 = (u32)ep, e1 = (u32)(ep >> 32);
          const bool ok = a[0] == e0 && a[1] == e1 && a[2] == e0 && a[3] == e1 && b[0] == e0 && b[1] == e1 && b[2] == e0 && b[3] == e1;
          if (__all(ok)) break;
          __builtin_amdgcn_s_sleep(32);
          asm volatile("" ::: "memory");
        }
      }
    } else {
      const int h2 = hw - 2;
      u32 nsync2 = 0;
      auto arrive2 = [&]() {
        LGKM0();
        if (lane == 0) lds_add(L + L_FLAG + F_SYNC2, 1u);
        nsync2 += 2;
        lds_wait_ge(L + L_FLAG + F_SYNC2, nsync2);
      };
      auto stage2 = [&](const u16* w, int ld) {
        const u16* wp = w + (size_t)(h2 * 64 + (lane >> 3)) * ld + (lane & 7) * 8;
#pragma unroll
        for (int i = 0; i < 8; ++i) dma1(lbase + L_STAGE + h2 * 8192 + i * 1024, wp + (size_t)i * 8 * ld);
      };
      if (h2 == 1) {
        const u32x4 z = {0u, ~tag32, 0u, ~tag32};
#pragma unroll
        for (int i = 0; i < 2; ++i)
          __builtin_amdgcn_raw_buffer_store_b128(z, wsr, WS_OP + ((((akv * 4 + 2 * i + (lane >> 5)) * 8 + ab) * 4 + as) * 64) * 8 + (lane & 31) * 16, 0, 16);
        if (lane < 4) __builtin_amdgcn_raw_buffer_store_b128(z, wsr, WS_ML + (((akv * 4 + lane) * 8 + ab) * 4 + as) * 16, 0, 16);
      }
      lds_wait_ge(L + L_FLAG + F_SYNC, 8);
      const bool f1 = r < 14, f15 = r >= 14 && r < 18, f2 = r >= 18;
      const int t1 = xg * 14 + r, sl1 = t1 >> 2, fq = t1 & 3, ks = r - 18;
      const u16* wo_tile = p.wo + (size_t)whead * 128 * DM + wcc * 64;
      if (f1) stage2(p.wg + (size_t)fq * 128 * FF + sl1 * 64, FF);
      else if (f2) stage2(p.wd + (size_t)ks * 128 * DM + xg * 64, DM);
      else stage2(wo_tile, DM);
      VMCNT(0);
      if (h2 == 1 && lane == 0) {
        __hip_atomic_store((GAS u64*)(p.ws + WS_START + blk * 8), ep, __ATOMIC_RELAXED, __HIP_MEMORY_SCOPE_AGENT);
        *(volatile LAS u64*)(L + L_FLAG + F_EPOCH) = ep;
      }
      arrive2();
      if (f1) {
        xn_frags(act, fq * 128);
        f32x4 d[2];
        d[0] = tile_mma(act, 2 * h2); d[1] = tile_mma(act, 2 * h2 + 1);
        asm volatile("" : "+v"(d[0]), "+v"(d[1]));
        arrive2();
        stage2(p.wu + (size_t)fq * 128 * FF + sl1 * 64, FF);
#pragma unroll
        for (int w = 0; w < 2; ++w) if (g < 2) {
#pragma unroll
          for (int i = 0; i < 4; ++i) st_gran(p.ws, WS_GU + ((((sl1 * 2 + 0) * 4 + fq) * 8 + 4 * g + i) * 64 + 16 * (2 * h2 + w) + c) * 8, f24(d[w][i]), t40hi);
        }
        VMCNT(8);
        arrive2();
        d[0] = tile_mma(act, 2 * h2); d[1] = tile_mma(act, 2 * h2 + 1);
        asm volatile("" : "+v"(d[0]), "+v"(d[1]));
        arrive2();
        stage2(wo_tile, DM);
#pragma unroll
        for (int w = 0; w < 2; ++w) if (g < 2) {
#pragma unroll
          for (int i = 0; i < 4; ++i) st_gran(p.ws, WS_GU + ((((sl1 * 2 + 1) * 4 + fq) * 8 + 4 * g + i) * 64 + 16 * (2 * h2 + w) + c) * 8, f24(d[w][i]), t40hi);
        }
        VMCNT(8);
        arrive2();
      } else if (f15) {
        const int sl = xg * 4 + (r - 14);
        if (sl < 28) {
          wait_until(t0 + TG15);
          {
            const u32 so = WS_GU + ((((sl * 2 + 1) * 4 + (lane & 3)) * 8) * 64 + 63) * 8;
            NOUNROLL
            for (u32 n = 0; n < SPIN_GLB; ++n) {
              const u64 t = ld_gran(p.ws, so);
              if (__all((((u32)t ^ t40lo) >> 24) == 0 && (u32)(t >> 32) == t40hi)) break;
              __builtin_amdgcn_s_sleep(16);
            }
          }
          float gs[2][2], us[2][2];
          const int cp = lane & 31;
          NOUNROLL
          for (u32 n = 0; n < SPIN_GLB; ++n) {
            bool ok = true;
#pragma unroll
            for (int bb = 0; bb < 2; ++bb) {
              const int b = 4 * h2 + 2 * bb + (lane >> 5);
              gs[bb][0] = gs[bb][1] = us[bb][0] = us[bb][1] = 0.f;
#pragma unroll
              for (int i = 0; i < 4; ++i) {
                const u32x4 tg = __builtin_amdgcn_raw_buffer_load_b128(wsr, WS_GU + ((((sl * 2 + 0) * 4 + i) * 8 + b) * 64 + 2 * cp) * 8, 0, 16);
                const u32x4 tu = __builtin_amdgcn_raw_buffer_load_b128(wsr, WS_GU + ((((sl * 2 + 1) * 4 + i) * 8 + b) * 64 + 2 * cp) * 8, 0, 16);
                ok &= ok40(tg) && ok40(tu);
                gs[bb][0] += __uint_as_float(tg[0] << 8); gs[bb][1] += __uint_as_float(tg[2] << 8);
                us[bb][0] += __uint_as_float(tu[0] << 8); us[bb][1] += __uint_as_float(tu[2] << 8);
              }
            }
            if (__all(ok)) break;
            __builtin_amdgcn_s_sleep(16);
            asm volatile("" ::: "memory");
          }
#pragma unroll
          for (int bb = 0; bb < 2; ++bb) {
            const int b = 4 * h2 + 2 * bb + (lane >> 5);
            const u32 hp = pk2(rbf(gs[bb][0]) * rbf(us[bb][0]), rbf(gs[bb][1]) * rbf(us[bb][1]));
            st_gran(p.ws, WS_H + (b * FF + sl * 64 + 2 * cp) * 8, t48lo | (hp & 0xffffu), t48hi);
            st_gran(p.ws, WS_H + (b * FF + sl * 64 + 2 * cp + 1) * 8, t48lo | (hp >> 16), t48hi);
          }
        }
      } else {
        wait_until(t0 + TG2);
        {
          const u32 so = WS_H + (7 * FF + ks * 128 + 64 * (lane & 1) + 63) * 8;
          NOUNROLL
          for (u32 n = 0; n < SPIN_GLB; ++n) {
            const u64 t = ld_gran(p.ws, so);
            if (__all((((u32)t ^ t48lo) >> 16) == 0 && (u32)(t >> 32) == t48hi)) break;
            __builtin_amdgcn_s_sleep(32);
          }
        }
        const int b = lane >> 3, k0 = 64 * h2 + 8 * (lane & 7);
        u32x4 hv = {0, 0, 0, 0};
        NOUNROLL
        for (u32 n = 0; n < SPIN_GLB; ++n) {
          bool ok = true;
#pragma unroll
          for (int i = 0; i < 4; ++i) {
            const u32x4 t = __builtin_amdgcn_raw_buffer_load_b128(wsr, WS_H + (b * FF + ks * 128 + k0 + 2 * i) * 8, 0, 16);
            ok &= ok48(t);
            hv[i] = (t[0] & 0xffffu) | (t[2] << 16);
          }
          if (__all(ok)) break;
          __builtin_amdgcn_s_sleep(16);
          asm volatile("" ::: "memory");
        }
        *(LAS u32x4*)(L + L_O + b * 256 + k0 * 2) = hv;
        arrive2();
#pragma unroll
        for (int s = 0; s < 4; ++s) act[s] = *(LAS u32x4*)(L + L_O + (c & 7) * 256 + 64 * s + 16 * g);
        f32x4 d[2];
        d[0] = tile_mma(act, 2 * h2); d[1] = tile_mma(act, 2 * h2 + 1);
        asm volatile("" : "+v"(d[0]), "+v"(d[1]));
        arrive2();
        stage2(wo_tile, DM);
#pragma unroll
        for (int w = 0; w < 2; ++w) if (g < 2) {
#pragma unroll
          for (int i = 0; i < 4; ++i) st_gran(p.ws, WS_OUT + ((((4 * g + i) * 32 + 4 * xg + 2 * h2 + w) * 48) + 32 + ks) * 128 + c * 8, f24(d[w][i]), t40hi);
        }
        VMCNT(8);
        arrive2();
      }
      if (h2 == 0 && lane == 0) *(volatile LAS u32*)(L + L_FLAG + F_WO) = 1;
    }
    NOUNROLL
    for (u32 n = 0; n < SPIN_LDS; ++n) { if (lds_ld(L + L_FLAG + F_WO) >= 1) break; __builtin_amdgcn_s_sleep(16); }
    asm volatile("" ::: "memory");
  }

  u32x4 wof[4];
  if (wave >= 4) {
    const int tq = c >> 2, tp = c & 3, w = wave - 4;
#pragma unroll
    for (int s = 0; s < 4; ++s) { LAS unsigned char* ap = L + L_STAGE + (32 * s + 8 * g + tq) * 128 + 32 * w + 8 * tp; wof[s] = trread2(ap, ap + 512); }
  }
  bar();
  const u64 ep = *(volatile LAS u64*)(L + L_FLAG + F_EPOCH);
  const u32 tag32 = (u32)ep, t40lo = ((u32)ep) << 24, t40hi = (u32)(ep >> 8);
  if (wave < 4) {
    const int hh = wave, ch = lane >> 3, c0 = (2 * lane) & 15;
    float mw[4], lw[4], v0[4], v1[4];
#pragma unroll
    for (int w = 0; w < 4; ++w) {
      LAS unsigned char* R = L + L_RING + w * (RING * 4096);
      const f32x2 ml = *(LAS f32x2*)(R + 2048 + hh * 8);
      mw[w] = ml[0]; lw[w] = ml[1];
      v0[w] = *(LAS float*)(R + ch * 256 + c0 * 16 + hh * 4);
      v1[w] = *(LAS float*)(R + ch * 256 + c0 * 16 + 16 + hh * 4);
    }
    const float M = fmaxf(fmaxf(mw[0], mw[1]), fmaxf(mw[2], mw[3]));
    float Ls = 0.f, o0 = 0.f, o1 = 0.f;
#pragma unroll
    for (int w = 0; w < 4; ++w) { const float f = __builtin_amdgcn_exp2f(mw[w] - M); Ls = fmaf(f, lw[w], Ls); o0 = fmaf(f, v0[w], o0); o1 = fmaf(f, v1[w], o1); }
    const float inv = __builtin_amdgcn_rcpf(Ls);
    const u32 slot = ((akv * 4 + hh) * 8 + ab) * 4 + as;
    st_gran(p.ws, WS_OP + (slot * 64 + lane) * 8, pk2(o0 * inv, o1 * inv), tag32);
    if (lane < 2) st_gran(p.ws, WS_ML + (slot * 2 + lane) * 8, __float_as_uint(lane ? Ls : M), tag32);
  } else {
    const int w = wave - 4;
    const u32 slot = (whead * 8 + 2 * w) * 4;
    u64 o[8] = {0, 0, 0, 0, 0, 0, 0, 0}, ml = 0;
    __builtin_amdgcn_s_sleep(H1DLY);
    {
      u32 have = 0;
      NOUNROLL
      for (u32 n = 0; n < SPIN_GLB; ++n) {
        u64 t[9];
#pragma unroll
        for (int s = 0; s < 8; ++s) t[s] = ld_gran(p.ws, ((have >> s) & 1) ? WS_DUMMY : WS_OP + ((slot + s) * 64 + lane) * 8);
        t[8] = ld_gran(p.ws, ((have >> 8) & 1) ? WS_DUMMY : WS_ML + (slot * 2 + (lane & 15)) * 8);
#pragma unroll
        for (int s = 0; s < 9; ++s) {
          const bool fresh = !((have >> s) & 1);
          if (s < 8) o[s] = fresh ? t[s] : o[s]; else ml = fresh ? t[8] : ml;
          if (__all((u32)(t[s] >> 32) == tag32)) have |= 1u << s;
        }
        if (have == 0x1ffu) break;
        __builtin_amdgcn_s_sleep(POLLSLP);
      }
    }
    const u32 mlv = (u32)ml;
#pragma unroll
    for (int bb = 0; bb < 2; ++bb) {
      float ms[4], ls[4];
#pragma unroll
      for (int s = 0; s < 4; ++s) { ms[s] = __uint_as_float(__builtin_amdgcn_readlane(mlv, 8 * bb + 2 * s)); ls[s] = __uint_as_float(__builtin_amdgcn_readlane(mlv, 8 * bb + 2 * s + 1)); }
      const float M = fmaxf(fmaxf(ms[0], ms[1]), fmaxf(ms[2], ms[3]));
      float W = 0.f, o0 = 0.f, o1 = 0.f;
#pragma unroll
      for (int s = 0; s < 4; ++s) { const float wt = ls[s] * __builtin_amdgcn_exp2f(ms[s] - M); W += wt; o0 = fmaf(wt, bflo((u32)o[4 * bb + s]), o0); o1 = fmaf(wt, bfhi((u32)o[4 * bb + s]), o1); }
      const float inv = __builtin_amdgcn_rcpf(W);
      *(LAS u32*)(L + L_O + (2 * w + bb) * 256 + lane * 4) = pk2(o0 * inv, o1 * inv);
    }
    LGKM0();
    if (lane == 0) lds_add(L + L_FLAG + F_SYNC3, 1u);
    NOUNROLL
    for (u32 n = 0; n < SPIN_LDS; ++n) { if (lds_ld(L + L_FLAG + F_SYNC3) >= 4) break; }
    asm volatile("" ::: "memory");
    f32x4 a = {0.f, 0.f, 0.f, 0.f}, a2 = {0.f, 0.f, 0.f, 0.f};
    a = mfma32(*(LAS u32x4*)(L + L_O + (c & 7) * 256 + 16 * g), wof[0], a);
    a2 = mfma32(*(LAS u32x4*)(L + L_O + (c & 7) * 256 + 64 + 16 * g), wof[1], a2);
    a = mfma32(*(LAS u32x4*)(L + L_O + (c & 7) * 256 + 128 + 16 * g), wof[2], a);
    a2 = mfma32(*(LAS u32x4*)(L + L_O + (c & 7) * 256 + 192 + 16 * g), wof[3], a2);
    a += a2;
    if (g < 2) {
#pragma unroll
      for (int i = 0; i < 4; ++i) {
        const u32 u = __float_as_uint(a[i]);
        st_gran(p.ws, WS_OUT + ((((4 * g + i) * 32 + 4 * wcc + w) * 48) + whead) * 128 + c * 8, t40lo | ((u + 0x7fu + ((u >> 8) & 1u)) >> 8), t40hi);
      }
    }
    if (lane == 0) lds_add(L + L_FLAG + F_OUTP, 1u);
  }
  if (wave == 0) {
    const float xres = bflo(p.x[(r >> 2) * DM + 16 * (4 * xg + (r & 3)) + c]);
    const u32 base = WS_OUT + (((r >> 2) * 32 + 4 * xg + (r & 3)) * 48) * 128 + c * 8;
    u64 t[12] = {0, 0, 0, 0, 0, 0, 0, 0, 0, 0, 0, 0};
    lds_wait_ge(L + L_FLAG + F_OUTP, 4);
    __builtin_amdgcn_s_sleep(H2DLY);
    {
      u32 have = 0;
      NOUNROLL
      for (u32 n = 0; n < SPIN_GLB; ++n) {
        u64 tt[12];
#pragma unroll
        for (int i = 0; i < 12; ++i) { const int pr = (4 * i + g < 46) ? 4 * i + g : 45; tt[i] = ld_gran(p.ws, ((have >> i) & 1) ? WS_DUMMY : base + pr * 128); }
#pragma unroll
        for (int i = 0; i < 12; ++i) {
          t[i] = ((have >> i) & 1) ? t[i] : tt[i];
          if (__all((((u32)tt[i] ^ t40lo) >> 24) == 0 && (u32)(tt[i] >> 32) == t40hi)) have |= 1u << i;
        }
        if (have == 0xfffu) break;
        __builtin_amdgcn_s_sleep(POLLSLP);
      }
    }
    float sa = 0.f, sf = 0.f;
#pragma unroll
    for (int i = 0; i < 8; ++i) sa += __uint_as_float((u32)t[i] << 8);
#pragma unroll
    for (int i = 8; i < 12; ++i) sf += (4 * i + g < 46) ? __uint_as_float((u32)t[i] << 8) : 0.f;
    sa = xrow_sum(sa); sf = xrow_sum(sf);
    const float res = rbf(xres + rbf(sa)) + rbf(sf);
    if (g == 0) p.out[(r >> 2) * DM + 16 * (4 * xg + (r & 3)) + c] = (u16)(pk2(res, 0.f) & 0xffffu);
    if (blk == 0 && lane == 0) __hip_atomic_store((GAS u64*)(p.ws + WS_EPOCH), ep, __ATOMIC_RELAXED, __HIP_MEMORY_SCOPE_AGENT);
  }
}

extern "C" void kernel_launch(void* const* d_in, const int* in_sizes, int n_in, void* d_out, int out_size, void* d_ws, size_t ws_size, hipStream_t stream) {
  Params p;
  p.x = (const u16*)d_in[0]; p.k = (const u16*)d_in[1]; p.v = (const u16*)d_in[2]; p.wq = (const u16*)d_in[3]; p.wo = (const u16*)d_in[4];
  p.wg = (const u16*)d_in[6]; p.wu = (const u16*)d_in[7]; p.wd = (const u16*)d_in[8];
  p.out = (u16*)d_out; p.ws = (unsigned char*)d_ws;
  hipLaunchKernelGGL(fused_decode_kernel, dim3(NWG), dim3(NTHR), 0, stream, p);
}
```

```cpp
#include <hip/hip_runtime.h>
#include <stdint.h>

#define GAS __attribute__((address_space(1)))
#define LAS __attribute__((address_space(3)))
typedef unsigned u32;
typedef unsigned long long u64;
typedef unsigned short u16;
typedef u32 u32x4 __attribute__((ext_vector_type(4)));
typedef u32 u32x2 __attribute__((ext_vector_type(2)));
typedef float f32x4 __attribute__((ext_vector_type(4)));
typedef float f32x2 __attribute__((ext_vector_type(2)));
typedef short s16x4 __attribute__((ext_vector_type(4)));
typedef __bf16 bf16x8 __attribute__((ext_vector_type(8)));
typedef __bf16 bf16x2 __attribute__((ext_vector_type(2)));

constexpr int NCODE = 21;
constexpr int PACE10 = 71;
constexpr int PACE_T0 = 250;
constexpr int PACE_FROM = 8;
constexpr int DAHEAD = 3;
constexpr int DPRO = 1;
constexpr int TQ0 = 380, TQR = 48;
constexpr int TGS = 1000;
constexpr int TG15 = 950;
constexpr int TG2 = 1350;
constexpr int POLLSLP = 4, H1DLY = 21, H2DLY = 13;

constexpr int DM = 512, SK = 8192, DH = 128, FF = 1792;
constexpr int NWG = 256, NTHR = 512;
constexpr int RING = 8;
constexpr int NUNIT = 64;
constexpr float SC = 0.08838834764831845f * 1.4426950408889634f;
constexpr float THR_RAW = 8.0f / SC;

constexpr u32 WS_EPOCH = 0;
constexpr u32 WS_DUMMY = 2048;
constexpr u32 WS_START = 4096;
constexpr u32 WS_T0 = 8192;
constexpr u32 WS_Q = 65536;
constexpr u32 WS_GU = WS_Q + 8 * 8 * 4 * 512 * 8;
constexpr u32 WS_H = WS_GU + 28 * 2 * 4 * 8 * 64 * 8;
constexpr u32 WS_OP = WS_H + 8 * 1792 * 8;
constexpr u32 WS_ML = WS_OP + 32 * 8 * 4 * 64 * 8;
constexpr u32 WS_OUT = WS_ML + 32 * 8 * 4 * 2 * 8;
constexpr u32 WS_END = WS_OUT + 8 * 32 * 48 * 16 * 8;
static_assert(WS_END <= (8u << 20), "ws map");

constexpr u32 L_RING = 0;
constexpr u32 L_STAGE = 4 * RING * 4096;
constexpr u32 L_XN = L_STAGE + 16384;
constexpr u32 XN_STR = 1056;
constexpr u32 L_Q = L_XN + 8 * XN_STR;
constexpr u32 L_O = L_Q + 1024;
constexpr u32 L_FLAG = L_O + 2048;
constexpr u32 L_SCR = L_FLAG + 256;
constexpr u32 L_END = L_SCR + 1024;
static_assert(L_END <= 163840, "lds");
constexpr u32 F_SYNC = 0, F_Q = 4, F_SYNC2 = 8, F_WO = 12, F_EPOCH = 16, F_SYNC3 = 24, F_ANCH = 28, F_OUTP = 32;

constexpr u32 PER256 = (u32)(4096.0 * 1024.0 / (PACE10 * 0.1e12) * 1e8 * 256.0);
constexpr u32 SPIN_LDS = 1u << 22, SPIN_GLB = 1u << 17;

struct Params {
  const u16* x; const u16* k; const u16* v; const u16* wq; const u16* wo; const u16* wg; const u16* wu; const u16* wd;
  u16* out; unsigned char* ws;
};

__device__ __forceinline__ u32 rt32() { return (u32)__builtin_amdgcn_s_memrealtime(); }
__device__ __forceinline__ float bflo(u32 w) { return __uint_as_float(w << 16); }
__device__ __forceinline__ float bfhi(u32 w) { return __uint_as_float(w & 0xffff0000u); }
__device__ __forceinline__ u32 pk2(float lo, float hi) { f32x2 t = {lo, hi}; return __builtin_bit_cast(u32, __builtin_convertvector(t, bf16x2)); }
__device__ __forceinline__ float rbf(float f) { return bflo(pk2(f, f)); }
template <int CTRL> __device__ __forceinline__ float dpp(float x) {
  return __builtin_bit_cast(float, __builtin_amdgcn_mov_dpp(__builtin_bit_cast(int, x), CTRL, 0xf, 0xf, true));
}
__device__ __forceinline__ float xrow_max(float x) {
  auto s = __builtin_amdgcn_permlane16_swap(__float_as_uint(x), __float_as_uint(x), false, false);
  x = fmaxf(__uint_as_float(s[0]), __uint_as_float(s[1]));
  auto t = __builtin_amdgcn_permlane32_swap(__float_as_uint(x), __float_as_uint(x), false, false);
  return fmaxf(__uint_as_float(t[0]), __uint_as_float(t[1]));
}
__device__ __forceinline__ float xrow_sum(float x) {
  auto s = __builtin_amdgcn_permlane16_swap(__float_as_uint(x), __float_as_uint(x), false, false);
  x = __uint_as_float(s[0]) + __uint_as_float(s[1]);
  auto t = __builtin_amdgcn_permlane32_swap(__float_as_uint(x), __float_as_uint(x), false, false);
  return __uint_as_float(t[0]) + __uint_as_float(t[1]);
}
__device__ __forceinline__ float wave_sum(float x) {
  x += dpp<0xB1>(x); x += dpp<0x4E>(x); x += dpp<0x141>(x); x += dpp<0x128>(x);
  return xrow_sum(x);
}
__device__ __forceinline__ f32x4 mfma32(u32x4 a, u32x4 b, f32x4 c) {
  return __builtin_amdgcn_mfma_f32_16x16x32_bf16(__builtin_bit_cast(bf16x8, a), __builtin_bit_cast(bf16x8, b), c, 0, 0, 0);
}
__device__ __forceinline__ f32x4 mfma16(u32x2 a, u32x2 b, f32x4 c) {
  return __builtin_amdgcn_mfma_f32_16x16x16bf16_1k(__builtin_bit_cast(s16x4, a), __builtin_bit_cast(s16x4, b), c, 0, 0, 0);
}
__device__ __forceinline__ u32x2 trread(LAS unsigned char* p) {
  return __builtin_bit_cast(u32x2, __builtin_amdgcn_ds_read_tr16_b64_v4i16((LAS s16x4*)p));
}
__device__ __forceinline__ u32x4 trread2(LAS unsigned char* p0, LAS unsigned char* p1) {
  const u32x2 a = trread(p0), b = trread(p1); return u32x4{a.x, a.y, b.x, b.y};
}
__device__ __forceinline__ void dma4(u32 ldst, u32 v0, u32 v1, u32 v2, u32 v3, __amdgpu_buffer_rsrc_t srd, u32 soff) {
  u32 keep;
  asm volatile("s_waitcnt lgkmcnt(0)\n\ts_mov_b32 %0, m0\n\ts_mov_b32 m0, %1\n\ts_nop 0\n\t"
               "buffer_load_dwordx4 %2, %6, %7 offen nt lds\n\t"
               "buffer_load_dwordx4 %3, %6, %7 offen offset:1024 nt lds\n\t"
               "buffer_load_dwordx4 %4, %6, %7 offen offset:2048 nt lds\n\t"
               "buffer_load_dwordx4 %5, %6, %7 offen offset:3072 nt lds\n\t"
               "s_mov_b32 m0, %0"
               : "=&s"(keep) : "s"(ldst), "v"(v0), "v"(v1), "v"(v2), "v"(v3), "s"(srd), "s"(soff) : "memory");
}
__device__ __forceinline__ void dma1(u32 ldst, const void* gp) {
  u32 keep;
  asm volatile("s_mov_b32 %0, m0\n\ts_mov_b32 m0, %1\n\ts_nop 0\n\tglobal_load_lds_dwordx4 %2, off nt\n\ts_mov_b32 m0, %0"
               : "=&s"(keep) : "s"(ldst), "v"(gp) : "memory");
}
__device__ __forceinline__ void dma1p(u32 ldst, const void* gp) {
  u32 keep;
  asm volatile("s_mov_b32 %0, m0\n\ts_mov_b32 m0, %1\n\ts_nop 0\n\tglobal_load_lds_dwordx4 %2, off\n\ts_mov_b32 m0, %0"
               : "=&s"(keep) : "s"(ldst), "v"(gp) : "memory");
}
#define VMCNT(N) asm volatile("s_waitcnt vmcnt(" #N ")" ::: "memory")
#define LGKM0() asm volatile("s_waitcnt lgkmcnt(0)" ::: "memory")
#define NOUNROLL _Pragma("nounroll")
__device__ __forceinline__ void wait_units(int n) {
  switch (n) {
    case 0: VMCNT(0); break;   case 1: VMCNT(4); break;   case 2: VMCNT(8); break;   case 3: VMCNT(12); break;
    case 4: VMCNT(16); break;  case 5: VMCNT(20); break;  case 6: VMCNT(24); break;  default: VMCNT(28); break;
  }
}
__device__ __forceinline__ void bar() { LGKM0(); __builtin_amdgcn_s_barrier(); asm volatile("" ::: "memory"); }
__device__ __forceinline__ u32 lds_ld(LAS unsigned char* p) { return *(volatile LAS u32*)p; }
__device__ __forceinline__ void lds_add(LAS unsigned char* p, u32 v) { __hip_atomic_fetch_add((LAS u32*)p, v, __ATOMIC_RELAXED, __HIP_MEMORY_SCOPE_WORKGROUP); }
__device__ __forceinline__ void lds_wait_ge(LAS unsigned char* p, u32 want) {
  NOUNROLL
  for (u32 n = 0; n < SPIN_LDS; ++n) { if (lds_ld(p) >= want) break; __builtin_amdgcn_s_sleep(1); }
  asm volatile("" ::: "memory");
}
__device__ __forceinline__ void wait_until(u32 due) { while ((int)(rt32() - due) < 0) __builtin_amdgcn_s_sleep(4); }
__device__ __forceinline__ void st_gran(unsigned char* ws, u32 off, u32 lo, u32 hi) {
  __hip_atomic_store((GAS u64*)(ws + off), ((u64)hi << 32) | lo, __ATOMIC_RELAXED, __HIP_MEMORY_SCOPE_AGENT);
}
__device__ __forceinline__ u64 ld_gran(unsigned char* ws, u32 off) {
  return __hip_atomic_load((GAS u64*)(ws + off), __ATOMIC_RELAXED, __HIP_MEMORY_SCOPE_AGENT);
}

__global__ void __launch_bounds__(NTHR, 2) fused_decode_kernel(Params p) {
  __shared__ __attribute__((aligned(16))) unsigned char lds_raw[L_END];
  LAS unsigned char* L = (LAS unsigned char*)lds_raw;
  const u32 lbase = (u32)(size_t)L;
  const int tid = threadIdx.x, lane = tid & 63, wave = __builtin_amdgcn_readfirstlane(tid >> 6);
  const int g = lane >> 4, c = lane & 15;
  const int blk = blockIdx.x, xg = blk & 7, r = blk >> 3;
  const u32 t0 = rt32();
  u64 pc0; asm volatile("s_getpc_b64 %0" : "=s"(pc0));
  const __amdgpu_buffer_rsrc_t wsr = __builtin_amdgcn_make_buffer_rsrc(p.ws, 0, WS_END, 0x00020000);
  const int as = r & 3, ab = r >> 2, akv = xg, apair = ab * 8 + akv;
  const int whead = 4 * xg + (r & 3), wcc = r >> 2;

  if (wave < 4) {
    const size_t pbase = (size_t)apair * SK * DH;
    const __amdgpu_buffer_rsrc_t ksr = __builtin_amdgcn_make_buffer_rsrc((void*)(p.k + pbase), 0, SK * DH * 2, 0x00020000);
    const __amdgpu_buffer_rsrc_t vsr = __builtin_amdgcn_make_buffer_rsrc((void*)(p.v + pbase), 0, SK * DH * 2, 0x00020000);
    const u32 vk0 = g * 256 + 16 * (c ^ g), vk1 = g * 256 + 16 * (c ^ (4 + g)), vk2 = g * 256 + 16 * (c ^ (8 + g)), vk3 = g * 256 + 16 * (c ^ (12 + g));
    const u32 vv0 = g * 256 + 16 * (c ^ (g << 1)), vv1 = g * 256 + 16 * (c ^ ((4 + g) << 1));
    const u32 ring = lbase + L_RING + wave * (RING * 4096);
    int nis = 0;
    int landed = 0;
    u32 tnow = t0, pA = t0 + PACE_T0;
    const u32 sbase = as * 16384u + wave * 4096u;
    auto issue = [&]() {
      if (nis >= PACE_FROM) {
        if (nis < PACE_FROM + 6) { const u32 an = __builtin_amdgcn_readfirstlane(lds_ld(L + L_FLAG + F_ANCH)); if (an) pA = an + PACE_T0; }
        const u32 due = pA + (((u32)nis * PER256) >> 8);
        if ((int)(tnow - due) < 0) {
          NOUNROLL
          for (u32 n = 0; n < SPIN_LDS; ++n) { tnow = rt32(); if ((int)(tnow - due) >= 0) break; __builtin_amdgcn_s_sleep(1); }
        }
      }
      const u32 soff = (u32)(nis >> 1) * 65536u + sbase, ldst = ring + (u32)(nis & (RING - 1)) * 4096u;
      if (nis & 1) dma4(ldst, vv0, vv1, vv0, vv1, vsr, soff); else dma4(ldst, vk0, vk1, vk2, vk3, ksr, soff);
      ++nis;
    };
    issue();
    bar();
    NOUNROLL
    while (nis < RING) {
      if (nis >= DPRO) { wait_units(DPRO - 1); landed = nis - (DPRO - 1); }
      if (lds_ld(L + L_FLAG + F_Q) >= 4) break;
      issue();
    }
    auto refill = [&](int cons) { while (nis - landed < DAHEAD && nis < cons + RING && nis < NUNIT) issue(); };
    lds_wait_ge(L + L_FLAG + F_Q, 4);
    u32x4 qf[4];
#pragma unroll
    for (int s = 0; s < 4; ++s) qf[s] = *(LAS u32x4*)(L + L_Q + (c & 3) * 256 + 64 * s + 16 * g);
    refill(0);
    LAS unsigned char* R = L + L_RING + wave * (RING * 4096);
    const u32 ka = c * 256 + 16 * (g ^ c);
    const u32 va = (4 * g + (c >> 2)) * 256 + 32 * ((4 * g + (c >> 2)) & 7) + 8 * (c & 3);
    float m = -INFINITY, lsum = 0.f;
    f32x4 acc[8];
#pragma unroll
    for (int i = 0; i < 8; ++i) acc[i] = f32x4{0.f, 0.f, 0.f, 0.f};
    NOUNROLL
    for (int j = 0; j < NUNIT / 2; ++j) {
      wait_units(nis - 2 * j - 1);
      landed = landed > 2 * j + 1 ? landed : 2 * j + 1;
      refill(2 * j);
      LAS unsigned char* Kt = R + ((2 * j) & (RING - 1)) * 4096;
      LAS unsigned char* Vt = R + ((2 * j + 1) & (RING - 1)) * 4096;
      f32x4 sa = {0.f, 0.f, 0.f, 0.f}, sb = {0.f, 0.f, 0.f, 0.f};
      {
        const u32x4 k0 = *(LAS u32x4*)(Kt + ka), k1 = *(LAS u32x4*)(Kt + (ka ^ 64)), k2 = *(LAS u32x4*)(Kt + (ka ^ 128)), k3 = *(LAS u32x4*)(Kt + (ka ^ 192));
        sa = mfma32(k0, qf[0], sa); sb = mfma32(k1, qf[1], sb); sa = mfma32(k2, qf[2], sa); sb = mfma32(k3, qf[3], sb);
      }
      const f32x4 sc = sa + sb;
      const float tmax = fmaxf(fmaxf(sc[0], sc[1]), fmaxf(sc[2], sc[3]));
      if (__builtin_amdgcn_ballot_w64(tmax > m + THR_RAW) != 0ull) {
        const float mn = fmaxf(m, xrow_max(tmax));
        const float al = __builtin_amdgcn_exp2f((m - mn) * SC);
        m = mn; lsum *= al;
        const float a0 = dpp<0x00>(al), a1 = dpp<0x55>(al), a2 = dpp<0xAA>(al), a3 = dpp<0xFF>(al);
#pragma unroll
        for (int i = 0; i < 8; ++i) { acc[i][0] *= a0; acc[i][1] *= a1; acc[i][2] *= a2; acc[i][3] *= a3; }
      }
      const float mc = m * SC;
      const float p0 = __builtin_amdgcn_exp2f(fmaf(sc[0], SC, -mc)), p1 = __builtin_amdgcn_exp2f(fmaf(sc[1], SC, -mc));
      const float p2 = __builtin_amdgcn_exp2f(fmaf(sc[2], SC, -mc)), p3 = __builtin_amdgcn_exp2f(fmaf(sc[3], SC, -mc));
      lsum += (p0 + p1) + (p2 + p3);
      const u32x2 pa = {pk2(p0, p1), pk2(p2, p3)};
      wait_units(nis - 2 * j - 2);
      landed = landed > 2 * j + 2 ? landed : 2 * j + 2;
      refill(2 * j + 1);
#pragma unroll
      for (int i = 0; i < 8; ++i) acc[i] = mfma16(pa, trread(Vt + (va ^ (32 * i))), acc[i]);
      refill(2 * j + 2);
    }

    lsum = xrow_sum(lsum);
    if (g == 0) {
#pragma unroll
      for (int i = 0; i < 8; ++i) *(LAS f32x4*)(R + i * 256 + c * 16) = acc[i];
      if (c < 4) *(LAS f32x2*)(R + 2048 + c * 8) = f32x2{m * SC, lsum};
    }
  } else {
    const int hw = wave - 4;
    __builtin_amdgcn_s_setprio(3);
    u32x4 xa, xb; u32x2 ev;
    if (hw == 0 && lane < 16) *(LAS u32x4*)(L + L_FLAG + lane * 16) = u32x4{0, 0, 0, 0};
    const int kq = r & 3;
    if (NCODE && r * 4 + hw < NCODE) {
      dma1p(lbase + L_SCR, (const char*)(pc0 & ~255ull) + (r * 4 + hw) * 1024 + lane * 16);
    }
    {
      const u16* xp = p.x + (2 * hw) * DM + lane * 8;
      const u16* wp = p.wq + (size_t)(kq * 128 + hw * 32 + (lane >> 3)) * 4096 + xg * 512 + (r >> 2) * 64 + (lane & 7) * 8;
      const u32 ldst = lbase + L_STAGE + hw * 4096;
      u32 keep;
      asm volatile("global_load_dwordx4 %0, %4, off\n\tglobal_load_dwordx4 %1, %4, off offset:1024\n\tglobal_load_dwordx2 %2, %5, off\n\t"
                   "s_mov_b32 %3, m0\n\ts_mov_b32 m0, %10\n\ts_nop 0\n\t"
                   "global_load_lds_dwordx4 %6, off nt\n\ts_add_u32 m0, m0, 0x400\n\ts_nop 0\n\t"
                   "global_load_lds_dwordx4 %7, off nt\n\ts_add_u32 m0, m0, 0x400\n\ts_nop 0\n\t"
                   "global_load_lds_dwordx4 %8, off nt\n\ts_add_u32 m0, m0, 0x400\n\ts_nop 0\n\t"
                   "global_load_lds_dwordx4 %9, off nt\n\ts_mov_b32 m0, %3\n\t"
                   "s_waitcnt lgkmcnt(0)\n\ts_barrier\n\ts_waitcnt vmcnt(4)"
                   : "=&v"(xa), "=&v"(xb), "=&v"(ev), "=&s"(keep)
                   : "v"(xp), "v"(p.ws + WS_EPOCH), "v"(wp), "v"(wp + (size_t)8 * 4096), "v"(wp + (size_t)16 * 4096), "v"(wp + (size_t)24 * 4096), "s"(ldst)
                   : "memory", "scc");
    }
    const u64 ep = (((u64)ev.y << 32) | ev.x) + 1ull;
    const u32 tag32 = (u32)ep, t48lo = ((u32)ep) << 16, t48hi = (u32)(ep >> 16), t40lo = ((u32)ep) << 24, t40hi = (u32)(ep >> 8);
#pragma unroll
    for (int q = 0; q < 2; ++q) {
      const u32x4 xv = q ? xb : xa;
      float v[8];
#pragma unroll
      for (int i = 0; i < 4; ++i) { v[2 * i] = bflo(xv[i]); v[2 * i + 1] = bfhi(xv[i]); }
      const float mean = wave_sum(((v[0] + v[1]) + (v[2] + v[3])) + ((v[4] + v[5]) + (v[6] + v[7]))) * (1.f / DM);
      float q2 = 0.f;
#pragma unroll
      for (int i = 0; i < 8; ++i) { const float d = v[i] - mean; q2 = fmaf(d, d, q2); }
      const float var = rbf(wave_sum(q2) * (1.f / DM));
      const float rden = __builtin_amdgcn_rcpf(rbf(__builtin_amdgcn_sqrtf(rbf(var + 1e-5f))));
      const float mb = rbf(mean);
      u32x4 o;
#pragma unroll
      for (int i = 0; i < 4; ++i) { const u32 d = pk2(v[2 * i] - mb, v[2 * i + 1] - mb); o[i] = pk2(bflo(d) * rden, bfhi(d) * rden); }
      *(LAS u32x4*)(L + L_XN + (2 * hw + q) * XN_STR + lane * 16) = o;
    }
    const int tq = c >> 2, tp = c & 3;
    auto tile_mma = [&](const u32x4 (&act)[4], int cg) {
      f32x4 a0 = {0.f, 0.f, 0.f, 0.f}, a1 = {0.f, 0.f, 0.f, 0.f};
      LAS unsigned char* ap = L + L_STAGE + (8 * g + tq) * 128 + 32 * cg + 8 * tp;
      a0 = mfma32(act[0], trread2(ap, ap + 512), a0);
      a1 = mfma32(act[1], trread2(ap + 4096, ap + 4096 + 512), a1);
      a0 = mfma32(act[2], trread2(ap + 8192, ap + 8192 + 512), a0);
      a1 = mfma32(act[3], trread2(ap + 12288, ap + 12288 + 512), a1);
      return a0 + a1;
    };
    auto xn_frags = [&](u32x4 (&act)[4], int k0) {
#pragma unroll
      for (int s = 0; s < 4; ++s) act[s] = *(LAS u32x4*)(L + L_XN + (c & 7) * XN_STR + 2 * k0 + 64 * s + 16 * g);
    };
    auto f24 = [&](float f) { const u32 u = __float_as_uint(f); return t40lo | ((u + 0x7fu + ((u >> 8) & 1u)) >> 8); };
    auto ok40 = [&](const u32x4& t) { return ((t[0] ^ t40lo) >> 24) == 0 && t[1] == t40hi && ((t[2] ^ t40lo) >> 24) == 0 && t[3] == t40hi; };
    auto ok48 = [&](const u32x4& t) { return ((t[0] ^ t48lo) >> 16) == 0 && t[1] == t48hi && ((t[2] ^ t48lo) >> 16) == 0 && t[3] == t48hi; };
    u32x4 act[4];
    asm volatile("s_waitcnt vmcnt(0) lgkmcnt(0)" ::: "memory");
    if (lane == 0) lds_add(L + L_FLAG + F_SYNC, 1u);
    lds_wait_ge(L + L_FLAG + F_SYNC, 4);
    xn_frags(act, kq * 128);
    {
      const f32x4 d = tile_mma(act, hw);
      if (g < 2) {
#pragma unroll
        for (int i = 0; i < 4; ++i) st_gran(p.ws, WS_Q + ((((xg * 8 + 4 * g + i) * 4 + kq) * 512) + (r >> 2) * 64 + 16 * hw + c) * 8, f24(d[i]), t40hi);
      }
    }
    if (hw == 1 && lane == 0) st_gran(p.ws, WS_T0 + blk * 8, t40lo | (t0 & 0xffffffu), t40hi);
    LGKM0();
    if (lane == 0) lds_add(L + L_FLAG + F_SYNC, 1u);
    if (hw < 2) {
      const u32 qb = WS_Q + ((akv * 8 + ab) * 4) * 512 * 8;
      wait_until(t0 + TQ0 - (u32)(r * TQR) / 16);
      float q0[2], q1[2];
      NOUNROLL
      for (u32 n = 0; n < SPIN_GLB; ++n) {
        bool ok = true;
#pragma unroll
        for (int h = 0; h < 2; ++h) {
          q0[h] = 0.f; q1[h] = 0.f;
#pragma unroll
          for (int i = 0; i < 4; ++i) {
            const u32x4 t = __builtin_amdgcn_raw_buffer_load_b128(wsr, qb + (i * 512 + (2 * hw + h) * 128 + 2 * lane) * 8, 0, 16);
            ok &= ok40(t); q0[h] += __uint_as_float(t[0] << 8); q1[h] += __uint_as_float(t[2] << 8);
          }
        }
        if (__all(ok)) break;
        __builtin_amdgcn_s_sleep(POLLSLP);
        asm volatile("" ::: "memory");
      }
      *(LAS u32*)(L + L_Q + (2 * hw) * 256 + lane * 4) = pk2(q0[0], q1[0]);
      *(LAS u32*)(L + L_Q + (2 * hw + 1) * 256 + lane * 4) = pk2(q0[1], q1[1]);
      LGKM0();
      if (lane == 0) lds_add(L + L_FLAG + F_Q, 2u);
      if (hw == 1) {
        const u32 ld_ = lbase + L_SCR;
        u32 keep;
        const u16* tp = (lane < 32 ? p.k : p.v) + (size_t)apair * SK * DH + (size_t)(lane & 31) * 32768 + as * 8192 + 4096;
        if ((lane & 31) >= 4)
          asm volatile("s_mov_b32 %0, m0\n\ts_mov_b32 m0, %1\n\ts_nop 0\n\tglobal_load_lds_dword %2, off nt\n\ts_mov_b32 m0, %0" : "=&s"(keep) : "s"(ld_), "v"(tp) : "memory");
        const unsigned char* tw = lane < 8 ? p.ws + WS_OUT + ((lane * 32 + 4 * wcc) * 48 + 47) * 128
                                : lane == 8 ? p.ws + WS_OUT + (((r >> 2) * 32 + 4 * xg + (r & 3)) * 48 + 47) * 128
                                : (const unsigned char*)(p.out + (r >> 2) * DM + 16 * (4 * xg + (r & 3)));
        if (lane < 10)
          asm volatile("s_mov_b32 %0, m0\n\ts_mov_b32 m0, %1\n\ts_nop 0\n\tglobal_load_lds_dword %2, off\n\ts_mov_b32 m0, %0" : "=&s"(keep) : "s"(ld_), "v"(tw) : "memory");
      }
      if (hw == 0) {
        u64 ts = 0;
        bool ok = false;
        NOUNROLL
        for (u32 n = 0; n < 64 && !ok; ++n) {
          ts = ld_gran(p.ws, WS_T0 + (xg + 8 * (lane & 31)) * 8);
          ok = __all((((u32)ts ^ t40lo) >> 24) == 0 && (u32)(ts >> 32) == t40hi);
          if (!ok) __builtin_amdgcn_s_sleep(8);
        }
        int d = ((int)(((u32)ts - t0) << 8)) >> 8;
        d = min(d, __builtin_amdgcn_mov_dpp(d, 0xB1, 0xf, 0xf, true)); d = min(d, __builtin_amdgcn_mov_dpp(d, 0x4E, 0xf, 0xf, true));
        d = min(d, __builtin_amdgcn_mov_dpp(d, 0x141, 0xf, 0xf, true)); d = min(d, __builtin_amdgcn_mov_dpp(d, 0x128, 0xf, 0xf, true));
        int dm = min(min(__builtin_amdgcn_readlane(d, 0), __builtin_amdgcn_readlane(d, 16)), 0);
        dm = dm < -400 ? -400 : dm;
        if (ok && lane == 0) *(volatile LAS u32*)(L + L_FLAG + F_ANCH) = (t0 + dm) | 1u;
      }
      if (hw == 1) {
        wait_until(t0 + TGS);
        NOUNROLL
        for (u32 n = 0; n < SPIN_GLB; ++n) {
          const u32x4 a = __builtin_amdgcn_raw_buffer_load_b128(wsr, WS_START + lane * 16, 0, 16);
          const u32x4 b = __builtin_amdgcn_raw_buffer_load_b128(wsr, WS_START + 1024 + lane * 16, 0, 16);
          const u32 e0 = (u32)ep, e1 = (u32)(ep >> 32);
          const bool ok = a[0] == e0 && a[1] == e1 && a[2] == e0 && a[3] == e1 && b[0] == e0 && b[1] == e1 && b[2] == e0 && b[3] == e1;
          if (__all(ok)) break;
          __builtin_amdgcn_s_sleep(32);
          asm volatile("" ::: "memory");
        }
      }
    } else {
      const int h2 = hw - 2;
      u32 nsync2 = 0;
      auto arrive2 = [&]() {
        LGKM0();
        if (lane == 0) lds_add(L + L_FLAG + F_SYNC2, 1u);
        nsync2 += 2;
        lds_wait_ge(L + L_FLAG + F_SYNC2, nsync2);
      };
      auto stage2 = [&](const u16* w, int ld) {
        const u16* wp = w + (size_t)(h2 * 64 + (lane >> 3)) * ld + (lane & 7) * 8;
#pragma unroll
        for (int i = 0; i < 8; ++i) dma1(lbase + L_STAGE + h2 * 8192 + i * 1024, wp + (size_t)i * 8 * ld);
      };
      if (h2 == 1) {
        const u32x4 z = {0u, ~tag32, 0u, ~tag32};
#pragma unroll
        for (int i = 0; i < 2; ++i)
          __builtin_amdgcn_raw_buffer_store_b128(z, wsr, WS_OP + ((((akv * 4 + 2 * i + (lane >> 5)) * 8 + ab) * 4 + as) * 64) * 8 + (lane & 31) * 16, 0, 16);
        if (lane < 4) __builtin_amdgcn_raw_buffer_store_b128(z, wsr, WS_ML + (((akv * 4 + lane) * 8 + ab) * 4 + as) * 16, 0, 16);
      }
      lds_wait_ge(L + L_FLAG + F_SYNC, 8);
      const bool f1 = r < 14, f15 = r >= 14 && r < 18, f2 = r >= 18;
      const int t1 = xg * 14 + r, sl1 = t1 >> 2, fq = t1 & 3, ks = r - 18;
      const u16* wo_tile = p.wo + (size_t)whead * 128 * DM + wcc * 64;
      if (f1) stage2(p.wg + (size_t)fq * 128 * FF + sl1 * 64, FF);
      else if (f2) stage2(p.wd + (size_t)ks * 128 * DM + xg * 64, DM);
      else stage2(wo_tile, DM);
      VMCNT(0);
      if (h2 == 1 && lane == 0) {
        __hip_atomic_store((GAS u64*)(p.ws + WS_START + blk * 8), ep, __ATOMIC_RELAXED, __HIP_MEMORY_SCOPE_AGENT);
        *(volatile LAS u64*)(L + L_FLAG + F_EPOCH) = ep;
      }
      arrive2();
      if (f1) {
        xn_frags(act, fq * 128);
        f32x4 d[2];
        d[0] = tile_mma(act, 2 * h2); d[1] = tile_mma(act, 2 * h2 + 1);
        asm volatile("" : "+v"(d[0]), "+v"(d[1]));
        arrive2();
        stage2(p.wu + (size_t)fq * 128 * FF + sl1 * 64, FF);
#pragma unroll
        for (int w = 0; w < 2; ++w) if (g < 2) {
#pragma unroll
          for (int i = 0; i < 4; ++i) st_gran(p.ws, WS_GU + ((((sl1 * 2 + 0) * 4 + fq) * 8 + 4 * g + i) * 64 + 16 * (2 * h2 + w) + c) * 8, f24(d[w][i]), t40hi);
        }
        VMCNT(8);
        arrive2();
        d[0] = tile_mma(act, 2 * h2); d[1] = tile_mma(act, 2 * h2 + 1);
        asm volatile("" : "+v"(d[0]), "+v"(d[1]));
        arrive2();
        stage2(wo_tile, DM);
#pragma unroll
        for (int w = 0; w < 2; ++w) if (g < 2) {
#pragma unroll
          for (int i = 0; i < 4; ++i) st_gran(p.ws, WS_GU + ((((sl1 * 2 + 1) * 4 + fq) * 8 + 4 * g + i) * 64 + 16 * (2 * h2 + w) + c) * 8, f24(d[w][i]), t40hi);
        }
        VMCNT(8);
        arrive2();
      } else if (f15) {
        const int sl = xg * 4 + (r - 14);
        if (sl < 28) {
          wait_until(t0 + TG15);
          {
            const u32 so = WS_GU + ((((sl * 2 + 1) * 4 + (lane & 3)) * 8) * 64 + 63) * 8;
            NOUNROLL
            for (u32 n = 0; n < SPIN_GLB; ++n) {
              const u64 t = ld_gran(p.ws, so);
              if (__all((((u32)t ^ t40lo) >> 24) == 0 && (u32)(t >> 32) == t40hi)) break;
              __builtin_amdgcn_s_sleep(16);
            }
          }
          float gs[2][2], us[2][2];
          const int cp = lane & 31;
          NOUNROLL
          for (u32 n = 0; n < SPIN_GLB; ++n) {
            bool ok = true;
#pragma unroll
            for (int bb = 0; bb < 2; ++bb) {
              const int b = 4 * h2 + 2 * bb + (lane >> 5);
              gs[bb][0] = gs[bb][1] = us[bb][0] = us[bb][1] = 0.f;
#pragma unroll
              for (int i = 0; i < 4; ++i) {
                const u32x4 tg = __builtin_amdgcn_raw_buffer_load_b128(wsr, WS_GU + ((((sl * 2 + 0) * 4 + i) * 8 + b) * 64 + 2 * cp) * 8, 0, 16);
                const u32x4 tu = __builtin_amdgcn_raw_buffer_load_b128(wsr, WS_GU + ((((sl * 2 + 1) * 4 + i) * 8 + b) * 64 + 2 * cp) * 8, 0, 16);
                ok &= ok40(tg) && ok40(tu);
                gs[bb][0] += __uint_as_float(tg[0] << 8); gs[bb][1] += __uint_as_float(tg[2] << 8);
                us[bb][0] += __uint_as_float(tu[0] << 8); us[bb][1] += __uint_as_float(tu[2] << 8);
              }
            }
            if (__all(ok)) break;
            __builtin_amdgcn_s_sleep(16);
            asm volatile("" ::: "memory");
          }
#pragma unroll
          for (int bb = 0; bb < 2; ++bb) {
            const int b = 4 * h2 + 2 * bb + (lane >> 5);
            const u32 hp = pk2(rbf(gs[bb][0]) * rbf(us[bb][0]), rbf(gs[bb][1]) * rbf(us[bb][1]));
            st_gran(p.ws, WS_H + (b * FF + sl * 64 + 2 * cp) * 8, t48lo | (hp & 0xffffu), t48hi);
            st_gran(p.ws, WS_H + (b * FF + sl * 64 + 2 * cp + 1) * 8, t48lo | (hp >> 16), t48hi);
          }
        }
      } else {
        wait_until(t0 + TG2);
        {
          const u32 so = WS_H + (7 * FF + ks * 128 + 64 * (lane & 1) + 63) * 8;
          NOUNROLL
          for (u32 n = 0; n < SPIN_GLB; ++n) {
            const u64 t = ld_gran(p.ws, so);
            if (__all((((u32)t ^ t48lo) >> 16) == 0 && (u32)(t >> 32) == t48hi)) break;
            __builtin_amdgcn_s_sleep(32);
          }
        }
        const int b = lane >> 3, k0 = 64 * h2 + 8 * (lane & 7);
        u32x4 hv = {0, 0, 0, 0};
        NOUNROLL
        for (u32 n = 0; n < SPIN_GLB; ++n) {
          bool ok = true;
#pragma unroll
          for (int i = 0; i < 4; ++i) {
            const u32x4 t = __builtin_amdgcn_raw_buffer_load_b128(wsr, WS_H + (b * FF + ks * 128 + k0 + 2 * i) * 8, 0, 16);
            ok &= ok48(t);
            hv[i] = (t[0] & 0xffffu) | (t[2] << 16);
          }
          if (__all(ok)) break;
          __builtin_amdgcn_s_sleep(16);
          asm volatile("" ::: "memory");
        }
        *(LAS u32x4*)(L + L_O + b * 256 + k0 * 2) = hv;
        arrive2();
#pragma unroll
        for (int s = 0; s < 4; ++s) act[s] = *(LAS u32x4*)(L + L_O + (c & 7) * 256 + 64 * s + 16 * g);
        f32x4 d[2];
        d[0] = tile_mma(act, 2 * h2); d[1] = tile_mma(act, 2 * h2 + 1);
        asm volatile("" : "+v"(d[0]), "+v"(d[1]));
        arrive2();
        stage2(wo_tile, DM);
#pragma unroll
        for (int w = 0; w < 2; ++w) if (g < 2) {
#pragma unroll
          for (int i = 0; i < 4; ++i) st_gran(p.ws, WS_OUT + ((((4 * g + i) * 32 + 4 * xg + 2 * h2 + w) * 48) + 32 + ks) * 128 + c * 8, f24(d[w][i]), t40hi);
        }
        VMCNT(8);
        arrive2();
      }
      if (h2 == 0 && lane == 0) *(volatile LAS u32*)(L + L_FLAG + F_WO) = 1;
    }
    NOUNROLL
    for (u32 n = 0; n < SPIN_LDS; ++n) { if (lds_ld(L + L_FLAG + F_WO) >= 1) break; __builtin_amdgcn_s_sleep(16); }
    asm volatile("" ::: "memory");
  }

  u32x4 wof[4];
  if (wave >= 4) {
    const int tq = c >> 2, tp = c & 3, w = wave - 4;
#pragma unroll
    for (int s = 0; s < 4; ++s) { LAS unsigned char* ap = L + L_STAGE + (32 * s + 8 * g + tq) * 128 + 32 * w + 8 * tp; wof[s] = trread2(ap, ap + 512); }
  }
  bar();
  const u64 ep = *(volatile LAS u64*)(L + L_FLAG + F_EPOCH);
  const u32 tag32 = (u32)ep, t40lo = ((u32)ep) << 24, t40hi = (u32)(ep >> 8);
  if (wave < 4) {
    const int hh = wave, ch = lane >> 3, c0 = (2 * lane) & 15;
    float mw[4], lw[4], v0[4], v1[4];
#pragma unroll
    for (int w = 0; w < 4; ++w) {
      LAS unsigned char* R = L + L_RING + w * (RING * 4096);
      const f32x2 ml = *(LAS f32x2*)(R + 2048 + hh * 8);
      mw[w] = ml[0]; lw[w] = ml[1];
      v0[w] = *(LAS float*)(R + ch * 256 + c0 * 16 + hh * 4);
      v1[w] = *(LAS float*)(R + ch * 256 + c0 * 16 + 16 + hh * 4);
    }
    const float M = fmaxf(fmaxf(mw[0], mw[1]), fmaxf(mw[2], mw[3]));
    float Ls = 0.f, o0 = 0.f, o1 = 0.f;
#pragma unroll
    for (int w = 0; w < 4; ++w) { const float f = __builtin_amdgcn_exp2f(mw[w] - M); Ls = fmaf(f, lw[w], Ls); o0 = fmaf(f, v0[w], o0); o1 = fmaf(f, v1[w], o1); }
    const float inv = __builtin_amdgcn_rcpf(Ls);
    const u32 slot = ((akv * 4 + hh) * 8 + ab) * 4 + as;
    st_gran(p.ws, WS_OP + (slot * 64 + lane) * 8, pk2(o0 * inv, o1 * inv), tag32);
    if (lane < 2) st_gran(p.ws, WS_ML + (slot * 2 + lane) * 8, __float_as_uint(lane ? Ls : M), tag32);
  } else {
    const int w = wave - 4;
    const u32 slot = (whead * 8 + 2 * w) * 4;
    u64 o[8] = {0, 0, 0, 0, 0, 0, 0, 0}, ml = 0;
    __builtin_amdgcn_s_sleep(H1DLY);
    {
      u32 have = 0;
      NOUNROLL
      for (u32 n = 0; n < SPIN_GLB; ++n) {
        u64 t[9];
#pragma unroll
        for (int s = 0; s < 8; ++s) t[s] = ld_gran(p.ws, ((have >> s) & 1) ? WS_DUMMY : WS_OP + ((slot + s) * 64 + lane) * 8);
        t[8] = ld_gran(p.ws, ((have >> 8) & 1) ? WS_DUMMY : WS_ML + (slot * 2 + (lane & 15)) * 8);
#pragma unroll
        for (int s = 0; s < 9; ++s) {
          const bool fresh = !((have >> s) & 1);
          if (s < 8) o[s] = fresh ? t[s] : o[s]; else ml = fresh ? t[8] : ml;
          if (__all((u32)(t[s] >> 32) == tag32)) have |= 1u << s;
        }
        if (have == 0x1ffu) break;
        __builtin_amdgcn_s_sleep(POLLSLP);
      }
    }
    const u32 mlv = (u32)ml;
#pragma unroll
    for (int bb = 0; bb < 2; ++bb) {
      float ms[4], ls[4];
#pragma unroll
      for (int s = 0; s < 4; ++s) { ms[s] = __uint_as_float(__builtin_amdgcn_readlane(mlv, 8 * bb + 2 * s)); ls[s] = __uint_as_float(__builtin_amdgcn_readlane(mlv, 8 * bb + 2 * s + 1)); }
      const float M = fmaxf(fmaxf(ms[0], ms[1]), fmaxf(ms[2], ms[3]));
      float W = 0.f, o0 = 0.f, o1 = 0.f;
#pragma unroll
      for (int s = 0; s < 4; ++s) { const float wt = ls[s] * __builtin_amdgcn_exp2f(ms[s] - M); W += wt; o0 = fmaf(wt, bflo((u32)o[4 * bb + s]), o0); o1 = fmaf(wt, bfhi((u32)o[4 * bb + s]), o1); }
      const float inv = __builtin_amdgcn_rcpf(W);
      *(LAS u32*)(L + L_O + (2 * w + bb) * 256 + lane * 4) = pk2(o0 * inv, o1 * inv);
    }
    LGKM0();
    if (lane == 0) lds_add(L + L_FLAG + F_SYNC3, 1u);
    NOUNROLL
    for (u32 n = 0; n < SPIN_LDS; ++n) { if (lds_ld(L + L_FLAG + F_SYNC3) >= 4) break; }
    asm volatile("" ::: "memory");
    f32x4 a = {0.f, 0.f, 0.f, 0.f}, a2 = {0.f, 0.f, 0.f, 0.f};
    a = mfma32(*(LAS u32x4*)(L + L_O + (c & 7) * 256 + 16 * g), wof[0], a);
    a2 = mfma32(*(LAS u32x4*)(L + L_O + (c & 7) * 256 + 64 + 16 * g), wof[1], a2);
    a = mfma32(*(LAS u32x4*)(L + L_O + (c & 7) * 256 + 128 + 16 * g), wof[2], a);
    a2 = mfma32(*(LAS u32x4*)(L + L_O + (c & 7) * 256 + 192 + 16 * g), wof[3], a2);
    a += a2;
    if (g < 2) {
#pragma unroll
      for (int i = 0; i < 4; ++i) {
        const u32 u = __float_as_uint(a[i]);
        st_gran(p.ws, WS_OUT + ((((4 * g + i) * 32 + 4 * wcc + w) * 48) + whead) * 128 + c * 8, t40lo | ((u + 0x7fu + ((u >> 8) & 1u)) >> 8), t40hi);
      }
    }
    if (lane == 0) lds_add(L + L_FLAG + F_OUTP, 1u);
  }
  if (wave == 0) {
    const float xres = bflo(p.x[(r >> 2) * DM + 16 * (4 * xg + (r & 3)) + c]);
    const u32 base = WS_OUT + (((r >> 2) * 32 + 4 * xg + (r & 3)) * 48) * 128 + c * 8;
    u64 t[12] = {0, 0, 0, 0, 0, 0, 0, 0, 0, 0, 0, 0};
    lds_wait_ge(L + L_FLAG + F_OUTP, 4);
    __builtin_amdgcn_s_sleep(H2DLY);
    {
      u32 have = 0;
      NOUNROLL
      for (u32 n = 0; n < SPIN_GLB; ++n) {
        u64 tt[12];
#pragma unroll
        for (int i = 0; i < 12; ++i) { const int pr = (4 * i + g < 46) ? 4 * i + g : 45; tt[i] = ld_gran(p.ws, ((have >> i) & 1) ? WS_DUMMY : base + pr * 128); }
#pragma unroll
        for (int i = 0; i < 12; ++i) {
          t[i] = ((have >> i) & 1) ? t[i] : tt[i];
          if (__all((((u32)tt[i] ^ t40lo) >> 24) == 0 && (u32)(tt[i] >> 32) == t40hi)) have |= 1u << i;
        }
        if (have == 0xfffu) break;
        __builtin_amdgcn_s_sleep(POLLSLP);
      }
    }
    float sa = 0.f, sf = 0.f;
#pragma unroll
    for (int i = 0; i < 8; ++i) sa += __uint_as_float((u32)t[i] << 8);
#pragma unroll
    for (int i = 8; i < 12; ++i) sf += (4 * i + g < 46) ? __uint_as_float((u32)t[i] << 8) : 0.f;
    sa = xrow_sum(sa); sf = xrow_sum(sf);
    const float res = rbf(xres + rbf(sa)) + rbf(sf);
    if (g == 0) p.out[(r >> 2) * DM + 16 * (4 * xg + (r & 3)) + c] = (u16)(pk2(res, 0.f) & 0xffffu);
    if (blk == 0 && lane == 0) __hip_atomic_store((GAS u64*)(p.ws + WS_EPOCH), ep, __ATOMIC_RELAXED, __HIP_MEMORY_SCOPE_AGENT);
  }
}

extern "C" void kernel_launch(void* const* d_in, const int* in_sizes, int n_in, void* d_out, int out_size, void* d_ws, size_t ws_size, hipStream_t stream) {
  Params p;
  p.x = (const u16*)d_in[0]; p.k = (const u16*)d_in[1]; p.v = (const u16*)d_in[2]; p.wq = (const u16*)d_in[3]; p.wo = (const u16*)d_in[4];
  p.wg = (const u16*)d_in[6]; p.wu = (const u16*)d_in[7]; p.wd = (const u16*)d_in[8];
  p.out = (u16*)d_out; p.ws = (unsigned char*)d_ws;
  hipLaunchKernelGGL(fused_decode_kernel, dim3(NWG), dim3(NTHR), 0, stream, p);
}
```
